```python
import torch

def _softmax(x, axis=-1):
    return torch.softmax(x, dim=axis)
def _gelu(x):
    return torch.nn.functional.gelu(x, approximate="tanh")
def _silu(x):
    return torch.nn.functional.silu(x)
def _sigmoid(x):
    return torch.sigmoid(x)
def _layernorm(x, axis=-1, eps=1e-5):
    m = x.mean(dim=axis, keepdim=True)
    v = x.var(dim=axis, keepdim=True, unbiased=False)
    return (x - m) / torch.sqrt(v + eps)
def _scatter_add(tgt, idx, src, axis=0):
    return tgt.clone().index_add_(axis, idx, src)
def _segment_sum(src, seg, n):
    out = torch.zeros((n,) + src.shape[1:], dtype=src.dtype)
    return out.index_add_(0, seg, src)
def _pad(x, pw):
    flat = tuple(v for pair in reversed(pw) for v in pair)
    return torch.nn.functional.pad(x, flat)

kernel_name = 'parallel_sum__ssm_conv_h3__glu_gated_bilinear__prompt'

def setup_inputs():

    _w = (lambda a: (lambda t: t.to(torch.bfloat16) if t.is_floating_point() else t)(torch.as_tensor(a)))
    def _A_setup():
        B, S, D = 1, 2048, 512
        ks = 4
        return {
            "q": _w((lambda *s: torch.randn(*s, dtype=torch.bfloat16) * 0.05)(B, S, D)),
            "k": _w((lambda *s: torch.randn(*s, dtype=torch.bfloat16) * 0.2)(B, S, D)),
            "v": _w((lambda *s: torch.randn(*s, dtype=torch.bfloat16) * 0.2)(B, S, D)),
            "shift_k": _w((lambda *s: torch.randn(*s, dtype=torch.bfloat16) * 0.2)(D, ks) * 0.5),
            "ssm_k": _w((lambda *s: torch.randn(*s, dtype=torch.bfloat16) * 0.4)(D, S) * (1.0 / S ** 0.5)),
            "ssm_D": _w((lambda *s: torch.randn(*s, dtype=torch.bfloat16) * 0.2)(D)),
        }
    _A_in = _A_setup()

    def _B_setup():
        B, S, D, F = 1, 2048, 512, 1376
        return {
            "x": _w((lambda *s: torch.randn(*s, dtype=torch.bfloat16) * 0.05)(B, S, D)),
            "w_gate": _w((lambda *s: torch.randn(*s, dtype=torch.bfloat16) * 0.2)(D, F) * (D ** -0.5)),
            "w_up": _w((lambda *s: torch.randn(*s, dtype=torch.bfloat16) * 0.2)(D, F) * (D ** -0.5)),
            "w_down": _w((lambda *s: torch.randn(*s, dtype=torch.bfloat16) * 0.2)(F, D) * (F ** -0.5)),
        }
    _B_in = _B_setup()

    return {
        "A_q": _A_in["q"],
        "A_k": _A_in["k"],
        "A_v": _A_in["v"],
        "A_shift_k": _A_in["shift_k"],
        "A_ssm_k": _A_in["ssm_k"],
        "A_ssm_D": _A_in["ssm_D"],
        "B_x": _B_in["x"],
        "B_w_gate": _B_in["w_gate"],
        "B_w_up": _B_in["w_up"],
        "B_w_down": _B_in["w_down"],
    }

def reference(A_q, A_k, A_v, A_shift_k, A_ssm_k, A_ssm_D, B_x, B_w_gate, B_w_up, B_w_down):
    def _A_ref(q, k, v, shift_k, ssm_k, ssm_D):
        B, S, D = q.shape
        ks = shift_k.shape[-1]
        k_pad = _pad(k, ((0, 0), (ks - 1, 0), (0, 0)))
        k_shift = torch.zeros_like(k)
        for t in range(ks):
            k_shift = k_shift + k_pad[:, t:t + S, :] * shift_k[:, ks - 1 - t][None, None, :]
        kv = k_shift * v
        L = 2 * S
        kv_pad = _pad(kv, ((0, 0), (0, S), (0, 0)))
        kf_pad = _pad(ssm_k, ((0, 0), (0, S)))
        kv_f = (lambda x, axes=(-2,-1): torch.fft.rfft2(x.float(), dim=axes))(kv_pad[:, None, :, :], axes=(1, 2))
        kf_f = (lambda x, axes=(-2,-1): torch.fft.rfft2(x.float(), dim=axes))(kf_pad[:, None, :], axes=(1, 2))
        kf_f = (lambda x, *d: x.permute(*d))(kf_f, 1, 2, 0)
        y_f = kv_f * kf_f[None, :, :, :]
        y = (lambda x, s, axes=(-2,-1): torch.fft.irfft2(x, s=s, dim=axes).to(torch.bfloat16))(y_f, (1, L), axes=(1, 2))[:, 0, :S, :]
        y = y + kv * ssm_D[None, None, :]
        return q * y

    def _B_ref(x, w_gate, w_up, w_down):
        g = torch.einsum("bsd,df->bsf", x, w_gate)
        u = torch.einsum("bsd,df->bsf", x, w_up)
        h = g * u
        return torch.einsum("bsf,fd->bsd", h, w_down)

    x = A_q
    xn = _layernorm(x, axis=-1)
    return x + _A_ref(xn, A_k, A_v, A_shift_k, A_ssm_k, A_ssm_D) + _B_ref(xn, B_w_gate, B_w_up, B_w_down)
```

```cpp
#include <hip/hip_runtime.h>
#include <hip/hip_bf16.h>
#include <stdint.h>

typedef __attribute__((ext_vector_type(8))) short bf16x8;
typedef __attribute__((ext_vector_type(4))) short s16x4;
typedef __attribute__((ext_vector_type(2))) __bf16 bf16x2;
typedef __attribute__((ext_vector_type(4))) float f32x4;
typedef __attribute__((ext_vector_type(2))) float f32x2;
typedef __attribute__((ext_vector_type(4))) unsigned u32x4;
typedef __attribute__((ext_vector_type(2))) unsigned u32x2;
typedef __attribute__((address_space(3))) s16x4* lds_s16x4_ptr;
typedef unsigned short u16;

#define DI __device__ __forceinline__

constexpr int S_ = 2048, D_ = 512, F_ = 1376, FP_ = 1408;

constexpr size_t WS_H = 0;
constexpr size_t WS_T = 6u * 1024 * 1024;
constexpr size_t WS_ST = 8u * 1024 * 1024 + 512 * 1024;

template <int N> DI void wait_vm() { asm volatile("s_waitcnt vmcnt(%0)" ::"i"(N) : "memory"); }
DI void wait_lgkm0() { asm volatile("s_waitcnt lgkmcnt(0)" ::: "memory"); }
DI void raw_barrier() {
  asm volatile("s_waitcnt lgkmcnt(0)" ::: "memory");
  __builtin_amdgcn_s_barrier();
  asm volatile("" ::: "memory");
}

DI unsigned pk_bf16(float lo, float hi) {
  f32x2 f = {lo, hi};
  return __builtin_bit_cast(unsigned, __builtin_convertvector(f, bf16x2));
}
DI float bf_lo(unsigned v) { return __uint_as_float(v << 16); }
DI float bf_hi(unsigned v) { return __uint_as_float(v & 0xffff0000u); }
DI float round_bf(float x) { return bf_lo(pk_bf16(x, x)); }
DI float dot2bf(unsigned a, unsigned b, float c) {
  return __builtin_amdgcn_fdot2_f32_bf16(__builtin_bit_cast(bf16x2, a), __builtin_bit_cast(bf16x2, b), c, false);
}
DI s16x4 tr_read(const char* p) {
  return __builtin_amdgcn_ds_read_tr16_b64_v4i16((lds_s16x4_ptr)p);
}
DI bf16x8 cat8(s16x4 lo, s16x4 hi) { return __builtin_shufflevector(lo, hi, 0, 1, 2, 3, 4, 5, 6, 7); }
DI f32x4 mfma16(bf16x8 a, bf16x8 b, f32x4 c) { return __builtin_amdgcn_mfma_f32_16x16x32_bf16(a, b, c, 0, 0, 0); }
DI void glds16(const void* g, char* lds) {
  unsigned keep;
  const unsigned dst = __builtin_amdgcn_readfirstlane((unsigned)(size_t)lds);
  asm volatile("s_mov_b32 %0, m0\n\ts_mov_b32 m0, %2\n\ts_nop 0\n\tglobal_load_lds_dwordx4 %1, off\n\ts_mov_b32 m0, %0"
               : "=&s"(keep) : "v"(g), "s"(dst) : "memory");
}

template <int I, int N, class Fn> DI void static_for(Fn&& fn) {
  if constexpr (I < N) {
    fn(std::integral_constant<int, I>{});
    static_for<I + 1, N>(fn);
  }
}

constexpr int K1_STAGE = 49152;
constexpr int K1_LDS = 3 * K1_STAGE;

constexpr int US_PITCH = 4160;
constexpr int HQ_PITCH = 4576;
constexpr int FB_PITCH = 4224;
constexpr int CV_US = 0;
constexpr int CV_HQ = 8 * US_PITCH;
constexpr int CV_FB = CV_HQ + 8 * HQ_PITCH;

DI void gemm1_block(char* smem, const u16* __restrict__ x, const u16* __restrict__ wg, const u16* __restrict__ wu,
                    u16* __restrict__ hbuf, float2* __restrict__ stats, int mt, int ft) {
  const int tid = threadIdx.x, lane = tid & 63;
  const int w = __builtin_amdgcn_readfirstlane(tid >> 6);
  const int wm = w >> 2, wn = w & 3;
  const int r = lane & 15, g = lane >> 4, q = (lane & 15) >> 2, p = lane & 3;
  const int row0 = mt * 128, f0 = ft * 128;

  const u16* asrc[2];
  const u16* wsrc[2];
  int woff[2];
#pragma unroll
  for (int e = 0; e < 2; ++e) {
    const int i = 2 * w + e;
    const int row = 8 * i + (lane >> 3);
    asrc[e] = x + (size_t)(row0 + row) * D_ + (((lane & 7) ^ (row & 7)) << 3);
    const int kr = 4 * i + (lane >> 4);
    const int phi = (kr & 3) | (((kr >> 3) & 1) << 2);
    const int sc = ((((lane & 15) >> 1) ^ phi) << 1) | (lane & 1);
    int col = f0 + sc * 8;
    col = col > F_ - 8 ? F_ - 8 : col;
    woff[e] = kr * F_ + col;
  }
  (void)wsrc;
  auto stage = [&](int kt, int buf) {
    char* sb = smem + buf * K1_STAGE;
#pragma unroll
    for (int e = 0; e < 2; ++e) glds16(asrc[e] + kt * 64, sb + (2 * w + e) * 1024);
#pragma unroll
    for (int e = 0; e < 2; ++e) glds16(wg + woff[e] + kt * 64 * F_, sb + 16384 + (2 * w + e) * 1024);
#pragma unroll
    for (int e = 0; e < 2; ++e) glds16(wu + woff[e] + kt * 64 * F_, sb + 32768 + (2 * w + e) * 1024);
  };

  int aoff[4][2];
#pragma unroll
  for (int i = 0; i < 4; ++i)
#pragma unroll
    for (int ks = 0; ks < 2; ++ks)
      aoff[i][ks] = (64 * wm + 16 * (i ^ wn) + r) * 128 + (((4 * ks + g) ^ (r & 7)) << 4);
  const int phiL = q | ((g & 1) << 2);
  int boff[4];
#pragma unroll
  for (int T = 0; T < 4; ++T) {
    const int mat = (T >> 1) ^ wm;
    boff[T] = 16384 + mat * 16384 + (8 * g + q) * 256 + (((2 * wn + (T & 1)) ^ phiL) << 5) + p * 8;
  }

  f32x4 acc[4][4];
#pragma unroll
  for (int T = 0; T < 4; ++T)
#pragma unroll
    for (int i = 0; i < 4; ++i) acc[T][i] = f32x4{0.f, 0.f, 0.f, 0.f};
  float s1 = 0.f, s2 = 0.f, c0 = 0.f, c1 = 0.f;
  const unsigned ones = 0x3f803f80u;

  stage(0, 0);
  stage(1, 1);
  static_for<0, 8>([&](auto KT) {
    constexpr int kt = KT.value;
    if constexpr (kt < 7) wait_vm<6>(); else wait_vm<0>();
    raw_barrier();
    if constexpr (kt + 2 < 8) stage(kt + 2, (kt + 2) % 3);
    const char* sb = smem + (kt % 3) * K1_STAGE;
#pragma unroll
    for (int ks = 0; ks < 2; ++ks) {
      bf16x8 xf[4], wf[4];
#pragma unroll
      for (int T = 0; T < 4; ++T) {
        const char* a = sb + boff[T] + ks * 8192;
        wf[T] = cat8(tr_read(a), tr_read(a + 1024));
      }
#pragma unroll
      for (int i = 0; i < 4; ++i) xf[i] = *(const bf16x8*)(sb + aoff[i][ks]);
#pragma unroll
      for (int T = 0; T < 4; ++T)
#pragma unroll
        for (int i = 0; i < 4; ++i) acc[T][i] = mfma16(wf[T], xf[i], acc[T][i]);
      const u32x4 xv = __builtin_bit_cast(u32x4, xf[0]);
      const u32x4 w0 = __builtin_bit_cast(u32x4, wf[0]);
      const u32x4 w1 = __builtin_bit_cast(u32x4, wf[1]);
#pragma unroll
      for (int j = 0; j < 4; ++j) {
        s1 = dot2bf(xv[j], ones, s1);
        s2 = dot2bf(xv[j], xv[j], s2);
        c0 = dot2bf(w0[j], ones, c0);
        c1 = dot2bf(w1[j], ones, c1);
      }
    }
  });
  __syncthreads();
  float2* st_s = (float2*)smem;
  float* cs_s = (float*)(smem + 1024);
  s1 += __shfl_xor(s1, 16); s1 += __shfl_xor(s1, 32);
  s2 += __shfl_xor(s2, 16); s2 += __shfl_xor(s2, 32);
  c0 += __shfl_xor(c0, 16); c0 += __shfl_xor(c0, 32);
  c1 += __shfl_xor(c1, 16); c1 += __shfl_xor(c1, 32);
  if (g == 0) {
    const float mean = s1 * (1.f / D_);
    float var = s2 * (1.f / D_) - mean * mean;
    var = var < 0.f ? 0.f : var;
    const float mb = round_bf(mean);
    const float vb = round_bf(var);
    const float sd = round_bf(sqrtf(round_bf(vb + 1e-5f)));
    const float2 stv = make_float2(mb, 1.f / sd);
    const int row = 64 * wm + 16 * wn + r;
    st_s[row] = stv;
    if (ft == 0) stats[row0 + row] = stv;
    cs_s[wm * 128 + 32 * wn + r] = c0;
    cs_s[wm * 128 + 32 * wn + 16 + r] = c1;
  }
  __syncthreads();
  const bool valid = (f0 + 32 * wn) < F_;
  f32x4 ca[2], cb[2];
#pragma unroll
  for (int T = 0; T < 2; ++T) {
    ca[T] = *(const f32x4*)(cs_s + wm * 128 + 32 * wn + 16 * T + 4 * g);
    cb[T] = *(const f32x4*)(cs_s + (wm ^ 1) * 128 + 32 * wn + 16 * T + 4 * g);
  }
#pragma unroll
  for (int i = 0; i < 4; ++i) {
    const int row = 64 * wm + 16 * (i ^ wn) + r;
    const float2 stv = st_s[row];
    unsigned pk[2][2];
#pragma unroll
    for (int T = 0; T < 2; ++T) {
      float hv[4];
#pragma unroll
      for (int j = 0; j < 4; ++j) {
        const float ga = round_bf(stv.y * (acc[T][i][j] - stv.x * ca[T][j]));
        const float gb = round_bf(stv.y * (acc[2 + T][i][j] - stv.x * cb[T][j]));
        hv[j] = ga * gb;
      }
      pk[T][0] = pk_bf16(hv[0], hv[1]);
      pk[T][1] = pk_bf16(hv[2], hv[3]);
    }
    auto r0 = __builtin_amdgcn_permlane16_swap(pk[0][0], pk[1][0], false, false);
    auto r1 = __builtin_amdgcn_permlane16_swap(pk[0][1], pk[1][1], false, false);
    u32x4 o = {r0[0], r1[0], r0[1], r1[1]};
    if (!valid) o = u32x4{0u, 0u, 0u, 0u};
    *(u32x4*)(hbuf + (size_t)(row0 + row) * FP_ + f0 + 32 * wn + 16 * (g & 1) + 8 * (g >> 1)) = o;
  }
}

DI void conv_block(char* smem, const u16* __restrict__ kin, const u16* __restrict__ vin,
                   const u16* __restrict__ shk, const u16* __restrict__ ssmk, const u16* __restrict__ ssmd,
                   u16* __restrict__ tT, int cg) {
  const int tid = threadIdx.x, lane = tid & 63;
  const int w = __builtin_amdgcn_readfirstlane(tid >> 6);
  const int r = lane & 15, g = lane >> 4;
  const int c0 = cg * 8, ch = c0 + w;

  u32x4 kx[4], vx[4], hal[4][3];
#pragma unroll
  for (int j = 0; j < 4; ++j) {
    const int s = 512 * j + tid;
    kx[j] = *(const u32x4*)(kin + (size_t)s * D_ + c0);
    vx[j] = *(const u32x4*)(vin + (size_t)s * D_ + c0);
#pragma unroll
    for (int d = 0; d < 3; ++d) hal[j][d] = u32x4{0u, 0u, 0u, 0u};
    if (lane == 0) {
#pragma unroll
      for (int d = 0; d < 3; ++d)
        if (s - 1 - d >= 0) hal[j][d] = *(const u32x4*)(kin + (size_t)(s - 1 - d) * D_ + c0);
    }
  }
  u32x4 hx[4];
#pragma unroll
  for (int i = 0; i < 4; ++i) hx[i] = *(const u32x4*)(ssmk + (size_t)ch * S_ + (i * 64 + lane) * 8);
  float wsh[8][4];
#pragma unroll
  for (int c = 0; c < 8; ++c) {
    const u32x2 t = *(const u32x2*)(shk + (c0 + c) * 4);
    wsh[c][0] = bf_lo(t[0]); wsh[c][1] = bf_hi(t[0]); wsh[c][2] = bf_lo(t[1]); wsh[c][3] = bf_hi(t[1]);
  }
  const float dch = __uint_as_float((unsigned)ssmd[ch] << 16);

  char* us = smem + CV_US;
  char* hq = smem + CV_HQ + w * HQ_PITCH;
  char* fb = smem + CV_FB + w * FB_PITCH;
  if (lane < 30) *(u32x4*)(hq + lane * 16) = u32x4{0u, 0u, 0u, 0u};
  if (lane < 4) *(u32x4*)(us + w * US_PITCH + lane * 16) = u32x4{0u, 0u, 0u, 0u};
#pragma unroll
  for (int i = 0; i < 4; ++i) *(u32x4*)(hq + 480 + (i * 64 + lane) * 16) = hx[i];

  auto make_u = [&](int j) {
    const int s = 512 * j + tid;
    u32x4 k1, k2, k3;
#pragma unroll
    for (int d = 0; d < 4; ++d) {
      k1[d] = (unsigned)__builtin_amdgcn_update_dpp((int)hal[j][0][d], (int)kx[j][d], 0x138, 0xf, 0xf, false);
      k2[d] = (unsigned)__builtin_amdgcn_update_dpp((int)hal[j][1][d], (int)k1[d], 0x138, 0xf, 0xf, false);
      k3[d] = (unsigned)__builtin_amdgcn_update_dpp((int)hal[j][2][d], (int)k2[d], 0x138, 0xf, 0xf, false);
    }
#pragma unroll
    for (int d = 0; d < 4; ++d) {
      const int ca = 2 * d, cb = 2 * d + 1;
      float a = bf_lo(k3[d]) * wsh[ca][3];
      a = fmaf(bf_lo(k2[d]), wsh[ca][2], a);
      a = fmaf(bf_lo(k1[d]), wsh[ca][1], a);
      a = fmaf(bf_lo(kx[j][d]), wsh[ca][0], a);
      float b = bf_hi(k3[d]) * wsh[cb][3];
      b = fmaf(bf_hi(k2[d]), wsh[cb][2], b);
      b = fmaf(bf_hi(k1[d]), wsh[cb][1], b);
      b = fmaf(bf_hi(kx[j][d]), wsh[cb][0], b);
      const unsigned pk = pk_bf16(a * bf_lo(vx[j][d]), b * bf_hi(vx[j][d]));
      *(u16*)(us + ca * US_PITCH + 64 + 2 * s) = (u16)(pk & 0xffffu);
      *(u16*)(us + cb * US_PITCH + 64 + 2 * s) = (u16)(pk >> 16);
    }
  };

  f32x4 acc[8];
#pragma unroll
  for (int n = 0; n < 8; ++n) acc[n] = f32x4{0.f, 0.f, 0.f, 0.f};

  const char* usw = us + w * US_PITCH;
  const int a_lane = (32 + r - 8 * g) * 8;
  const int b_lane = 448 + 32 * r + 16 * g;

  make_u(0);
  __syncthreads();
  static_for<0, 4>([&](auto J) {
    constexpr int j = J.value;
#pragma unroll
    for (int it = 0; it < 5; ++it) {
      const int i = it * 64 + lane;
      if (i < 264) {
        const unsigned* src = (const unsigned*)(usw + 2 * (512 * j + 16 + 2 * i));
        const unsigned d2 = src[0], d1 = src[-1], d0 = src[-2];
        u32x4 e;
        e[0] = (d2 & 0xffffu) | (d1 & 0xffff0000u);
        e[1] = (d1 & 0xffffu) | (d0 & 0xffff0000u);
        e[2] = (d2 >> 16) | (d2 << 16);
        e[3] = (d1 >> 16) | (d1 << 16);
        *(u32x4*)(fb + i * 16) = e;
      }
    }
    if constexpr (j < 3) make_u(j + 1);
    wait_lgkm0();
#pragma unroll
    for (int k0 = 0; k0 < 8; ++k0) {
      bf16x8 af[2];
#pragma unroll
      for (int kk = 0; kk < 2; ++kk) {
        const char* a = fb + a_lane + (8 * kk + k0) * 256;
        const s16x4 lo = *(const s16x4*)a;
        const s16x4 hi = *(const s16x4*)(a - 32);
        af[kk] = cat8(lo, hi);
      }
#pragma unroll
      for (int nu = 0; nu < 8 - 2 * j; ++nu) {
        const bf16x8 bfr = *(const bf16x8*)(hq + b_lane + 512 * nu - 64 * k0);
        acc[2 * j + nu] = mfma16(af[0], bfr, acc[2 * j + nu]);
        if (2 * j + 1 + nu < 8) acc[2 * j + 1 + nu] = mfma16(af[1], bfr, acc[2 * j + 1 + nu]);
      }
    }
    if constexpr (j < 3) __syncthreads();
  });
#pragma unroll
  for (int n = 0; n < 8; ++n) {
    const int s = 256 * n + 16 * r + 4 * g;
    const u32x2 uu = *(const u32x2*)(usw + 64 + 2 * s);
    float t[4];
    t[0] = round_bf(acc[n][0]) + round_bf(bf_lo(uu[0]) * dch);
    t[1] = round_bf(acc[n][1]) + round_bf(bf_hi(uu[0]) * dch);
    t[2] = round_bf(acc[n][2]) + round_bf(bf_lo(uu[1]) * dch);
    t[3] = round_bf(acc[n][3]) + round_bf(bf_hi(uu[1]) * dch);
    u32x2 o = {pk_bf16(t[0], t[1]), pk_bf16(t[2], t[3])};
    *(u32x2*)(tT + (size_t)ch * S_ + s) = o;
  }
}

__global__ __launch_bounds__(512, 2) void k1_kernel(const u16* __restrict__ x, const u16* __restrict__ kin,
                                                     const u16* __restrict__ vin, const u16* __restrict__ shk,
                                                     const u16* __restrict__ ssmk, const u16* __restrict__ ssmd,
                                                     const u16* __restrict__ wg, const u16* __restrict__ wu,
                                                     u16* __restrict__ hbuf, u16* __restrict__ tT,
                                                     float2* __restrict__ stats) {
  __shared__ __attribute__((aligned(16))) char smem[K1_LDS];
  const int b = blockIdx.x, xcd = b & 7, idx = b >> 3;
  if (idx < 8) {
    conv_block(smem, kin, vin, shk, ssmk, ssmd, tT, xcd * 8 + idx);
  } else {
    const int j = idx - 8;
    gemm1_block(smem, x, wg, wu, hbuf, stats, 2 * xcd + j / 11, j % 11);
  }
}

constexpr int K2_NB = 8;
constexpr int K2_STAGE = 16384;
constexpr int K2_TT = K2_NB * K2_STAGE;
constexpr int K2_LDS = K2_TT + 8192;
constexpr int K2_NT = FP_ / 64;

__global__ __launch_bounds__(512, 2) void k2_kernel(const u16* __restrict__ x, const u16* __restrict__ hbuf,
                                                     const u16* __restrict__ wd, const u16* __restrict__ tT,
                                                     const float2* __restrict__ stats, u16* __restrict__ out) {
  __shared__ __attribute__((aligned(16))) char smem[K2_LDS];
  const int tid = threadIdx.x, lane = tid & 63;
  const int w = __builtin_amdgcn_readfirstlane(tid >> 6);
  const int kg = w >> 2, wm = (w >> 1) & 1, wn = w & 1;
  const int r = lane & 15, g = lane >> 4, q = (lane & 15) >> 2, p = lane & 3;
  const int b = blockIdx.x, xcd = b & 7, idx = b >> 3;
  const int row0 = (4 * xcd + (idx >> 3)) * 64, d0 = (idx & 7) * 64;

  const int erow = row0 + 32 * wm + 16 * kg + r;
  const int edoff = d0 + 32 * wn + 16 * (g & 1) + 8 * (g >> 1);
  u32x4 xv = *(const u32x4*)(x + (size_t)erow * D_ + edoff);
  f32x2 stv = *(const f32x2*)(stats + erow);
  glds16(tT + (size_t)(d0 + 8 * w + (lane >> 3)) * S_ + row0 + (lane & 7) * 8, smem + K2_TT + w * 1024);

  const int srow = 8 * w + (lane >> 3);
  const u16* asrc = hbuf + (size_t)(row0 + srow) * FP_ + (((lane & 7) ^ (srow & 7)) << 3);
  const int phi_s = ((srow >> 1) & 1) | (((srow >> 3) & 1) << 1);
  const int wcol = d0 + ((((((lane & 7) >> 1) ^ phi_s) << 1) | (lane & 1)) << 3);
  auto stage = [&](int kt) {
    char* sb = smem + (kt % K2_NB) * K2_STAGE;
    glds16(asrc + kt * 64, sb + w * 1024);
    int kr = kt * 64 + srow;
    kr = kr > F_ - 1 ? F_ - 1 : kr;
    glds16(wd + (size_t)kr * D_ + wcol, sb + 8192 + w * 1024);
  };

  int aoff[2], boff[2];
#pragma unroll
  for (int i = 0; i < 2; ++i) aoff[i] = (32 * wm + 16 * i + r) * 128 + (((4 * kg + g) ^ (r & 7)) << 4);
  const int phiL = ((q >> 1) & 1) | ((g & 1) << 1);
#pragma unroll
  for (int T = 0; T < 2; ++T)
    boff[T] = 8192 + (32 * kg + 8 * g + q) * 128 + (((2 * wn + T) ^ phiL) << 5) + p * 8;

  f32x4 acc[2][2];
#pragma unroll
  for (int T = 0; T < 2; ++T)
#pragma unroll
    for (int i = 0; i < 2; ++i) acc[T][i] = f32x4{0.f, 0.f, 0.f, 0.f};

#pragma unroll
  for (int t = 0; t < K2_NB - 1; ++t) stage(t);
  static_for<0, K2_NT>([&](auto KT) {
    constexpr int kt = KT.value;
    constexpr int inflight = (K2_NT - 1 - kt) < (K2_NB - 2) ? (K2_NT - 1 - kt) : (K2_NB - 2);
    wait_vm<2 * inflight>();
    raw_barrier();
    if constexpr (kt + K2_NB - 1 < K2_NT) stage(kt + K2_NB - 1);
    const char* sb = smem + (kt % K2_NB) * K2_STAGE;
    bf16x8 hf[2], wf[2];
#pragma unroll
    for (int T = 0; T < 2; ++T) wf[T] = cat8(tr_read(sb + boff[T]), tr_read(sb + boff[T] + 512));
#pragma unroll
    for (int i = 0; i < 2; ++i) hf[i] = *(const bf16x8*)(sb + aoff[i]);
#pragma unroll
    for (int T = 0; T < 2; ++T)
#pragma unroll
      for (int i = 0; i < 2; ++i) acc[T][i] = mfma16(wf[T], hf[i], acc[T][i]);
  });
  __syncthreads();
  asm volatile("" : "+v"(xv), "+v"(stv));
  {
    f32x4* xb = (f32x4*)smem;
    const int wq = w & 3;
#pragma unroll
    for (int T = 0; T < 2; ++T) xb[((kg * 4 + wq) * 2 + T) * 64 + lane] = kg ? acc[T][0] : acc[T][1];
    __syncthreads();
    f32x4 fin[2];
#pragma unroll
    for (int T = 0; T < 2; ++T) {
      const f32x4 o = xb[(((kg ^ 1) * 4 + wq) * 2 + T) * 64 + lane];
      fin[T] = (kg ? acc[T][1] : acc[T][0]) + o;
    }
    unsigned pb[2][2], pt[2][2];
#pragma unroll
    for (int T = 0; T < 2; ++T) {
      pb[T][0] = pk_bf16(fin[T][0], fin[T][1]);
      pb[T][1] = pk_bf16(fin[T][2], fin[T][3]);
      const s16x4 tv = tr_read(smem + K2_TT + (32 * wn + 16 * T + 4 * g + q) * 128 + (32 * wm + 16 * kg + 4 * p) * 2);
      const u32x2 tu = __builtin_bit_cast(u32x2, tv);
      pt[T][0] = tu[0];
      pt[T][1] = tu[1];
    }
    auto b0 = __builtin_amdgcn_permlane16_swap(pb[0][0], pb[1][0], false, false);
    auto b1 = __builtin_amdgcn_permlane16_swap(pb[0][1], pb[1][1], false, false);
    auto t0 = __builtin_amdgcn_permlane16_swap(pt[0][0], pt[1][0], false, false);
    auto t1 = __builtin_amdgcn_permlane16_swap(pt[0][1], pt[1][1], false, false);
    const u32x4 bv = {b0[0], b1[0], b0[1], b1[1]};
    const u32x4 tv = {t0[0], t1[0], t0[1], t1[1]};
    u32x4 o;
#pragma unroll
    for (int j = 0; j < 4; ++j) {
      float res[2];
#pragma unroll
      for (int hh = 0; hh < 2; ++hh) {
        const float xe = hh ? bf_hi(xv[j]) : bf_lo(xv[j]);
        const float te = hh ? bf_hi(tv[j]) : bf_lo(tv[j]);
        const float be = hh ? bf_hi(bv[j]) : bf_lo(bv[j]);
        const float xn = round_bf(round_bf(xe - stv[0]) * stv[1]);
        const float a = round_bf(xn * te);
        res[hh] = round_bf(xe + a) + be;
      }
      o[j] = pk_bf16(res[0], res[1]);
    }
    *(u32x4*)(out + (size_t)erow * D_ + edoff) = o;
  }
}

extern "C" void kernel_launch(void* const* d_in, const int* in_sizes, int n_in, void* d_out, int out_size,
                              void* d_ws, size_t ws_size, hipStream_t stream) {
  const u16* q = (const u16*)d_in[0];
  const u16* k = (const u16*)d_in[1];
  const u16* v = (const u16*)d_in[2];
  const u16* shk = (const u16*)d_in[3];
  const u16* ssmk = (const u16*)d_in[4];
  const u16* ssmd = (const u16*)d_in[5];
  const u16* wg = (const u16*)d_in[7];
  const u16* wu = (const u16*)d_in[8];
  const u16* wd = (const u16*)d_in[9];
  char* ws = (char*)d_ws;
  u16* hbuf = (u16*)(ws + WS_H);
  u16* tT = (u16*)(ws + WS_T);
  float2* stats = (float2*)(ws + WS_ST);
  k1_kernel<<<240, 512, 0, stream>>>(q, k, v, shk, ssmk, ssmd, wg, wu, hbuf, tT, stats);
  k2_kernel<<<256, 512, 0, stream>>>(q, hbuf, wd, tT, stats, (u16*)d_out);
}
```

```cpp
#include <hip/hip_runtime.h>
#include <hip/hip_bf16.h>
#include <stdint.h>

typedef __attribute__((ext_vector_type(8))) short bf16x8;
typedef __attribute__((ext_vector_type(4))) short s16x4;
typedef __attribute__((ext_vector_type(2))) __bf16 bf16x2;
typedef __attribute__((ext_vector_type(4))) float f32x4;
typedef __attribute__((ext_vector_type(2))) float f32x2;
typedef __attribute__((ext_vector_type(4))) unsigned u32x4;
typedef __attribute__((ext_vector_type(2))) unsigned u32x2;
typedef __attribute__((address_space(3))) s16x4* lds_s16x4_ptr;
typedef unsigned short u16;

#define DI __device__ __forceinline__

constexpr int S_ = 2048, D_ = 512, F_ = 1376, FP_ = 1408;

constexpr size_t WS_H = 0;
constexpr size_t WS_T = 6u * 1024 * 1024;
constexpr size_t WS_ST = 8u * 1024 * 1024 + 512 * 1024;

template <int N> DI void wait_vm() { asm volatile("s_waitcnt vmcnt(%0)" ::"i"(N) : "memory"); }
DI void wait_lgkm0() { asm volatile("s_waitcnt lgkmcnt(0)" ::: "memory"); }
DI void raw_barrier() {
  asm volatile("s_waitcnt lgkmcnt(0)" ::: "memory");
  __builtin_amdgcn_s_barrier();
  asm volatile("" ::: "memory");
}

DI unsigned pk_bf16(float lo, float hi) {
  f32x2 f = {lo, hi};
  return __builtin_bit_cast(unsigned, __builtin_convertvector(f, bf16x2));
}
DI float bf_lo(unsigned v) { return __uint_as_float(v << 16); }
DI float bf_hi(unsigned v) { return __uint_as_float(v & 0xffff0000u); }
DI float round_bf(float x) { return bf_lo(pk_bf16(x, x)); }
DI float dot2bf(unsigned a, unsigned b, float c) {
  return __builtin_amdgcn_fdot2_f32_bf16(__builtin_bit_cast(bf16x2, a), __builtin_bit_cast(bf16x2, b), c, false);
}
DI s16x4 tr_read(const char* p) {
  return __builtin_amdgcn_ds_read_tr16_b64_v4i16((lds_s16x4_ptr)p);
}
DI bf16x8 cat8(s16x4 lo, s16x4 hi) { return __builtin_shufflevector(lo, hi, 0, 1, 2, 3, 4, 5, 6, 7); }
DI f32x4 mfma16(bf16x8 a, bf16x8 b, f32x4 c) { return __builtin_amdgcn_mfma_f32_16x16x32_bf16(a, b, c, 0, 0, 0); }

DI void glds16(const void* g, char* lds) {
  unsigned keep;
  const unsigned dst = __builtin_amdgcn_readfirstlane((unsigned)(size_t)lds);
  asm volatile("s_mov_b32 %0, m0\n\ts_mov_b32 m0, %2\n\ts_nop 0\n\tglobal_load_lds_dwordx4 %1, off\n\ts_mov_b32 m0, %0"
               : "=&s"(keep) : "v"(g), "s"(dst) : "memory");
}
DI void touch(const void* g, char* dummy) {
  unsigned keep;
  const unsigned dst = __builtin_amdgcn_readfirstlane((unsigned)(size_t)dummy);
  asm volatile("s_mov_b32 %0, m0\n\ts_mov_b32 m0, %2\n\ts_nop 0\n\tglobal_load_lds_dword %1, off\n\ts_mov_b32 m0, %0"
               : "=&s"(keep) : "v"(g), "s"(dst) : "memory");
}

#define STAMP_DECL do {} while (0)
#define STAMP(I) do {} while (0)
#define STAMP_FLUSH(K) do {} while (0)
#define STAMP_ARG
#define STAMP_PASS

template <int I, int N, class Fn> DI void static_for(Fn&& fn) {
  if constexpr (I < N) {
    fn(std::integral_constant<int, I>{});
    static_for<I + 1, N>(fn);
  }
}

constexpr int K1_STAGE = 49152;
constexpr int K1_DUMMY = 3 * K1_STAGE;
constexpr int K1_LDS = 3 * K1_STAGE + 256;

constexpr int US_PITCH = 4160;
constexpr int HQ_PITCH = 4576;
constexpr int FB_PITCH = 4224;
constexpr int CV_US = 0;
constexpr int CV_HQ = 8 * US_PITCH;
constexpr int CV_FB = CV_HQ + 8 * HQ_PITCH;

DI void gemm1_block(char* smem, const u16* __restrict__ x, const u16* __restrict__ wg, const u16* __restrict__ wu,
                    u16* __restrict__ hbuf, float2* __restrict__ stats, int mt, int ft STAMP_ARG) {
  const int tid = threadIdx.x, lane = tid & 63;
  const int w = __builtin_amdgcn_readfirstlane(tid >> 6);
  const int wm = w >> 2, wn = w & 3;
  const int r = lane & 15, g = lane >> 4, q = (lane & 15) >> 2, p = lane & 3;
  const int row0 = mt * 128, f0 = ft * 128;

  const u16* asrc[2];
  const u16* gsrc[2];
  const u16* usrc[2];
#pragma unroll
  for (int e = 0; e < 2; ++e) {
    const int i = 2 * w + e;
    const int row = 8 * i + (lane >> 3);
    asrc[e] = x + (size_t)(row0 + row) * D_ + (((lane & 7) ^ (row & 7)) << 3);
    const int kr = 4 * i + (lane >> 4);
    const int phi = (kr & 3) | (((kr >> 3) & 1) << 2);
    const int sc = ((((lane & 15) >> 1) ^ phi) << 1) | (lane & 1);
    int col = f0 + sc * 8;
    col = col > F_ - 8 ? F_ - 8 : col;
    gsrc[e] = wg + kr * F_ + col;
    usrc[e] = wu + kr * F_ + col;
  }
  auto stage = [&](int kt, int buf) {
    char* sb = smem + buf * K1_STAGE;
#pragma unroll
    for (int e = 0; e < 2; ++e) glds16(asrc[e] + kt * 64, sb + (2 * w + e) * 1024);
#pragma unroll
    for (int e = 0; e < 2; ++e) glds16(gsrc[e] + kt * 64 * F_, sb + 16384 + (2 * w + e) * 1024);
#pragma unroll
    for (int e = 0; e < 2; ++e) glds16(usrc[e] + kt * 64 * F_, sb + 32768 + (2 * w + e) * 1024);
  };

  stage(0, 0);
  stage(1, 1);
  {
    const int kr = 64 * (mt & 7) + (tid >> 3) % 64;
    const int sel = tid & 7;
    int col = f0 + (sel & 1) * 64;
    col = col > F_ - 2 ? F_ - 2 : col;
    const u16* pw = ((sel & 2) ? wu : wg) + kr * F_ + col;
    const int t2 = (tid >> 3) * 4 + (tid & 3);
    const int xr = 3 * (t2 >> 3) + (ft % 3);
    const u16* px = x + (size_t)(row0 + xr) * D_ + (t2 & 7) * 64;
    touch((sel & 4) ? px : pw, smem + K1_DUMMY);
    const int xr2 = 96 + 3 * ((tid >> 3) % 11) + (ft % 3);
    const u16* px2 = x + (size_t)(row0 + (xr2 > 127 ? 127 : xr2)) * D_ + (tid & 7) * 64;
    touch(px2, smem + K1_DUMMY);
  }

  int aoff[4][2];
#pragma unroll
  for (int i = 0; i < 4; ++i)
#pragma unroll
    for (int ks = 0; ks < 2; ++ks)
      aoff[i][ks] = (64 * wm + 16 * (i ^ wn) + r) * 128 + (((4 * ks + g) ^ (r & 7)) << 4);
  const int phiL = q | ((g & 1) << 2);
  int boff[4];
#pragma unroll
  for (int T = 0; T < 4; ++T) {
    const int mat = (T >> 1) ^ wm;
    boff[T] = 16384 + mat * 16384 + (8 * g + q) * 256 + (((2 * wn + (T & 1)) ^ phiL) << 5) + p * 8;
  }

  f32x4 acc[4][4];
#pragma unroll
  for (int T = 0; T < 4; ++T)
#pragma unroll
    for (int i = 0; i < 4; ++i) acc[T][i] = f32x4{0.f, 0.f, 0.f, 0.f};
  float s1 = 0.f, s2 = 0.f, c0 = 0.f, c1 = 0.f;
  const unsigned ones = 0x3f803f80u;

  int buf = 0;
#pragma unroll 1
  for (int kt = 0; kt < 8; ++kt) {
    if (kt < 2) wait_vm<8>(); else if (kt < 7) wait_vm<6>(); else wait_vm<0>();
    raw_barrier();
    const int nb = buf == 0 ? 2 : buf - 1;
    if (kt < 6) stage(kt + 2, nb);
    const char* sb = smem + buf * K1_STAGE;
    bf16x8 xf[2][4], wf[2][4];
#pragma unroll
    for (int ks = 0; ks < 2; ++ks) {
#pragma unroll
      for (int T = 0; T < 4; ++T) {
        const char* a = sb + boff[T] + ks * 8192;
        wf[ks][T] = cat8(tr_read(a), tr_read(a + 1024));
      }
#pragma unroll
      for (int i = 0; i < 4; ++i) xf[ks][i] = *(const bf16x8*)(sb + aoff[i][ks]);
    }
#pragma unroll
    for (int ks = 0; ks < 2; ++ks) {
#pragma unroll
      for (int T = 0; T < 4; ++T)
#pragma unroll
        for (int i = 0; i < 4; ++i) acc[T][i] = mfma16(wf[ks][T], xf[ks][i], acc[T][i]);
      const u32x4 xv = __builtin_bit_cast(u32x4, xf[ks][0]);
      const u32x4 w0 = __builtin_bit_cast(u32x4, wf[ks][0]);
      const u32x4 w1 = __builtin_bit_cast(u32x4, wf[ks][1]);
#pragma unroll
      for (int j = 0; j < 4; ++j) {
        s1 = dot2bf(xv[j], ones, s1);
        s2 = dot2bf(xv[j], xv[j], s2);
        c0 = dot2bf(w0[j], ones, c0);
        c1 = dot2bf(w1[j], ones, c1);
      }
    }
    buf = buf == 2 ? 0 : buf + 1;
  }
  __syncthreads();
  STAMP(9);
  float2* st_s = (float2*)smem;
  float* cs_s = (float*)(smem + 1024);
  s1 += __shfl_xor(s1, 16); s1 += __shfl_xor(s1, 32);
  s2 += __shfl_xor(s2, 16); s2 += __shfl_xor(s2, 32);
  c0 += __shfl_xor(c0, 16); c0 += __shfl_xor(c0, 32);
  c1 += __shfl_xor(c1, 16); c1 += __shfl_xor(c1, 32);
  if (g == 0) {
    const float mean = s1 * (1.f / D_);
    float var = s2 * (1.f / D_) - mean * mean;
    var = var < 0.f ? 0.f : var;
    const float mb = round_bf(mean);
    const float vb = round_bf(var);
    const float sd = round_bf(sqrtf(round_bf(vb + 1e-5f)));
    const float2 stv = make_float2(mb, 1.f / sd);
    const int row = 64 * wm + 16 * wn + r;
    st_s[row] = stv;
    if (ft == 0) stats[row0 + row] = stv;
    cs_s[wm * 128 + 32 * wn + r] = c0;
    cs_s[wm * 128 + 32 * wn + 16 + r] = c1;
  }
  __syncthreads();
  const bool valid = (f0 + 32 * wn) < F_;
  f32x4 ca[2], cb[2];
#pragma unroll
  for (int T = 0; T < 2; ++T) {
    ca[T] = *(const f32x4*)(cs_s + wm * 128 + 32 * wn + 16 * T + 4 * g);
    cb[T] = *(const f32x4*)(cs_s + (wm ^ 1) * 128 + 32 * wn + 16 * T + 4 * g);
  }
#pragma unroll
  for (int i = 0; i < 4; ++i) {
    const int row = 64 * wm + 16 * (i ^ wn) + r;
    const float2 stv = st_s[row];
    unsigned pk[2][2];
#pragma unroll
    for (int T = 0; T < 2; ++T) {
      float hv[4];
#pragma unroll
      for (int j = 0; j < 4; ++j) {
        const float ga = round_bf(stv.y * (acc[T][i][j] - stv.x * ca[T][j]));
        const float gb = round_bf(stv.y * (acc[2 + T][i][j] - stv.x * cb[T][j]));
        hv[j] = ga * gb;
      }
      pk[T][0] = pk_bf16(hv[0], hv[1]);
      pk[T][1] = pk_bf16(hv[2], hv[3]);
    }
    auto r0 = __builtin_amdgcn_permlane16_swap(pk[0][0], pk[1][0], false, false);
    auto r1 = __builtin_amdgcn_permlane16_swap(pk[0][1], pk[1][1], false, false);
    u32x4 o = {r0[0], r1[0], r0[1], r1[1]};
    if (!valid) o = u32x4{0u, 0u, 0u, 0u};
    *(u32x4*)(hbuf + (size_t)(row0 + row) * FP_ + f0 + 32 * wn + 16 * (g & 1) + 8 * (g >> 1)) = o;
  }
}

DI void conv_block(char* smem, const u16* __restrict__ kin, const u16* __restrict__ vin,
                   const u16* __restrict__ shk, const u16* __restrict__ ssmk, const u16* __restrict__ ssmd,
                   u16* __restrict__ tT, int cg STAMP_ARG) {
  const int tid = threadIdx.x, lane = tid & 63;
  const int w = __builtin_amdgcn_readfirstlane(tid >> 6);
  const int r = lane & 15, g = lane >> 4;
  const int c0 = cg * 8, ch = c0 + w;

  u32x4 hx[4];
#pragma unroll
  for (int i = 0; i < 4; ++i) hx[i] = *(const u32x4*)(ssmk + (size_t)ch * S_ + (i * 64 + lane) * 8);
  touch(((tid & 256) ? vin : kin) + (size_t)(256 * (cg & 7) + (tid & 255)) * D_ + (cg >> 3) * 64, smem + K1_DUMMY);
  u32x4 kx[4], vx[4], hal[4][3];
#pragma unroll
  for (int j = 0; j < 4; ++j) {
    const int s = 512 * j + tid;
    kx[j] = *(const u32x4*)(kin + (size_t)s * D_ + c0);
    vx[j] = *(const u32x4*)(vin + (size_t)s * D_ + c0);
#pragma unroll
    for (int d = 0; d < 3; ++d) hal[j][d] = u32x4{0u, 0u, 0u, 0u};
    if (lane == 0) {
#pragma unroll
      for (int d = 0; d < 3; ++d)
        if (s - 1 - d >= 0) hal[j][d] = *(const u32x4*)(kin + (size_t)(s - 1 - d) * D_ + c0);
    }
  }
  float wsh[8][4];
#pragma unroll
  for (int c = 0; c < 8; ++c) {
    const u32x2 t = *(const u32x2*)(shk + (c0 + c) * 4);
    wsh[c][0] = bf_lo(t[0]); wsh[c][1] = bf_hi(t[0]); wsh[c][2] = bf_lo(t[1]); wsh[c][3] = bf_hi(t[1]);
  }
  const float dch = __uint_as_float((unsigned)ssmd[ch] << 16);

  char* us = smem + CV_US;
  char* hq = smem + CV_HQ + w * HQ_PITCH;
  char* fb = smem + CV_FB + w * FB_PITCH;
  if (lane < 30) *(u32x4*)(hq + lane * 16) = u32x4{0u, 0u, 0u, 0u};
  if (lane < 4) *(u32x4*)(us + w * US_PITCH + lane * 16) = u32x4{0u, 0u, 0u, 0u};
#pragma unroll
  for (int i = 0; i < 4; ++i) *(u32x4*)(hq + 480 + (i * 64 + lane) * 16) = hx[i];

  auto make_u = [&](int j) {
    const int s = 512 * j + tid;
    u32x4 k1, k2, k3;
#pragma unroll
    for (int d = 0; d < 4; ++d) {
      k1[d] = (unsigned)__builtin_amdgcn_update_dpp((int)hal[j][0][d], (int)kx[j][d], 0x138, 0xf, 0xf, false);
      k2[d] = (unsigned)__builtin_amdgcn_update_dpp((int)hal[j][1][d], (int)k1[d], 0x138, 0xf, 0xf, false);
      k3[d] = (unsigned)__builtin_amdgcn_update_dpp((int)hal[j][2][d], (int)k2[d], 0x138, 0xf, 0xf, false);
    }
#pragma unroll
    for (int d = 0; d < 4; ++d) {
      const int ca = 2 * d, cb = 2 * d + 1;
      float a = bf_lo(k3[d]) * wsh[ca][3];
      a = fmaf(bf_lo(k2[d]), wsh[ca][2], a);
      a = fmaf(bf_lo(k1[d]), wsh[ca][1], a);
      a = fmaf(bf_lo(kx[j][d]), wsh[ca][0], a);
      float b = bf_hi(k3[d]) * wsh[cb][3];
      b = fmaf(bf_hi(k2[d]), wsh[cb][2], b);
      b = fmaf(bf_hi(k1[d]), wsh[cb][1], b);
      b = fmaf(bf_hi(kx[j][d]), wsh[cb][0], b);
      const unsigned pk = pk_bf16(a * bf_lo(vx[j][d]), b * bf_hi(vx[j][d]));
      *(u16*)(us + ca * US_PITCH + 64 + 2 * s) = (u16)(pk & 0xffffu);
      *(u16*)(us + cb * US_PITCH + 64 + 2 * s) = (u16)(pk >> 16);
    }
  };

  f32x4 acc[8];
#pragma unroll
  for (int n = 0; n < 8; ++n) acc[n] = f32x4{0.f, 0.f, 0.f, 0.f};

  const char* usw = us + w * US_PITCH;
  const char* a_lane = fb + (32 + r - 8 * g) * 8;
  const char* b_lane = hq + 448 + 32 * r + 16 * g;

  make_u(0);
  __syncthreads();
  static_for<0, 4>([&](auto J) {
    constexpr int j = J.value;
    constexpr int NNU = 8 - 2 * j;
    STAMP(1 + j);
#pragma unroll
    for (int it = 0; it < 5; ++it) {
      const int i = it * 64 + lane;
      if (i < 264) {
        const unsigned* src = (const unsigned*)(usw + 2 * (512 * j + 16 + 2 * i));
        const unsigned d2 = src[0], d1 = src[-1], d0 = src[-2];
        u32x4 e;
        e[0] = (d2 & 0xffffu) | (d1 & 0xffff0000u);
        e[1] = (d1 & 0xffffu) | (d0 & 0xffff0000u);
        e[2] = (d2 >> 16) | (d2 << 16);
        e[3] = (d1 >> 16) | (d1 << 16);
        *(u32x4*)(fb + i * 16) = e;
      }
    }
    if constexpr (j < 3) make_u(j + 1);
    wait_lgkm0();
    bf16x8 af[2][2], bfr[2][NNU];
    auto load_set = [&](int set, int k0) {
#pragma unroll
      for (int kk = 0; kk < 2; ++kk) {
        const char* a = a_lane + (8 * kk + k0) * 256;
        af[set][kk] = cat8(*(const s16x4*)a, *(const s16x4*)(a - 32));
      }
#pragma unroll
      for (int nu = 0; nu < NNU; ++nu) bfr[set][nu] = *(const bf16x8*)(b_lane + 512 * nu - 64 * k0);
    };
    auto mma_set = [&](int set) {
#pragma unroll
      for (int nu = 0; nu < NNU; ++nu) {
        acc[2 * j + nu] = mfma16(af[set][0], bfr[set][nu], acc[2 * j + nu]);
        if (2 * j + 1 + nu < 8) acc[2 * j + 1 + nu] = mfma16(af[set][1], bfr[set][nu], acc[2 * j + 1 + nu]);
      }
    };
    load_set(0, 0);
#pragma unroll 1
    for (int k0 = 0; k0 < 8; k0 += 2) {
      load_set(1, k0 + 1);
      mma_set(0);
      load_set(0, (k0 + 2) & 7);
      mma_set(1);
    }
    if constexpr (j < 3) __syncthreads();
  });
  STAMP(9);
#pragma unroll
  for (int n = 0; n < 8; ++n) {
    const int s = 256 * n + 16 * r + 4 * g;
    const u32x2 uu = *(const u32x2*)(usw + 64 + 2 * s);
    float t[4];
    t[0] = round_bf(acc[n][0]) + round_bf(bf_lo(uu[0]) * dch);
    t[1] = round_bf(acc[n][1]) + round_bf(bf_hi(uu[0]) * dch);
    t[2] = round_bf(acc[n][2]) + round_bf(bf_lo(uu[1]) * dch);
    t[3] = round_bf(acc[n][3]) + round_bf(bf_hi(uu[1]) * dch);
    u32x2 o = {pk_bf16(t[0], t[1]), pk_bf16(t[2], t[3])};
    *(u32x2*)(tT + (size_t)ch * S_ + s) = o;
  }
}

__global__ __launch_bounds__(512, 2) void k1_kernel(const u16* __restrict__ x, const u16* __restrict__ kin,
                                                     const u16* __restrict__ vin, const u16* __restrict__ shk,
                                                     const u16* __restrict__ ssmk, const u16* __restrict__ ssmd,
                                                     const u16* __restrict__ wg, const u16* __restrict__ wu,
                                                     const u16* __restrict__ wd,
                                                     u16* __restrict__ hbuf, u16* __restrict__ tT,
                                                     float2* __restrict__ stats) {
  __shared__ __attribute__((aligned(16))) char smem[K1_LDS];
  const int b = blockIdx.x, xcd = b & 7, idx = b >> 3;
  STAMP_DECL;
  STAMP(0);
  if (idx < 8) {
    conv_block(smem, kin, vin, shk, ssmk, ssmd, tT, xcd * 8 + idx STAMP_PASS);
  } else if (idx < 30) {
    const int t = 22 * (xcd & 3) + (idx - 8);
    gemm1_block(smem, x, wg, wu, hbuf, stats, 8 * (xcd >> 2) + (t & 7), t >> 3 STAMP_PASS);
  } else {
    const int part = (idx - 30) * 8 + xcd;
    for (int i = threadIdx.x; i < 688; i += 512) touch(wd + (size_t)(part * 688 + i) * 64, smem + K1_DUMMY);
    wait_vm<0>();
  }
  STAMP(15);
  STAMP_FLUSH(0);
}

constexpr int K2_NB = 8;
constexpr int K2_STAGE = 16384;
constexpr int K2_TT = K2_NB * K2_STAGE;
constexpr int K2_X = K2_TT + 8192;
constexpr int K2_ST = K2_X + 8192;
constexpr int K2_DUMMY = K2_ST + 1024;
constexpr int K2_LDS = K2_DUMMY + 256;
constexpr int K2_NT = FP_ / 64;

__global__ __launch_bounds__(512, 2) void k2_kernel(const u16* __restrict__ x, const u16* __restrict__ hbuf,
                                                     const u16* __restrict__ wd, const u16* __restrict__ tT,
                                                     const float2* __restrict__ stats, u16* __restrict__ out) {
  __shared__ __attribute__((aligned(16))) char smem[K2_LDS];
  const int tid = threadIdx.x, lane = tid & 63;
  const int w = __builtin_amdgcn_readfirstlane(tid >> 6);
  const int kg = w >> 2, wm = (w >> 1) & 1, wn = w & 1;
  const int r = lane & 15, g = lane >> 4, q = (lane & 15) >> 2, p = lane & 3;
  const int b = blockIdx.x, xcd = b & 7, idx = b >> 3;
  const int row0 = (4 * xcd + (idx >> 3)) * 64, d0 = (idx & 7) * 64;
  STAMP_DECL;
  STAMP(0);

  const int srow = 8 * w + (lane >> 3);
  const u16* asrc = hbuf + (size_t)(row0 + srow) * FP_ + (((lane & 7) ^ (srow & 7)) << 3);
  const int phi_s = ((srow >> 1) & 1) | (((srow >> 3) & 1) << 1);
  const int wcol = d0 + ((((((lane & 7) >> 1) ^ phi_s) << 1) | (lane & 1)) << 3);
  auto stage = [&](int kt) {
    char* sb = smem + (kt & (K2_NB - 1)) * K2_STAGE;
    glds16(asrc + kt * 64, sb + w * 1024);
    int kr = kt * 64 + srow;
    kr = kr > F_ - 1 ? F_ - 1 : kr;
    glds16(wd + (size_t)kr * D_ + wcol, sb + 8192 + w * 1024);
  };
  stage(0);
  stage(1);
  {
    const bool ish = tid < 176;
    const int hr = tid / 22, hl = tid % 22;
    int kr = 344 * (idx >> 3) + (tid - 176);
    kr = kr > F_ - 1 ? F_ - 1 : kr;
    const u16* ph = hbuf + (size_t)(row0 + 8 * (idx & 7) + (hr & 7)) * FP_ + hl * 64;
    const u16* pw = wd + (size_t)kr * D_ + d0;
    touch(ish ? ph : pw, smem + K2_DUMMY);
  }
#pragma unroll
  for (int t = 2; t < K2_NB - 1; ++t) stage(t);

  const int erow = row0 + 32 * wm + 16 * kg + r;
  const int edoff = d0 + 32 * wn + 16 * (g & 1) + 8 * (g >> 1);

  int aoff[2], boff[2];
#pragma unroll
  for (int i = 0; i < 2; ++i) aoff[i] = (32 * wm + 16 * i + r) * 128 + (((4 * kg + g) ^ (r & 7)) << 4);
  const int phiL = ((q >> 1) & 1) | ((g & 1) << 1);
#pragma unroll
  for (int T = 0; T < 2; ++T)
    boff[T] = 8192 + (32 * kg + 8 * g + q) * 128 + (((2 * wn + T) ^ phiL) << 5) + p * 8;

  f32x4 acc[2][2];
#pragma unroll
  for (int T = 0; T < 2; ++T)
#pragma unroll
    for (int i = 0; i < 2; ++i) acc[T][i] = f32x4{0.f, 0.f, 0.f, 0.f};

  auto compute = [&](int kt) {
    const char* sb = smem + (kt & (K2_NB - 1)) * K2_STAGE;
    bf16x8 hf[2], wf[2];
#pragma unroll
    for (int T = 0; T < 2; ++T) wf[T] = cat8(tr_read(sb + boff[T]), tr_read(sb + boff[T] + 512));
#pragma unroll
    for (int i = 0; i < 2; ++i) hf[i] = *(const bf16x8*)(sb + aoff[i]);
#pragma unroll
    for (int T = 0; T < 2; ++T)
#pragma unroll
      for (int i = 0; i < 2; ++i) acc[T][i] = mfma16(wf[T], hf[i], acc[T][i]);
  };

#pragma unroll 1
  for (int kt = 0; kt < K2_NT - K2_NB + 1; ++kt) {
    if (kt < 2) wait_vm<13>(); else wait_vm<12>();
    raw_barrier();
    stage(kt + K2_NB - 1);
    compute(kt);
  }
  glds16(tT + (size_t)(d0 + 8 * w + (lane >> 3)) * S_ + row0 + (lane & 7) * 8, smem + K2_TT + w * 1024);
  glds16(x + (size_t)(row0 + 8 * w + (lane >> 3)) * D_ + d0 + (lane & 7) * 8, smem + K2_X + w * 1024);
  glds16(stats + row0 + 2 * (lane & 31), smem + K2_ST);
  static_for<K2_NT - K2_NB + 1, K2_NT>([&](auto KT) {
    constexpr int kt = KT.value;
    wait_vm<2 * (K2_NT - 1 - kt) + 3>();
    raw_barrier();
    compute(kt);
  });
  STAMP(12);
  wait_vm<0>();
  raw_barrier();
  const int lrow = 32 * wm + 16 * kg + r;
  const u32x4 xv = *(const u32x4*)(smem + K2_X + lrow * 128 + (32 * wn + 16 * (g & 1) + 8 * (g >> 1)) * 2);
  const f32x2 stv = *(const f32x2*)(smem + K2_ST + lrow * 8);
  {
    f32x4* xb = (f32x4*)smem;
    const int wq = w & 3;
#pragma unroll
    for (int T = 0; T < 2; ++T) xb[((kg * 4 + wq) * 2 + T) * 64 + lane] = kg ? acc[T][0] : acc[T][1];
    raw_barrier();
    f32x4 fin[2];
#pragma unroll
    for (int T = 0; T < 2; ++T) {
      const f32x4 o = xb[(((kg ^ 1) * 4 + wq) * 2 + T) * 64 + lane];
      fin[T] = (kg ? acc[T][1] : acc[T][0]) + o;
    }
    unsigned pb[2][2], pt[2][2];
#pragma unroll
    for (int T = 0; T < 2; ++T) {
      pb[T][0] = pk_bf16(fin[T][0], fin[T][1]);
      pb[T][1] = pk_bf16(fin[T][2], fin[T][3]);
      const s16x4 tv = tr_read(smem + K2_TT + (32 * wn + 16 * T + 4 * g + q) * 128 + (32 * wm + 16 * kg + 4 * p) * 2);
      const u32x2 tu = __builtin_bit_cast(u32x2, tv);
      pt[T][0] = tu[0];
      pt[T][1] = tu[1];
    }
    auto b0 = __builtin_amdgcn_permlane16_swap(pb[0][0], pb[1][0], false, false);
    auto b1 = __builtin_amdgcn_permlane16_swap(pb[0][1], pb[1][1], false, false);
    auto t0 = __builtin_amdgcn_permlane16_swap(pt[0][0], pt[1][0], false, false);
    auto t1 = __builtin_amdgcn_permlane16_swap(pt[0][1], pt[1][1], false, false);
    const u32x4 bv = {b0[0], b1[0], b0[1], b1[1]};
    const u32x4 tv = {t0[0], t1[0], t0[1], t1[1]};
    u32x4 o;
#pragma unroll
    for (int j = 0; j < 4; ++j) {
      float res[2];
#pragma unroll
      for (int hh = 0; hh < 2; ++hh) {
        const float xe = hh ? bf_hi(xv[j]) : bf_lo(xv[j]);
        const float te = hh ? bf_hi(tv[j]) : bf_lo(tv[j]);
        const float be = hh ? bf_hi(bv[j]) : bf_lo(bv[j]);
        const float xn = round_bf(round_bf(xe - stv[0]) * stv[1]);
        const float a = round_bf(xn * te);
        res[hh] = round_bf(xe + a) + be;
      }
      o[j] = pk_bf16(res[0], res[1]);
    }
    *(u32x4*)(out + (size_t)erow * D_ + edoff) = o;
  }
  STAMP(15);
  STAMP_FLUSH(1);
}

extern "C" void kernel_launch(void* const* d_in, const int* in_sizes, int n_in, void* d_out, int out_size,
                              void* d_ws, size_t ws_size, hipStream_t stream) {
  const u16* q = (const u16*)d_in[0];
  const u16* k = (const u16*)d_in[1];
  const u16* v = (const u16*)d_in[2];
  const u16* shk = (const u16*)d_in[3];
  const u16* ssmk = (const u16*)d_in[4];
  const u16* ssmd = (const u16*)d_in[5];
  const u16* wg = (const u16*)d_in[7];
  const u16* wu = (const u16*)d_in[8];
  const u16* wd = (const u16*)d_in[9];
  char* ws = (char*)d_ws;
  u16* hbuf = (u16*)(ws + WS_H);
  u16* tT = (u16*)(ws + WS_T);
  float2* stats = (float2*)(ws + WS_ST);
  k1_kernel<<<256, 512, 0, stream>>>(q, k, v, shk, ssmk, ssmd, wg, wu, wd, hbuf, tT, stats);
  k2_kernel<<<256, 512, 0, stream>>>(q, hbuf, wd, tT, stats, (u16*)d_out);
}
```

```cpp
#include <hip/hip_runtime.h>
#include <hip/hip_bf16.h>
#include <stdint.h>

typedef __attribute__((ext_vector_type(8))) short bf16x8;
typedef __attribute__((ext_vector_type(4))) short s16x4;
typedef __attribute__((ext_vector_type(2))) __bf16 bf16x2;
typedef __attribute__((ext_vector_type(4))) float f32x4;
typedef __attribute__((ext_vector_type(2))) float f32x2;
typedef __attribute__((ext_vector_type(4))) unsigned u32x4;
typedef __attribute__((ext_vector_type(2))) unsigned u32x2;
typedef __attribute__((address_space(3))) s16x4* lds_s16x4_ptr;
typedef unsigned short u16;

#define DI __device__ __forceinline__

constexpr int S_ = 2048, D_ = 512, F_ = 1376, FP_ = 1408;

constexpr size_t WS_H = 0;
constexpr size_t WS_T = 6u * 1024 * 1024;
constexpr size_t WS_ST = 8u * 1024 * 1024 + 512 * 1024;

template <int N> DI void wait_vm() { asm volatile("s_waitcnt vmcnt(%0)" ::"i"(N) : "memory"); }
DI void wait_lgkm0() { asm volatile("s_waitcnt lgkmcnt(0)" ::: "memory"); }
DI void raw_barrier() {
  asm volatile("s_waitcnt lgkmcnt(0)" ::: "memory");
  __builtin_amdgcn_s_barrier();
  asm volatile("" ::: "memory");
}

DI unsigned pk_bf16(float lo, float hi) {
  f32x2 f = {lo, hi};
  return __builtin_bit_cast(unsigned, __builtin_convertvector(f, bf16x2));
}
DI float bf_lo(unsigned v) { return __uint_as_float(v << 16); }
DI float bf_hi(unsigned v) { return __uint_as_float(v & 0xffff0000u); }
DI float round_bf(float x) { return bf_lo(pk_bf16(x, x)); }
DI float dot2bf(unsigned a, unsigned b, float c) {
  return __builtin_amdgcn_fdot2_f32_bf16(__builtin_bit_cast(bf16x2, a), __builtin_bit_cast(bf16x2, b), c, false);
}
DI s16x4 tr_read(const char* p) {
  return __builtin_amdgcn_ds_read_tr16_b64_v4i16((lds_s16x4_ptr)p);
}
DI bf16x8 cat8(s16x4 lo, s16x4 hi) { return __builtin_shufflevector(lo, hi, 0, 1, 2, 3, 4, 5, 6, 7); }
DI f32x4 mfma16(bf16x8 a, bf16x8 b, f32x4 c) { return __builtin_amdgcn_mfma_f32_16x16x32_bf16(a, b, c, 0, 0, 0); }

DI void glds16(const void* g, char* lds) {
  unsigned keep;
  const unsigned dst = __builtin_amdgcn_readfirstlane((unsigned)(size_t)lds);
  asm volatile("s_mov_b32 %0, m0\n\ts_mov_b32 m0, %2\n\ts_nop 0\n\tglobal_load_lds_dwordx4 %1, off\n\ts_mov_b32 m0, %0"
               : "=&s"(keep) : "v"(g), "s"(dst) : "memory");
}
DI void touch(const void* g, char* dummy) {
  unsigned keep;
  const unsigned dst = __builtin_amdgcn_readfirstlane((unsigned)(size_t)dummy);
  asm volatile("s_mov_b32 %0, m0\n\ts_mov_b32 m0, %2\n\ts_nop 0\n\tglobal_load_lds_dword %1, off\n\ts_mov_b32 m0, %0"
               : "=&s"(keep) : "v"(g), "s"(dst) : "memory");
}

#define STAMP_DECL do {} while (0)
#define STAMP(I) do {} while (0)
#define STAMP_FLUSH(K) do {} while (0)
#define STAMP_ARG
#define STAMP_PASS

template <int I, int N, class Fn> DI void static_for(Fn&& fn) {
  if constexpr (I < N) {
    fn(std::integral_constant<int, I>{});
    static_for<I + 1, N>(fn);
  }
}

constexpr int K1_STAGE = 49152;
constexpr int K1_DUMMY = 3 * K1_STAGE;
constexpr int K1_LDS = 3 * K1_STAGE + 256;

constexpr int US_PITCH = 4160;
constexpr int HQ_PITCH = 4576;
constexpr int FB_PITCH = 5120;
constexpr int CV_US = 0;
constexpr int CV_HQ = 8 * US_PITCH;
constexpr int CV_FB = CV_HQ + 8 * HQ_PITCH;

DI void gemm1_block(char* smem, const u16* __restrict__ x, const u16* __restrict__ wg, const u16* __restrict__ wu,
                    u16* __restrict__ hbuf, float2* __restrict__ stats, int mt, int ft STAMP_ARG) {
  const int tid = threadIdx.x, lane = tid & 63;
  const int w = __builtin_amdgcn_readfirstlane(tid >> 6);
  const int wm = w >> 2, wn = w & 3;
  const int r = lane & 15, g = lane >> 4, q = (lane & 15) >> 2, p = lane & 3;
  const int row0 = mt * 128, f0 = ft * 128;

  const u16* asrc[2];
  const u16* gsrc[2];
  const u16* usrc[2];
#pragma unroll
  for (int e = 0; e < 2; ++e) {
    const int i = 2 * w + e;
    const int row = 8 * i + (lane >> 3);
    asrc[e] = x + (size_t)(row0 + row) * D_ + (((lane & 7) ^ (row & 7)) << 3);
    const int kr = 4 * i + (lane >> 4);
    const int phi = (kr & 3) | (((kr >> 3) & 1) << 2);
    const int sc = ((((lane & 15) >> 1) ^ phi) << 1) | (lane & 1);
    int col = f0 + sc * 8;
    col = col > F_ - 8 ? F_ - 8 : col;
    gsrc[e] = wg + kr * F_ + col;
    usrc[e] = wu + kr * F_ + col;
  }
  auto stage = [&](int kt, int buf) {
    char* sb = smem + buf * K1_STAGE;
#pragma unroll
    for (int e = 0; e < 2; ++e) glds16(asrc[e] + kt * 64, sb + (2 * w + e) * 1024);
#pragma unroll
    for (int e = 0; e < 2; ++e) glds16(gsrc[e] + kt * 64 * F_, sb + 16384 + (2 * w + e) * 1024);
#pragma unroll
    for (int e = 0; e < 2; ++e) glds16(usrc[e] + kt * 64 * F_, sb + 32768 + (2 * w + e) * 1024);
  };

  stage(0, 0);
  stage(1, 1);
  stage(2, 2);
  {
    const int kr = 64 * (mt & 7) + (tid >> 3) % 64;
    const int sel = tid & 7;
    int col = f0 + (sel & 1) * 64;
    col = col > F_ - 2 ? F_ - 2 : col;
    const u16* pw = ((sel & 2) ? wu : wg) + kr * F_ + col;
    const int t2 = (tid >> 3) * 4 + (tid & 3);
    const int xr = 3 * (t2 >> 3) + (ft % 3);
    const u16* px = x + (size_t)(row0 + xr) * D_ + (t2 & 7) * 64;
    touch((sel & 4) ? px : pw, smem + K1_DUMMY);
    const int xr2 = 96 + 3 * ((tid >> 3) % 11) + (ft % 3);
    const u16* px2 = x + (size_t)(row0 + (xr2 > 127 ? 127 : xr2)) * D_ + (tid & 7) * 64;
    touch(px2, smem + K1_DUMMY);
  }

  int aoff[4][2];
#pragma unroll
  for (int i = 0; i < 4; ++i)
#pragma unroll
    for (int ks = 0; ks < 2; ++ks)
      aoff[i][ks] = (64 * wm + 16 * (i ^ wn) + r) * 128 + (((4 * ks + g) ^ (r & 7)) << 4);
  const int phiL = q | ((g & 1) << 2);
  int boff[4];
#pragma unroll
  for (int T = 0; T < 4; ++T) {
    const int mat = (T >> 1) ^ wm;
    boff[T] = 16384 + mat * 16384 + (8 * g + q) * 256 + (((2 * wn + (T & 1)) ^ phiL) << 5) + p * 8;
  }

  f32x4 acc[4][4];
#pragma unroll
  for (int T = 0; T < 4; ++T)
#pragma unroll
    for (int i = 0; i < 4; ++i) acc[T][i] = f32x4{0.f, 0.f, 0.f, 0.f};
  float s1 = 0.f, s2 = 0.f, c0 = 0.f, c1 = 0.f;
  const unsigned ones = 0x3f803f80u;

  bf16x8 xa[4], wa[4], xb[4], wb[4];
  auto read_frags = [&](const char* sb, int ks, bf16x8 (&xf)[4], bf16x8 (&wf)[4]) {
#pragma unroll
    for (int T = 0; T < 4; ++T) {
      const char* a = sb + boff[T] + ks * 8192;
      wf[T] = cat8(tr_read(a), tr_read(a + 1024));
    }
#pragma unroll
    for (int i = 0; i < 4; ++i) xf[i] = *(const bf16x8*)(sb + aoff[i][ks]);
  };
  auto stat_part = [&](const bf16x8 (&xf)[4], const bf16x8 (&wf)[4], int j) {
    const u32x4 xv = __builtin_bit_cast(u32x4, xf[0]);
    const u32x4 w0 = __builtin_bit_cast(u32x4, wf[0]);
    const u32x4 w1 = __builtin_bit_cast(u32x4, wf[1]);
    s1 = dot2bf(xv[j], ones, s1);
    s2 = dot2bf(xv[j], xv[j], s2);
    c0 = dot2bf(w0[j], ones, c0);
    c1 = dot2bf(w1[j], ones, c1);
  };

  wait_vm<14>();
  raw_barrier();
  STAMP(1);
  read_frags(smem, 0, xa, wa);
  int buf = 0;
#pragma unroll 1
  for (int kt = 0; kt < 8; ++kt) {
    char* sb = smem + buf * K1_STAGE;
    read_frags(sb, 1, xb, wb);
    __builtin_amdgcn_sched_barrier(0);
#pragma unroll
    for (int T = 0; T < 4; ++T) {
#pragma unroll
      for (int i = 0; i < 4; ++i) acc[T][i] = mfma16(wa[T], xa[i], acc[T][i]);
      stat_part(xa, wa, T);
    }
    __builtin_amdgcn_sched_barrier(0);
    buf = buf == 2 ? 0 : buf + 1;
    if (kt < 7) {
      if (kt < 2) wait_vm<8>(); else if (kt < 6) wait_vm<6>(); else wait_vm<0>();
      raw_barrier();
      read_frags(smem + buf * K1_STAGE, 0, xa, wa);
    }
    __builtin_amdgcn_sched_barrier(0);
    const bool more = kt < 5;
#pragma unroll
    for (int T = 0; T < 4; ++T) {
#pragma unroll
      for (int i = 0; i < 4; ++i) acc[T][i] = mfma16(wb[T], xb[i], acc[T][i]);
      stat_part(xb, wb, T);
      if (more) {
        if (T == 0) { glds16(asrc[0] + (kt + 3) * 64, sb + (2 * w) * 1024); glds16(asrc[1] + (kt + 3) * 64, sb + (2 * w + 1) * 1024); }
        if (T == 1) { glds16(gsrc[0] + (kt + 3) * 64 * F_, sb + 16384 + (2 * w) * 1024); glds16(gsrc[1] + (kt + 3) * 64 * F_, sb + 16384 + (2 * w + 1) * 1024); }
        if (T == 2) { glds16(usrc[0] + (kt + 3) * 64 * F_, sb + 32768 + (2 * w) * 1024); glds16(usrc[1] + (kt + 3) * 64 * F_, sb + 32768 + (2 * w + 1) * 1024); }
      }
      __builtin_amdgcn_sched_barrier(0);
    }
  }
  __syncthreads();
  STAMP(9);
  float2* st_s = (float2*)smem;
  float* cs_s = (float*)(smem + 1024);
  s1 += __shfl_xor(s1, 16); s1 += __shfl_xor(s1, 32);
  s2 += __shfl_xor(s2, 16); s2 += __shfl_xor(s2, 32);
  c0 += __shfl_xor(c0, 16); c0 += __shfl_xor(c0, 32);
  c1 += __shfl_xor(c1, 16); c1 += __shfl_xor(c1, 32);
  if (g == 0) {
    const float mean = s1 * (1.f / D_);
    float var = s2 * (1.f / D_) - mean * mean;
    var = var < 0.f ? 0.f : var;
    const float mb = round_bf(mean);
    const float vb = round_bf(var);
    const float sd = round_bf(sqrtf(round_bf(vb + 1e-5f)));
    const float2 stv = make_float2(mb, 1.f / sd);
    const int row = 64 * wm + 16 * wn + r;
    st_s[row] = stv;
    if (ft == 0) stats[row0 + row] = stv;
    cs_s[wm * 128 + 32 * wn + r] = c0;
    cs_s[wm * 128 + 32 * wn + 16 + r] = c1;
  }
  __syncthreads();
  const bool valid = (f0 + 32 * wn) < F_;
  f32x4 ca[2], cb[2];
#pragma unroll
  for (int T = 0; T < 2; ++T) {
    ca[T] = *(const f32x4*)(cs_s + wm * 128 + 32 * wn + 16 * T + 4 * g);
    cb[T] = *(const f32x4*)(cs_s + (wm ^ 1) * 128 + 32 * wn + 16 * T + 4 * g);
  }
#pragma unroll
  for (int i = 0; i < 4; ++i) {
    const int row = 64 * wm + 16 * (i ^ wn) + r;
    const float2 stv = st_s[row];
    unsigned pk[2][2];
#pragma unroll
    for (int T = 0; T < 2; ++T) {
      float hv[4];
#pragma unroll
      for (int j = 0; j < 4; ++j) {
        const float ga = round_bf(stv.y * (acc[T][i][j] - stv.x * ca[T][j]));
        const float gb = round_bf(stv.y * (acc[2 + T][i][j] - stv.x * cb[T][j]));
        hv[j] = ga * gb;
      }
      pk[T][0] = pk_bf16(hv[0], hv[1]);
      pk[T][1] = pk_bf16(hv[2], hv[3]);
    }
    auto r0 = __builtin_amdgcn_permlane16_swap(pk[0][0], pk[1][0], false, false);
    auto r1 = __builtin_amdgcn_permlane16_swap(pk[0][1], pk[1][1], false, false);
    u32x4 o = {r0[0], r1[0], r0[1], r1[1]};
    if (!valid) o = u32x4{0u, 0u, 0u, 0u};
    *(u32x4*)(hbuf + (size_t)(row0 + row) * FP_ + f0 + 32 * wn + 16 * (g & 1) + 8 * (g >> 1)) = o;
  }
}

DI void conv_block(char* smem, const u16* __restrict__ kin, const u16* __restrict__ vin,
                   const u16* __restrict__ shk, const u16* __restrict__ ssmk, const u16* __restrict__ ssmd,
                   u16* __restrict__ tT, int cg STAMP_ARG) {
  const int tid = threadIdx.x, lane = tid & 63;
  const int w = __builtin_amdgcn_readfirstlane(tid >> 6);
  const int r = lane & 15, g = lane >> 4;
  const int c0 = cg * 8, ch = c0 + w;

  u32x4 hx[4];
#pragma unroll
  for (int i = 0; i < 4; ++i) hx[i] = *(const u32x4*)(ssmk + (size_t)ch * S_ + (i * 64 + lane) * 8);
  touch(((tid & 256) ? vin : kin) + (size_t)(256 * (cg & 7) + (tid & 255)) * D_ + (cg >> 3) * 64, smem + K1_DUMMY);
  u32x4 kx[4], vx[4], hal[4][3];
#pragma unroll
  for (int j = 0; j < 4; ++j) {
    const int s = 512 * j + tid;
    kx[j] = *(const u32x4*)(kin + (size_t)s * D_ + c0);
    vx[j] = *(const u32x4*)(vin + (size_t)s * D_ + c0);
#pragma unroll
    for (int d = 0; d < 3; ++d) {
      const int sr = 512 * j + 64 * w - 1 - d;
      hal[j][d] = *(const u32x4*)(kin + (size_t)(sr < 0 ? 0 : sr) * D_ + c0);
      if (j == 0 && sr < 0) hal[j][d] = u32x4{0u, 0u, 0u, 0u};
    }
  }
  float wsh[8][4];
#pragma unroll
  for (int c = 0; c < 8; ++c) {
    const u32x2 t = *(const u32x2*)(shk + (c0 + c) * 4);
    wsh[c][0] = bf_lo(t[0]); wsh[c][1] = bf_hi(t[0]); wsh[c][2] = bf_lo(t[1]); wsh[c][3] = bf_hi(t[1]);
  }
  const float dch = __uint_as_float((unsigned)ssmd[ch] << 16);

  char* us = smem + CV_US;
  char* hq = smem + CV_HQ + w * HQ_PITCH;
  char* fb = smem + CV_FB + w * FB_PITCH;
  if (lane < 30) *(u32x4*)(hq + lane * 16) = u32x4{0u, 0u, 0u, 0u};
  if (lane < 4) *(u32x4*)(us + w * US_PITCH + lane * 16) = u32x4{0u, 0u, 0u, 0u};
#pragma unroll
  for (int i = 0; i < 4; ++i) *(u32x4*)(hq + 480 + (i * 64 + lane) * 16) = hx[i];

  auto make_u = [&](int j) {
    const int s = 512 * j + tid;
    u32x4 k1, k2, k3;
#pragma unroll
    for (int d = 0; d < 4; ++d) {
      k1[d] = (unsigned)__builtin_amdgcn_update_dpp((int)hal[j][0][d], (int)kx[j][d], 0x138, 0xf, 0xf, false);
      k2[d] = (unsigned)__builtin_amdgcn_update_dpp((int)hal[j][1][d], (int)k1[d], 0x138, 0xf, 0xf, false);
      k3[d] = (unsigned)__builtin_amdgcn_update_dpp((int)hal[j][2][d], (int)k2[d], 0x138, 0xf, 0xf, false);
    }
#pragma unroll
    for (int d = 0; d < 4; ++d) {
      const int ca = 2 * d, cb = 2 * d + 1;
      float a = bf_lo(k3[d]) * wsh[ca][3];
      a = fmaf(bf_lo(k2[d]), wsh[ca][2], a);
      a = fmaf(bf_lo(k1[d]), wsh[ca][1], a);
      a = fmaf(bf_lo(kx[j][d]), wsh[ca][0], a);
      float b = bf_hi(k3[d]) * wsh[cb][3];
      b = fmaf(bf_hi(k2[d]), wsh[cb][2], b);
      b = fmaf(bf_hi(k1[d]), wsh[cb][1], b);
      b = fmaf(bf_hi(kx[j][d]), wsh[cb][0], b);
      const unsigned pk = pk_bf16(a * bf_lo(vx[j][d]), b * bf_hi(vx[j][d]));
      *(u16*)(us + ca * US_PITCH + 64 + 2 * s) = (u16)(pk & 0xffffu);
      *(u16*)(us + cb * US_PITCH + 64 + 2 * s) = (u16)(pk >> 16);
    }
  };

  f32x4 acc[8];
#pragma unroll
  for (int n = 0; n < 8; ++n) acc[n] = f32x4{0.f, 0.f, 0.f, 0.f};

  const char* usw = us + w * US_PITCH;
  const char* a_lane = fb + (32 + r - 8 * g) * 8;
  const char* b_lane = hq + 448 + 32 * r + 16 * g;

  make_u(0);
  __syncthreads();
  static_for<0, 4>([&](auto J) {
    constexpr int j = J.value;
    constexpr int NNU = 8 - 2 * j;
    STAMP(1 + j);
    {
      unsigned d0[5], d1[5], d2[5];
#pragma unroll
      for (int it = 0; it < 5; ++it) {
        const unsigned* src = (const unsigned*)(usw + 2 * (512 * j + 16 + 2 * (it * 64 + lane)));
        d2[it] = src[0]; d1[it] = src[-1]; d0[it] = src[-2];
      }
#pragma unroll
      for (int it = 0; it < 5; ++it) {
        u32x4 e;
        e[0] = (d2[it] & 0xffffu) | (d1[it] & 0xffff0000u);
        e[1] = (d1[it] & 0xffffu) | (d0[it] & 0xffff0000u);
        e[2] = (d2[it] >> 16) | (d2[it] << 16);
        e[3] = (d1[it] >> 16) | (d1[it] << 16);
        *(u32x4*)(fb + (it * 64 + lane) * 16) = e;
      }
    }
    if constexpr (j < 3) make_u(j + 1);
    wait_lgkm0();
    bf16x8 af[2][2], bfr[2][NNU];
    auto load_set = [&](int set, int k0) {
#pragma unroll
      for (int kk = 0; kk < 2; ++kk) {
        const char* a = a_lane + (8 * kk + k0) * 256;
        af[set][kk] = cat8(*(const s16x4*)a, *(const s16x4*)(a - 32));
      }
#pragma unroll
      for (int nu = 0; nu < NNU; ++nu) bfr[set][nu] = *(const bf16x8*)(b_lane + 512 * nu - 64 * k0);
    };
    auto mma_set = [&](int set) {
#pragma unroll
      for (int nu = 0; nu < NNU; ++nu) {
        acc[2 * j + nu] = mfma16(af[set][0], bfr[set][nu], acc[2 * j + nu]);
        if (2 * j + 1 + nu < 8) acc[2 * j + 1 + nu] = mfma16(af[set][1], bfr[set][nu], acc[2 * j + 1 + nu]);
      }
    };
    load_set(0, 0);
#pragma unroll 1
    for (int k0 = 0; k0 < 8; k0 += 2) {
      load_set(1, k0 + 1);
      mma_set(0);
      load_set(0, (k0 + 2) & 7);
      mma_set(1);
    }
    if constexpr (j < 3) __syncthreads();
  });
  STAMP(9);
#pragma unroll
  for (int n = 0; n < 8; ++n) {
    const int s = 256 * n + 16 * r + 4 * g;
    const u32x2 uu = *(const u32x2*)(usw + 64 + 2 * s);
    float t[4];
    t[0] = round_bf(acc[n][0]) + round_bf(bf_lo(uu[0]) * dch);
    t[1] = round_bf(acc[n][1]) + round_bf(bf_hi(uu[0]) * dch);
    t[2] = round_bf(acc[n][2]) + round_bf(bf_lo(uu[1]) * dch);
    t[3] = round_bf(acc[n][3]) + round_bf(bf_hi(uu[1]) * dch);
    u32x2 o = {pk_bf16(t[0], t[1]), pk_bf16(t[2], t[3])};
    *(u32x2*)(tT + (size_t)ch * S_ + s) = o;
  }
}

__global__ __launch_bounds__(512, 2) void k1_kernel(const u16* __restrict__ x, const u16* __restrict__ kin,
                                                     const u16* __restrict__ vin, const u16* __restrict__ shk,
                                                     const u16* __restrict__ ssmk, const u16* __restrict__ ssmd,
                                                     const u16* __restrict__ wg, const u16* __restrict__ wu,
                                                     const u16* __restrict__ wd,
                                                     u16* __restrict__ hbuf, u16* __restrict__ tT,
                                                     float2* __restrict__ stats) {
  __shared__ __attribute__((aligned(16))) char smem[K1_LDS];
  const int b = blockIdx.x, xcd = b & 7, idx = b >> 3;
  STAMP_DECL;
  STAMP(0);
  if (idx < 8) {
    conv_block(smem, kin, vin, shk, ssmk, ssmd, tT, xcd * 8 + idx STAMP_PASS);
  } else if (idx < 30) {
    const int t = 22 * (xcd & 3) + (idx - 8);
    gemm1_block(smem, x, wg, wu, hbuf, stats, 8 * (xcd >> 2) + (t & 7), t >> 3 STAMP_PASS);
  } else {
    const int part = (idx - 30) * 8 + xcd;
    for (int i = threadIdx.x; i < 688; i += 512) touch(wd + (size_t)(part * 688 + i) * 64, smem + K1_DUMMY);
    wait_vm<0>();
  }
  STAMP(15);
  STAMP_FLUSH(0);
}

constexpr int K2_NB = 4;
constexpr int K2_STAGE = 32768;
constexpr int K2_TT = K2_NB * K2_STAGE;
constexpr int K2_X = K2_TT + 8192;
constexpr int K2_ST = K2_X + 8192;
constexpr int K2_DUMMY = K2_ST + 1024;
constexpr int K2_LDS = K2_DUMMY + 256;
constexpr int K2_NT = FP_ / 128;

__global__ __launch_bounds__(512, 2) void k2_kernel(const u16* __restrict__ x, const u16* __restrict__ hbuf,
                                                     const u16* __restrict__ wd, const u16* __restrict__ tT,
                                                     const float2* __restrict__ stats, u16* __restrict__ out) {
  __shared__ __attribute__((aligned(16))) char smem[K2_LDS];
  const int tid = threadIdx.x, lane = tid & 63;
  const int w = __builtin_amdgcn_readfirstlane(tid >> 6);
  const int kg = w >> 2, wm = (w >> 1) & 1, wn = w & 1;
  const int r = lane & 15, g = lane >> 4, q = (lane & 15) >> 2, p = lane & 3;
  const int b = blockIdx.x, xcd = b & 7, idx = b >> 3;
  const int row0 = (4 * xcd + (idx >> 3)) * 64, d0 = (idx & 7) * 64;
  STAMP_DECL;
  STAMP(0);

  const u16* asrc[2];
  int wrow[2], wcol[2];
#pragma unroll
  for (int e = 0; e < 2; ++e) {
    const int ar = 8 * w + 4 * e + (lane >> 4);
    asrc[e] = hbuf + (size_t)(row0 + ar) * FP_ + (((lane & 15) ^ (ar & 15)) << 3);
    const int kr = 16 * w + 8 * e + (lane >> 3);
    const int phi_s = ((kr >> 1) & 1) | (((kr >> 3) & 1) << 1);
    wrow[e] = kr;
    wcol[e] = d0 + ((((((lane & 7) >> 1) ^ phi_s) << 1) | (lane & 1)) << 3);
  }
  auto stage = [&](int kt) {
    char* sb = smem + (kt & (K2_NB - 1)) * K2_STAGE;
#pragma unroll
    for (int e = 0; e < 2; ++e) glds16(asrc[e] + kt * 128, sb + (2 * w + e) * 1024);
#pragma unroll
    for (int e = 0; e < 2; ++e) {
      int kr = kt * 128 + wrow[e];
      kr = kr > F_ - 1 ? F_ - 1 : kr;
      glds16(wd + (size_t)kr * D_ + wcol[e], sb + 16384 + (2 * w + e) * 1024);
    }
  };
  stage(0);
  {
    const bool ish = tid < 176;
    const int hr = tid / 22, hl = tid % 22;
    int kr = 344 * (idx >> 3) + (tid - 176);
    kr = kr > F_ - 1 ? F_ - 1 : kr;
    const u16* ph = hbuf + (size_t)(row0 + 8 * (idx & 7) + (hr & 7)) * FP_ + hl * 64;
    const u16* pw = wd + (size_t)kr * D_ + d0;
    touch(ish ? ph : pw, smem + K2_DUMMY);
  }
  stage(1);
  stage(2);

  const int erow = row0 + 32 * wm + 16 * kg + r;
  const int edoff = d0 + 32 * wn + 16 * (g & 1) + 8 * (g >> 1);

  int aoff[2][2], boff[2];
#pragma unroll
  for (int i = 0; i < 2; ++i)
#pragma unroll
    for (int hs = 0; hs < 2; ++hs)
      aoff[i][hs] = (32 * wm + 16 * i + r) * 256 + (((4 * (kg + 2 * hs) + g) ^ r) << 4);
  const int phiL = ((q >> 1) & 1) | ((g & 1) << 1);
#pragma unroll
  for (int T = 0; T < 2; ++T)
    boff[T] = 16384 + (32 * kg + 8 * g + q) * 128 + (((2 * wn + T) ^ phiL) << 5) + p * 8;

  f32x4 acc[2][2];
#pragma unroll
  for (int T = 0; T < 2; ++T)
#pragma unroll
    for (int i = 0; i < 2; ++i) acc[T][i] = f32x4{0.f, 0.f, 0.f, 0.f};

  bf16x8 hA[2][2], wA[2][2], hB[2][2], wB[2][2];
  auto read_set = [&](int kt, bf16x8 (&hf)[2][2], bf16x8 (&wf)[2][2]) {
    const char* sb = smem + (kt & (K2_NB - 1)) * K2_STAGE;
#pragma unroll
    for (int hs = 0; hs < 2; ++hs) {
#pragma unroll
      for (int T = 0; T < 2; ++T) wf[hs][T] = cat8(tr_read(sb + boff[T] + hs * 8192), tr_read(sb + boff[T] + hs * 8192 + 512));
#pragma unroll
      for (int i = 0; i < 2; ++i) hf[hs][i] = *(const bf16x8*)(sb + aoff[i][hs]);
    }
  };
  auto mma_set = [&](const bf16x8 (&hf)[2][2], const bf16x8 (&wf)[2][2]) {
#pragma unroll
    for (int hs = 0; hs < 2; ++hs)
#pragma unroll
      for (int T = 0; T < 2; ++T)
#pragma unroll
        for (int i = 0; i < 2; ++i) acc[T][i] = mfma16(wf[hs][T], hf[hs][i], acc[T][i]);
  };
  wait_vm<9>();
  raw_barrier();
  STAMP(1);
  read_set(0, hA, wA);
  stage(3);
#pragma unroll 1
  for (int kt = 1; kt < 9; kt += 2) {
    wait_vm<8>();
    raw_barrier();
    read_set(kt, hB, wB);
    __builtin_amdgcn_sched_barrier(0);
    mma_set(hA, wA);
    stage(kt + 3);
    __builtin_amdgcn_sched_barrier(0);
    wait_vm<8>();
    raw_barrier();
    read_set(kt + 1, hA, wA);
    __builtin_amdgcn_sched_barrier(0);
    mma_set(hB, wB);
    if (kt < 7) stage(kt + 4);
    __builtin_amdgcn_sched_barrier(0);
  }
  glds16(tT + (size_t)(d0 + 8 * w + (lane >> 3)) * S_ + row0 + (lane & 7) * 8, smem + K2_TT + w * 1024);
  glds16(x + (size_t)(row0 + 8 * w + (lane >> 3)) * D_ + d0 + (lane & 7) * 8, smem + K2_X + w * 1024);
  glds16(stats + row0 + 2 * (lane & 31), smem + K2_ST);
  wait_vm<7>();
  raw_barrier();
  STAMP(10);
  read_set(9, hB, wB);
  __builtin_amdgcn_sched_barrier(0);
  mma_set(hA, wA);
  __builtin_amdgcn_sched_barrier(0);
  wait_vm<3>();
  raw_barrier();
  STAMP(11);
  read_set(10, hA, wA);
  __builtin_amdgcn_sched_barrier(0);
  mma_set(hB, wB);
  __builtin_amdgcn_sched_barrier(0);
  mma_set(hA, wA);
  STAMP(12);
  wait_vm<0>();
  raw_barrier();
  const int lrow = 32 * wm + 16 * kg + r;
  const u32x4 xv = *(const u32x4*)(smem + K2_X + lrow * 128 + (32 * wn + 16 * (g & 1) + 8 * (g >> 1)) * 2);
  const f32x2 stv = *(const f32x2*)(smem + K2_ST + lrow * 8);
  {
    f32x4* xb = (f32x4*)smem;
    const int wq = w & 3;
#pragma unroll
    for (int T = 0; T < 2; ++T) xb[((kg * 4 + wq) * 2 + T) * 64 + lane] = kg ? acc[T][0] : acc[T][1];
    raw_barrier();
    f32x4 fin[2];
#pragma unroll
    for (int T = 0; T < 2; ++T) {
      const f32x4 o = xb[(((kg ^ 1) * 4 + wq) * 2 + T) * 64 + lane];
      fin[T] = (kg ? acc[T][1] : acc[T][0]) + o;
    }
    unsigned pb[2][2], pt[2][2];
#pragma unroll
    for (int T = 0; T < 2; ++T) {
      pb[T][0] = pk_bf16(fin[T][0], fin[T][1]);
      pb[T][1] = pk_bf16(fin[T][2], fin[T][3]);
      const s16x4 tv = tr_read(smem + K2_TT + (32 * wn + 16 * T + 4 * g + q) * 128 + (32 * wm + 16 * kg + 4 * p) * 2);
      const u32x2 tu = __builtin_bit_cast(u32x2, tv);
      pt[T][0] = tu[0];
      pt[T][1] = tu[1];
    }
    auto b0 = __builtin_amdgcn_permlane16_swap(pb[0][0], pb[1][0], false, false);
    auto b1 = __builtin_amdgcn_permlane16_swap(pb[0][1], pb[1][1], false, false);
    auto t0 = __builtin_amdgcn_permlane16_swap(pt[0][0], pt[1][0], false, false);
    auto t1 = __builtin_amdgcn_permlane16_swap(pt[0][1], pt[1][1], false, false);
    const u32x4 bv = {b0[0], b1[0], b0[1], b1[1]};
    const u32x4 tv = {t0[0], t1[0], t0[1], t1[1]};
    u32x4 o;
#pragma unroll
    for (int j = 0; j < 4; ++j) {
      float res[2];
#pragma unroll
      for (int hh = 0; hh < 2; ++hh) {
        const float xe = hh ? bf_hi(xv[j]) : bf_lo(xv[j]);
        const float te = hh ? bf_hi(tv[j]) : bf_lo(tv[j]);
        const float be = hh ? bf_hi(bv[j]) : bf_lo(bv[j]);
        const float xn = round_bf(round_bf(xe - stv[0]) * stv[1]);
        const float a = round_bf(xn * te);
        res[hh] = round_bf(xe + a) + be;
      }
      o[j] = pk_bf16(res[0], res[1]);
    }
    *(u32x4*)(out + (size_t)erow * D_ + edoff) = o;
  }
  STAMP(15);
  STAMP_FLUSH(1);
}

extern "C" void kernel_launch(void* const* d_in, const int* in_sizes, int n_in, void* d_out, int out_size,
                              void* d_ws, size_t ws_size, hipStream_t stream) {
  const u16* q = (const u16*)d_in[0];
  const u16* k = (const u16*)d_in[1];
  const u16* v = (const u16*)d_in[2];
  const u16* shk = (const u16*)d_in[3];
  const u16* ssmk = (const u16*)d_in[4];
  const u16* ssmd = (const u16*)d_in[5];
  const u16* wg = (const u16*)d_in[7];
  const u16* wu = (const u16*)d_in[8];
  const u16* wd = (const u16*)d_in[9];
  char* ws = (char*)d_ws;
  u16* hbuf = (u16*)(ws + WS_H);
  u16* tT = (u16*)(ws + WS_T);
  float2* stats = (float2*)(ws + WS_ST);
  k1_kernel<<<256, 512, 0, stream>>>(q, k, v, shk, ssmk, ssmd, wg, wu, wd, hbuf, tT, stats);
  k2_kernel<<<256, 512, 0, stream>>>(q, hbuf, wd, tT, stats, (u16*)d_out);
}
```

```cpp
#include <hip/hip_runtime.h>
#include <hip/hip_bf16.h>
#include <stdint.h>

typedef __attribute__((ext_vector_type(8))) short bf16x8;
typedef __attribute__((ext_vector_type(4))) short s16x4;
typedef __attribute__((ext_vector_type(2))) __bf16 bf16x2;
typedef __attribute__((ext_vector_type(4))) float f32x4;
typedef __attribute__((ext_vector_type(2))) float f32x2;
typedef __attribute__((ext_vector_type(4))) unsigned u32x4;
typedef __attribute__((ext_vector_type(2))) unsigned u32x2;
typedef __attribute__((address_space(3))) s16x4* lds_s16x4_ptr;
typedef unsigned short u16;

#define DI __device__ __forceinline__

constexpr int S_ = 2048, D_ = 512, F_ = 1376, FP_ = 1408;

constexpr size_t WS_H = 0;
constexpr size_t WS_T = 6u * 1024 * 1024;
constexpr size_t WS_ST = 8u * 1024 * 1024 + 512 * 1024;

template <int N> DI void wait_vm() { asm volatile("s_waitcnt vmcnt(%0)" ::"i"(N) : "memory"); }
DI void wait_lgkm0() { asm volatile("s_waitcnt lgkmcnt(0)" ::: "memory"); }
DI void raw_barrier() {
  asm volatile("s_waitcnt lgkmcnt(0)" ::: "memory");
  __builtin_amdgcn_s_barrier();
  asm volatile("" ::: "memory");
}

DI unsigned pk_bf16(float lo, float hi) {
  f32x2 f = {lo, hi};
  return __builtin_bit_cast(unsigned, __builtin_convertvector(f, bf16x2));
}
DI float bf_lo(unsigned v) { return __uint_as_float(v << 16); }
DI float bf_hi(unsigned v) { return __uint_as_float(v & 0xffff0000u); }
DI float round_bf(float x) { return bf_lo(pk_bf16(x, x)); }
DI float dot2bf(unsigned a, unsigned b, float c) {
  return __builtin_amdgcn_fdot2_f32_bf16(__builtin_bit_cast(bf16x2, a), __builtin_bit_cast(bf16x2, b), c, false);
}
DI s16x4 tr_read(const char* p) {
  return __builtin_amdgcn_ds_read_tr16_b64_v4i16((lds_s16x4_ptr)p);
}
DI bf16x8 cat8(s16x4 lo, s16x4 hi) { return __builtin_shufflevector(lo, hi, 0, 1, 2, 3, 4, 5, 6, 7); }
DI f32x4 mfma16(bf16x8 a, bf16x8 b, f32x4 c) { return __builtin_amdgcn_mfma_f32_16x16x32_bf16(a, b, c, 0, 0, 0); }

DI void glds16(const void* g, char* lds) {
  unsigned keep;
  const unsigned dst = __builtin_amdgcn_readfirstlane((unsigned)(size_t)lds);
  asm volatile("s_mov_b32 %0, m0\n\ts_mov_b32 m0, %2\n\ts_nop 0\n\tglobal_load_lds_dwordx4 %1, off\n\ts_mov_b32 m0, %0"
               : "=&s"(keep) : "v"(g), "s"(dst) : "memory");
}
DI void touch(const void* g, char* dummy) {
  unsigned keep;
  const unsigned dst = __builtin_amdgcn_readfirstlane((unsigned)(size_t)dummy);
  asm volatile("s_mov_b32 %0, m0\n\ts_mov_b32 m0, %2\n\ts_nop 0\n\tglobal_load_lds_dword %1, off\n\ts_mov_b32 m0, %0"
               : "=&s"(keep) : "v"(g), "s"(dst) : "memory");
}

#define STAMP_DECL do {} while (0)
#define STAMP(I) do {} while (0)
#define STAMP_FLUSH(K) do {} while (0)
#define STAMP_ARG
#define STAMP_PASS

template <int I, int N, class Fn> DI void static_for(Fn&& fn) {
  if constexpr (I < N) {
    fn(std::integral_constant<int, I>{});
    static_for<I + 1, N>(fn);
  }
}

constexpr int K1_STAGE = 49152;
constexpr int K1_DUMMY = 3 * K1_STAGE;
constexpr int K1_LDS = 3 * K1_STAGE + 256;

constexpr int US_PITCH = 4160;
constexpr int HQ_PITCH = 4576;
constexpr int FB_PITCH = 5120;
constexpr int CV_US = 0;
constexpr int CV_HQ = 8 * US_PITCH;
constexpr int CV_FB = CV_HQ + 8 * HQ_PITCH;

DI void gemm1_block(char* smem, const u16* __restrict__ x, const u16* __restrict__ wg, const u16* __restrict__ wu,
                    u16* __restrict__ hbuf, float2* __restrict__ stats, int mt, int ft STAMP_ARG) {
  const int tid = threadIdx.x, lane = tid & 63;
  const int w = __builtin_amdgcn_readfirstlane(tid >> 6);
  const int wm = w >> 2, wn = w & 3;
  const int r = lane & 15, g = lane >> 4, q = (lane & 15) >> 2, p = lane & 3;
  const int row0 = mt * 128, f0 = ft * 128;

  const u16* asrc[2];
  const u16* gsrc[2];
  const u16* usrc[2];
#pragma unroll
  for (int e = 0; e < 2; ++e) {
    const int i = 2 * w + e;
    const int row = 8 * i + (lane >> 3);
    asrc[e] = x + (size_t)(row0 + row) * D_ + (((lane & 7) ^ (row & 7)) << 3);
    const int kr = 4 * i + (lane >> 4);
    const int phi = (kr & 3) | (((kr >> 3) & 1) << 2);
    const int sc = ((((lane & 15) >> 1) ^ phi) << 1) | (lane & 1);
    int col = f0 + sc * 8;
    col = col > F_ - 8 ? F_ - 8 : col;
    gsrc[e] = wg + kr * F_ + col;
    usrc[e] = wu + kr * F_ + col;
  }
  auto stage = [&](int kt, int buf) {
    char* sb = smem + buf * K1_STAGE;
#pragma unroll
    for (int e = 0; e < 2; ++e) glds16(asrc[e] + kt * 64, sb + (2 * w + e) * 1024);
#pragma unroll
    for (int e = 0; e < 2; ++e) glds16(gsrc[e] + kt * 64 * F_, sb + 16384 + (2 * w + e) * 1024);
#pragma unroll
    for (int e = 0; e < 2; ++e) glds16(usrc[e] + kt * 64 * F_, sb + 32768 + (2 * w + e) * 1024);
  };

  stage(0, 0);
  stage(1, 1);
  {
    const int kr = 64 * (mt & 7) + (tid >> 3) % 64;
    const int sel = tid & 7;
    int col = f0 + (sel & 1) * 64;
    col = col > F_ - 2 ? F_ - 2 : col;
    const u16* pw = ((sel & 2) ? wu : wg) + kr * F_ + col;
    const int t2 = (tid >> 3) * 4 + (tid & 3);
    const int xr = 3 * (t2 >> 3) + (ft % 3);
    const u16* px = x + (size_t)(row0 + xr) * D_ + (t2 & 7) * 64;
    touch((sel & 4) ? px : pw, smem + K1_DUMMY);
    const int xr2 = 96 + 3 * ((tid >> 3) % 11) + (ft % 3);
    const u16* px2 = x + (size_t)(row0 + (xr2 > 127 ? 127 : xr2)) * D_ + (tid & 7) * 64;
    touch(px2, smem + K1_DUMMY);
  }
  stage(2, 2);

  int aoff[4][2];
#pragma unroll
  for (int i = 0; i < 4; ++i)
#pragma unroll
    for (int ks = 0; ks < 2; ++ks)
      aoff[i][ks] = (64 * wm + 16 * (i ^ wn) + r) * 128 + (((4 * ks + g) ^ (r & 7)) << 4);
  const int phiL = q | ((g & 1) << 2);
  int boff[4];
#pragma unroll
  for (int T = 0; T < 4; ++T) {
    const int mat = (T >> 1) ^ wm;
    boff[T] = 16384 + mat * 16384 + (8 * g + q) * 256 + (((2 * wn + (T & 1)) ^ phiL) << 5) + p * 8;
  }

  f32x4 acc[4][4];
#pragma unroll
  for (int T = 0; T < 4; ++T)
#pragma unroll
    for (int i = 0; i < 4; ++i) acc[T][i] = f32x4{0.f, 0.f, 0.f, 0.f};
  f32x4 accS = {0.f, 0.f, 0.f, 0.f}, accR = accS, accC[2] = {accS, accS};
  const bf16x8 ones = {0x3f80, 0x3f80, 0x3f80, 0x3f80, 0x3f80, 0x3f80, 0x3f80, 0x3f80};

  bf16x8 xa[4], wa[4], xb[4], wb[4];
  auto read_frags = [&](const char* sb, int ks, bf16x8 (&xf)[4], bf16x8 (&wf)[4]) {
#pragma unroll
    for (int T = 0; T < 4; ++T) {
      const char* a = sb + boff[T] + ks * 8192;
      wf[T] = cat8(tr_read(a), tr_read(a + 1024));
    }
#pragma unroll
    for (int i = 0; i < 4; ++i) xf[i] = *(const bf16x8*)(sb + aoff[i][ks]);
  };
  auto mma_T = [&](const bf16x8 (&xf)[4], const bf16x8 (&wf)[4], int T) {
#pragma unroll
    for (int i = 0; i < 4; ++i) acc[T][i] = mfma16(wf[T], xf[i], acc[T][i]);
    if (T == 0) accS = mfma16(xf[0], xf[0], accS);
    if (T == 1) accR = mfma16(ones, xf[0], accR);
    if (T == 2) accC[0] = mfma16(wf[0], ones, accC[0]);
    if (T == 3) accC[1] = mfma16(wf[1], ones, accC[1]);
  };

  wait_vm<14>();
  raw_barrier();
  STAMP(1);
  read_frags(smem, 0, xa, wa);
  int buf = 0;
#pragma unroll 1
  for (int kt = 0; kt < 7; ++kt) {
    char* sb = smem + buf * K1_STAGE;
    read_frags(sb, 1, xb, wb);
    __builtin_amdgcn_sched_barrier(0);
#pragma unroll
    for (int T = 0; T < 4; ++T) mma_T(xa, wa, T);
    __builtin_amdgcn_sched_barrier(0);
    buf = buf == 2 ? 0 : buf + 1;
    if (kt < 6) wait_vm<6>(); else wait_vm<0>();
    raw_barrier();
    read_frags(smem + buf * K1_STAGE, 0, xa, wa);
    __builtin_amdgcn_sched_barrier(0);
    const bool more = kt < 5;
#pragma unroll
    for (int T = 0; T < 4; ++T) {
      mma_T(xb, wb, T);
      if (more) {
        if (T == 0) { glds16(asrc[0] + (kt + 3) * 64, sb + (2 * w) * 1024); glds16(asrc[1] + (kt + 3) * 64, sb + (2 * w + 1) * 1024); }
        if (T == 1) { glds16(gsrc[0] + (kt + 3) * 64 * F_, sb + 16384 + (2 * w) * 1024); glds16(gsrc[1] + (kt + 3) * 64 * F_, sb + 16384 + (2 * w + 1) * 1024); }
        if (T == 2) { glds16(usrc[0] + (kt + 3) * 64 * F_, sb + 32768 + (2 * w) * 1024); glds16(usrc[1] + (kt + 3) * 64 * F_, sb + 32768 + (2 * w + 1) * 1024); }
      }
      __builtin_amdgcn_sched_barrier(0);
    }
  }
  read_frags(smem + buf * K1_STAGE, 1, xb, wb);
  __builtin_amdgcn_sched_barrier(0);
#pragma unroll
  for (int T = 0; T < 4; ++T) mma_T(xa, wa, T);
  __builtin_amdgcn_sched_barrier(0);
#pragma unroll
  for (int T = 0; T < 4; ++T) mma_T(xb, wb, T);
  __syncthreads();
  STAMP(9);
  float2* st_s = (float2*)smem;
  float* cs_s = (float*)(smem + 1024);
  if (g == (r >> 2)) {
    const int rr = r & 3;
    const float s2 = rr == 0 ? accS[0] : rr == 1 ? accS[1] : rr == 2 ? accS[2] : accS[3];
    const float mean = accR[0] * (1.f / D_);
    float var = s2 * (1.f / D_) - mean * mean;
    var = var < 0.f ? 0.f : var;
    const float mb = round_bf(mean);
    const float vb = round_bf(var);
    const float sd = round_bf(sqrtf(round_bf(vb + 1e-5f)));
    const float2 stv = make_float2(mb, 1.f / sd);
    const int row = 64 * wm + 16 * wn + r;
    st_s[row] = stv;
    if (ft == 0) stats[row0 + row] = stv;
  }
  if (r == 0) {
    *(f32x4*)(cs_s + wm * 128 + 32 * wn + 4 * g) = accC[0];
    *(f32x4*)(cs_s + wm * 128 + 32 * wn + 16 + 4 * g) = accC[1];
  }
  __syncthreads();
  const bool valid = (f0 + 32 * wn) < F_;
  f32x4 ca[2], cb[2];
#pragma unroll
  for (int T = 0; T < 2; ++T) {
    ca[T] = accC[T];
    cb[T] = *(const f32x4*)(cs_s + (wm ^ 1) * 128 + 32 * wn + 16 * T + 4 * g);
  }
#pragma unroll
  for (int i = 0; i < 4; ++i) {
    const int row = 64 * wm + 16 * (i ^ wn) + r;
    const float2 stv = st_s[row];
    unsigned pk[2][2];
#pragma unroll
    for (int T = 0; T < 2; ++T) {
      float hv[4];
#pragma unroll
      for (int j = 0; j < 4; ++j) {
        const float ga = round_bf(stv.y * (acc[T][i][j] - stv.x * ca[T][j]));
        const float gb = round_bf(stv.y * (acc[2 + T][i][j] - stv.x * cb[T][j]));
        hv[j] = ga * gb;
      }
      pk[T][0] = pk_bf16(hv[0], hv[1]);
      pk[T][1] = pk_bf16(hv[2], hv[3]);
    }
    auto r0 = __builtin_amdgcn_permlane16_swap(pk[0][0], pk[1][0], false, false);
    auto r1 = __builtin_amdgcn_permlane16_swap(pk[0][1], pk[1][1], false, false);
    u32x4 o = {r0[0], r1[0], r0[1], r1[1]};
    if (!valid) o = u32x4{0u, 0u, 0u, 0u};
    *(u32x4*)(hbuf + (size_t)(row0 + row) * FP_ + f0 + 32 * wn + 16 * (g & 1) + 8 * (g >> 1)) = o;
  }
}

DI void conv_block(char* smem, const u16* __restrict__ kin, const u16* __restrict__ vin,
                   const u16* __restrict__ shk, const u16* __restrict__ ssmk, const u16* __restrict__ ssmd,
                   u16* __restrict__ tT, int cg STAMP_ARG) {
  const int tid = threadIdx.x, lane = tid & 63;
  const int w = __builtin_amdgcn_readfirstlane(tid >> 6);
  const int r = lane & 15, g = lane >> 4;
  const int c0 = cg * 8, ch = c0 + w;

  u32x4 hx[4];
#pragma unroll
  for (int i = 0; i < 4; ++i) hx[i] = *(const u32x4*)(ssmk + (size_t)ch * S_ + (i * 64 + lane) * 8);
  touch(((tid & 256) ? vin : kin) + (size_t)(256 * (cg & 7) + (tid & 255)) * D_ + (cg >> 3) * 64, smem + K1_DUMMY);
  wait_vm<0>();
  u32x4 kx[4], vx[4], hal[4][3];
#pragma unroll
  for (int j = 0; j < 4; ++j) {
    const int s = 512 * j + tid;
    kx[j] = *(const u32x4*)(kin + (size_t)s * D_ + c0);
    vx[j] = *(const u32x4*)(vin + (size_t)s * D_ + c0);
#pragma unroll
    for (int d = 0; d < 3; ++d) {
      const int sr = 512 * j + 64 * w - 1 - d;
      hal[j][d] = *(const u32x4*)(kin + (size_t)(sr < 0 ? 0 : sr) * D_ + c0);
      if (j == 0 && sr < 0) hal[j][d] = u32x4{0u, 0u, 0u, 0u};
    }
  }
  float wsh[8][4];
#pragma unroll
  for (int c = 0; c < 8; ++c) {
    const u32x2 t = *(const u32x2*)(shk + (c0 + c) * 4);
    wsh[c][0] = bf_lo(t[0]); wsh[c][1] = bf_hi(t[0]); wsh[c][2] = bf_lo(t[1]); wsh[c][3] = bf_hi(t[1]);
  }
  const float dch = __uint_as_float((unsigned)ssmd[ch] << 16);

  char* us = smem + CV_US;
  char* hq = smem + CV_HQ + w * HQ_PITCH;
  char* fb = smem + CV_FB + w * FB_PITCH;
  if (lane < 30) *(u32x4*)(hq + lane * 16) = u32x4{0u, 0u, 0u, 0u};
  if (lane < 4) *(u32x4*)(us + w * US_PITCH + lane * 16) = u32x4{0u, 0u, 0u, 0u};
#pragma unroll
  for (int i = 0; i < 4; ++i) *(u32x4*)(hq + 480 + (i * 64 + lane) * 16) = hx[i];

  auto make_u = [&](int j) {
    const int s = 512 * j + tid;
    u32x4 k1, k2, k3;
#pragma unroll
    for (int d = 0; d < 4; ++d) {
      k1[d] = (unsigned)__builtin_amdgcn_update_dpp((int)hal[j][0][d], (int)kx[j][d], 0x138, 0xf, 0xf, false);
      k2[d] = (unsigned)__builtin_amdgcn_update_dpp((int)hal[j][1][d], (int)k1[d], 0x138, 0xf, 0xf, false);
      k3[d] = (unsigned)__builtin_amdgcn_update_dpp((int)hal[j][2][d], (int)k2[d], 0x138, 0xf, 0xf, false);
    }
#pragma unroll
    for (int d = 0; d < 4; ++d) {
      const int ca = 2 * d, cb = 2 * d + 1;
      float a = bf_lo(k3[d]) * wsh[ca][3];
      a = fmaf(bf_lo(k2[d]), wsh[ca][2], a);
      a = fmaf(bf_lo(k1[d]), wsh[ca][1], a);
      a = fmaf(bf_lo(kx[j][d]), wsh[ca][0], a);
      float b = bf_hi(k3[d]) * wsh[cb][3];
      b = fmaf(bf_hi(k2[d]), wsh[cb][2], b);
      b = fmaf(bf_hi(k1[d]), wsh[cb][1], b);
      b = fmaf(bf_hi(kx[j][d]), wsh[cb][0], b);
      const unsigned pk = pk_bf16(a * bf_lo(vx[j][d]), b * bf_hi(vx[j][d]));
      *(u16*)(us + ca * US_PITCH + 64 + 2 * s) = (u16)(pk & 0xffffu);
      *(u16*)(us + cb * US_PITCH + 64 + 2 * s) = (u16)(pk >> 16);
    }
  };

  f32x4 acc[8];
#pragma unroll
  for (int n = 0; n < 8; ++n) acc[n] = f32x4{0.f, 0.f, 0.f, 0.f};

  const char* usw = us + w * US_PITCH;
  const char* a_lane = fb + (32 + r - 8 * g) * 8;
  const char* b_lane = hq + 448 + 32 * r + 16 * g;

  STAMP(8);
  make_u(0);
  STAMP(10);
  __syncthreads();
  static_for<0, 4>([&](auto J) {
    constexpr int j = J.value;
    constexpr int NNU = 8 - 2 * j;
    STAMP(1 + j);
    {
      unsigned d0[5], d1[5], d2[5];
#pragma unroll
      for (int it = 0; it < 5; ++it) {
        const unsigned* src = (const unsigned*)(usw + 2 * (512 * j + 16 + 2 * (it * 64 + lane)));
        d2[it] = src[0]; d1[it] = src[-1]; d0[it] = src[-2];
      }
#pragma unroll
      for (int it = 0; it < 5; ++it) {
        u32x4 e;
        e[0] = (d2[it] & 0xffffu) | (d1[it] & 0xffff0000u);
        e[1] = (d1[it] & 0xffffu) | (d0[it] & 0xffff0000u);
        e[2] = (d2[it] >> 16) | (d2[it] << 16);
        e[3] = (d1[it] >> 16) | (d1[it] << 16);
        *(u32x4*)(fb + (it * 64 + lane) * 16) = e;
      }
    }
    if constexpr (j < 3) make_u(j + 1);
    wait_lgkm0();
    if constexpr (j == 0) STAMP(6);
    bf16x8 af[2][2], bfr[2][NNU];
    auto load_set = [&](int set, int k0) {
#pragma unroll
      for (int kk = 0; kk < 2; ++kk) {
        const char* a = a_lane + (8 * kk + k0) * 256;
        af[set][kk] = cat8(*(const s16x4*)a, *(const s16x4*)(a - 32));
      }
#pragma unroll
      for (int nu = 0; nu < NNU; ++nu) bfr[set][nu] = *(const bf16x8*)(b_lane + 512 * nu - 64 * k0);
    };
    auto mma_set = [&](int set) {
#pragma unroll
      for (int nu = 0; nu < NNU; ++nu) {
        acc[2 * j + nu] = mfma16(af[set][0], bfr[set][nu], acc[2 * j + nu]);
        if (2 * j + 1 + nu < 8) acc[2 * j + 1 + nu] = mfma16(af[set][1], bfr[set][nu], acc[2 * j + 1 + nu]);
      }
    };
    load_set(0, 0);
#pragma unroll 1
    for (int k0 = 0; k0 < 8; k0 += 2) {
      load_set(1, k0 + 1);
      __builtin_amdgcn_sched_barrier(0);
      mma_set(0);
      __builtin_amdgcn_sched_barrier(0);
      load_set(0, (k0 + 2) & 7);
      __builtin_amdgcn_sched_barrier(0);
      mma_set(1);
      __builtin_amdgcn_sched_barrier(0);
    }
    if constexpr (j < 3) __syncthreads();
  });
  STAMP(9);
#pragma unroll
  for (int n = 0; n < 8; ++n) {
    const int s = 256 * n + 16 * r + 4 * g;
    const u32x2 uu = *(const u32x2*)(usw + 64 + 2 * s);
    float t[4];
    t[0] = round_bf(acc[n][0]) + round_bf(bf_lo(uu[0]) * dch);
    t[1] = round_bf(acc[n][1]) + round_bf(bf_hi(uu[0]) * dch);
    t[2] = round_bf(acc[n][2]) + round_bf(bf_lo(uu[1]) * dch);
    t[3] = round_bf(acc[n][3]) + round_bf(bf_hi(uu[1]) * dch);
    u32x2 o = {pk_bf16(t[0], t[1]), pk_bf16(t[2], t[3])};
    *(u32x2*)(tT + (size_t)ch * S_ + s) = o;
  }
}

__global__ __launch_bounds__(512, 2) void k1_kernel(const u16* __restrict__ x, const u16* __restrict__ kin,
                                                     const u16* __restrict__ vin, const u16* __restrict__ shk,
                                                     const u16* __restrict__ ssmk, const u16* __restrict__ ssmd,
                                                     const u16* __restrict__ wg, const u16* __restrict__ wu,
                                                     const u16* __restrict__ wd,
                                                     u16* __restrict__ hbuf, u16* __restrict__ tT,
                                                     float2* __restrict__ stats) {
  __shared__ __attribute__((aligned(16))) char smem[K1_LDS];
  const int b = blockIdx.x, xcd = b & 7, idx = b >> 3;
  STAMP_DECL;
  STAMP(0);
  if (idx < 8) {
    conv_block(smem, kin, vin, shk, ssmk, ssmd, tT, xcd * 8 + idx STAMP_PASS);
  } else if (idx < 30) {
    const int t = 22 * (xcd & 3) + (idx - 8);
    gemm1_block(smem, x, wg, wu, hbuf, stats, 8 * (xcd >> 2) + (t & 7), t >> 3 STAMP_PASS);
  } else {
    const int part = (idx - 30) * 8 + xcd;
    for (int i = threadIdx.x; i < 688; i += 512) touch(wd + (size_t)(part * 688 + i) * 64, smem + K1_DUMMY);
    wait_vm<0>();
  }
  STAMP(15);
  STAMP_FLUSH(0);
}

constexpr int K2_NB = 4;
constexpr int K2_STAGE = 32768;
constexpr int K2_TT = K2_NB * K2_STAGE;
constexpr int K2_X = K2_TT + 8192;
constexpr int K2_ST = K2_X + 8192;
constexpr int K2_DUMMY = K2_ST + 1024;
constexpr int K2_LDS = K2_DUMMY + 256;
constexpr int K2_NT = FP_ / 128;

__global__ __launch_bounds__(512, 2) void k2_kernel(const u16* __restrict__ x, const u16* __restrict__ hbuf,
                                                     const u16* __restrict__ wd, const u16* __restrict__ tT,
                                                     const float2* __restrict__ stats, u16* __restrict__ out) {
  __shared__ __attribute__((aligned(16))) char smem[K2_LDS];
  const int tid = threadIdx.x, lane = tid & 63;
  const int w = __builtin_amdgcn_readfirstlane(tid >> 6);
  const int kg = w >> 2, wm = (w >> 1) & 1, wn = w & 1;
  const int r = lane & 15, g = lane >> 4, q = (lane & 15) >> 2, p = lane & 3;
  const int b = blockIdx.x, xcd = b & 7, idx = b >> 3;
  const int row0 = (4 * xcd + (idx >> 3)) * 64, d0 = (idx & 7) * 64;
  STAMP_DECL;
  STAMP(0);

  const u16* asrc[2];
  int wrow[2], wcol[2];
#pragma unroll
  for (int e = 0; e < 2; ++e) {
    const int ar = 8 * w + 4 * e + (lane >> 4);
    asrc[e] = hbuf + (size_t)(row0 + ar) * FP_ + (((lane & 15) ^ (ar & 15)) << 3);
    const int kr = 16 * w + 8 * e + (lane >> 3);
    const int phi_s = ((kr >> 1) & 1) | (((kr >> 3) & 1) << 1);
    wrow[e] = kr;
    wcol[e] = d0 + ((((((lane & 7) >> 1) ^ phi_s) << 1) | (lane & 1)) << 3);
  }
  auto stage = [&](int kt) {
    char* sb = smem + (kt & (K2_NB - 1)) * K2_STAGE;
#pragma unroll
    for (int e = 0; e < 2; ++e) glds16(asrc[e] + kt * 128, sb + (2 * w + e) * 1024);
#pragma unroll
    for (int e = 0; e < 2; ++e) {
      int kr = kt * 128 + wrow[e];
      kr = kr > F_ - 1 ? F_ - 1 : kr;
      glds16(wd + (size_t)kr * D_ + wcol[e], sb + 16384 + (2 * w + e) * 1024);
    }
  };
  stage(0);
  {
    const bool ish = tid < 176;
    const int hr = tid / 22, hl = tid % 22;
    int kr = 344 * (idx >> 3) + (tid - 176);
    kr = kr > F_ - 1 ? F_ - 1 : kr;
    const u16* ph = hbuf + (size_t)(row0 + 8 * (idx & 7) + (hr & 7)) * FP_ + hl * 64;
    const u16* pw = wd + (size_t)kr * D_ + d0;
    touch(ish ? ph : pw, smem + K2_DUMMY);
  }
  stage(1);
  stage(2);

  const int erow = row0 + 32 * wm + 16 * kg + r;
  const int edoff = d0 + 32 * wn + 16 * (g & 1) + 8 * (g >> 1);

  int aoff[2][2], boff[2];
#pragma unroll
  for (int i = 0; i < 2; ++i)
#pragma unroll
    for (int hs = 0; hs < 2; ++hs)
      aoff[i][hs] = (32 * wm + 16 * i + r) * 256 + (((4 * (kg + 2 * hs) + g) ^ r) << 4);
  const int phiL = ((q >> 1) & 1) | ((g & 1) << 1);
#pragma unroll
  for (int T = 0; T < 2; ++T)
    boff[T] = 16384 + (32 * kg + 8 * g + q) * 128 + (((2 * wn + T) ^ phiL) << 5) + p * 8;

  f32x4 acc[2][2];
#pragma unroll
  for (int T = 0; T < 2; ++T)
#pragma unroll
    for (int i = 0; i < 2; ++i) acc[T][i] = f32x4{0.f, 0.f, 0.f, 0.f};

  bf16x8 hA[2][2], wA[2][2], hB[2][2], wB[2][2];
  auto read_set = [&](int kt, bf16x8 (&hf)[2][2], bf16x8 (&wf)[2][2]) {
    const char* sb = smem + (kt & (K2_NB - 1)) * K2_STAGE;
#pragma unroll
    for (int hs = 0; hs < 2; ++hs) {
#pragma unroll
      for (int T = 0; T < 2; ++T) wf[hs][T] = cat8(tr_read(sb + boff[T] + hs * 8192), tr_read(sb + boff[T] + hs * 8192 + 512));
#pragma unroll
      for (int i = 0; i < 2; ++i) hf[hs][i] = *(const bf16x8*)(sb + aoff[i][hs]);
    }
  };
  auto mma_set = [&](const bf16x8 (&hf)[2][2], const bf16x8 (&wf)[2][2]) {
#pragma unroll
    for (int hs = 0; hs < 2; ++hs)
#pragma unroll
      for (int T = 0; T < 2; ++T)
#pragma unroll
        for (int i = 0; i < 2; ++i) acc[T][i] = mfma16(wf[hs][T], hf[hs][i], acc[T][i]);
  };
  wait_vm<9>();
  raw_barrier();
  STAMP(1);
  read_set(0, hA, wA);
  stage(3);
#pragma unroll 1
  for (int kt = 1; kt < 9; kt += 2) {
    wait_vm<8>();
    raw_barrier();
    read_set(kt, hB, wB);
    __builtin_amdgcn_sched_barrier(0);
    mma_set(hA, wA);
    stage(kt + 3);
    __builtin_amdgcn_sched_barrier(0);
    wait_vm<8>();
    raw_barrier();
    read_set(kt + 1, hA, wA);
    __builtin_amdgcn_sched_barrier(0);
    mma_set(hB, wB);
    if (kt < 7) stage(kt + 4);
    __builtin_amdgcn_sched_barrier(0);
  }
  glds16(tT + (size_t)(d0 + 8 * w + (lane >> 3)) * S_ + row0 + (lane & 7) * 8, smem + K2_TT + w * 1024);
  glds16(x + (size_t)(row0 + 8 * w + (lane >> 3)) * D_ + d0 + (lane & 7) * 8, smem + K2_X + w * 1024);
  glds16(stats + row0 + 2 * (lane & 31), smem + K2_ST);
  wait_vm<7>();
  raw_barrier();
  STAMP(10);
  read_set(9, hB, wB);
  __builtin_amdgcn_sched_barrier(0);
  mma_set(hA, wA);
  __builtin_amdgcn_sched_barrier(0);
  wait_vm<3>();
  raw_barrier();
  STAMP(11);
  read_set(10, hA, wA);
  __builtin_amdgcn_sched_barrier(0);
  mma_set(hB, wB);
  __builtin_amdgcn_sched_barrier(0);
  mma_set(hA, wA);
  STAMP(12);
  wait_vm<0>();
  raw_barrier();
  const int lrow = 32 * wm + 16 * kg + r;
  const u32x4 xv = *(const u32x4*)(smem + K2_X + lrow * 128 + (32 * wn + 16 * (g & 1) + 8 * (g >> 1)) * 2);
  const f32x2 stv = *(const f32x2*)(smem + K2_ST + lrow * 8);
  {
    f32x4* xb = (f32x4*)smem;
    const int wq = w & 3;
#pragma unroll
    for (int T = 0; T < 2; ++T) xb[((kg * 4 + wq) * 2 + T) * 64 + lane] = kg ? acc[T][0] : acc[T][1];
    raw_barrier();
    f32x4 fin[2];
#pragma unroll
    for (int T = 0; T < 2; ++T) {
      const f32x4 o = xb[(((kg ^ 1) * 4 + wq) * 2 + T) * 64 + lane];
      fin[T] = (kg ? acc[T][1] : acc[T][0]) + o;
    }
    unsigned pb[2][2], pt[2][2];
#pragma unroll
    for (int T = 0; T < 2; ++T) {
      pb[T][0] = pk_bf16(fin[T][0], fin[T][1]);
      pb[T][1] = pk_bf16(fin[T][2], fin[T][3]);
      const s16x4 tv = tr_read(smem + K2_TT + (32 * wn + 16 * T + 4 * g + q) * 128 + (32 * wm + 16 * kg + 4 * p) * 2);
      const u32x2 tu = __builtin_bit_cast(u32x2, tv);
      pt[T][0] = tu[0];
      pt[T][1] = tu[1];
    }
    auto b0 = __builtin_amdgcn_permlane16_swap(pb[0][0], pb[1][0], false, false);
    auto b1 = __builtin_amdgcn_permlane16_swap(pb[0][1], pb[1][1], false, false);
    auto t0 = __builtin_amdgcn_permlane16_swap(pt[0][0], pt[1][0], false, false);
    auto t1 = __builtin_amdgcn_permlane16_swap(pt[0][1], pt[1][1], false, false);
    const u32x4 bv = {b0[0], b1[0], b0[1], b1[1]};
    const u32x4 tv = {t0[0], t1[0], t0[1], t1[1]};
    u32x4 o;
#pragma unroll
    for (int j = 0; j < 4; ++j) {
      float res[2];
#pragma unroll
      for (int hh = 0; hh < 2; ++hh) {
        const float xe = hh ? bf_hi(xv[j]) : bf_lo(xv[j]);
        const float te = hh ? bf_hi(tv[j]) : bf_lo(tv[j]);
        const float be = hh ? bf_hi(bv[j]) : bf_lo(bv[j]);
        const float xn = round_bf(round_bf(xe - stv[0]) * stv[1]);
        const float a = round_bf(xn * te);
        res[hh] = round_bf(xe + a) + be;
      }
      o[j] = pk_bf16(res[0], res[1]);
    }
    *(u32x4*)(out + (size_t)erow * D_ + edoff) = o;
  }
  STAMP(15);
  STAMP_FLUSH(1);
}

extern "C" void kernel_launch(void* const* d_in, const int* in_sizes, int n_in, void* d_out, int out_size,
                              void* d_ws, size_t ws_size, hipStream_t stream) {
  const u16* q = (const u16*)d_in[0];
  const u16* k = (const u16*)d_in[1];
  const u16* v = (const u16*)d_in[2];
  const u16* shk = (const u16*)d_in[3];
  const u16* ssmk = (const u16*)d_in[4];
  const u16* ssmd = (const u16*)d_in[5];
  const u16* wg = (const u16*)d_in[7];
  const u16* wu = (const u16*)d_in[8];
  const u16* wd = (const u16*)d_in[9];
  char* ws = (char*)d_ws;
  u16* hbuf = (u16*)(ws + WS_H);
  u16* tT = (u16*)(ws + WS_T);
  float2* stats = (float2*)(ws + WS_ST);
  k1_kernel<<<256, 512, 0, stream>>>(q, k, v, shk, ssmk, ssmd, wg, wu, wd, hbuf, tT, stats);
  k2_kernel<<<256, 512, 0, stream>>>(q, hbuf, wd, tT, stats, (u16*)d_out);
}
```

```cpp
#include <hip/hip_runtime.h>
#include <hip/hip_bf16.h>
#include <stdint.h>

typedef __attribute__((ext_vector_type(8))) short bf16x8;
typedef __attribute__((ext_vector_type(4))) short s16x4;
typedef __attribute__((ext_vector_type(2))) __bf16 bf16x2;
typedef __attribute__((ext_vector_type(4))) float f32x4;
typedef __attribute__((ext_vector_type(2))) float f32x2;
typedef __attribute__((ext_vector_type(4))) unsigned u32x4;
typedef __attribute__((ext_vector_type(2))) unsigned u32x2;
typedef __attribute__((address_space(3))) s16x4* lds_s16x4_ptr;
typedef unsigned short u16;

#define DI __device__ __forceinline__

constexpr int S_ = 2048, D_ = 512, F_ = 1376, FP_ = 1408;

constexpr size_t WS_H = 0;
constexpr size_t WS_T = 6u * 1024 * 1024;
constexpr size_t WS_ST = 8u * 1024 * 1024 + 512 * 1024;

template <int N> DI void wait_vm() { asm volatile("s_waitcnt vmcnt(%0)" ::"i"(N) : "memory"); }
DI void wait_lgkm0() { asm volatile("s_waitcnt lgkmcnt(0)" ::: "memory"); }
DI void raw_barrier() {
  asm volatile("s_waitcnt lgkmcnt(0)" ::: "memory");
  __builtin_amdgcn_s_barrier();
  asm volatile("" ::: "memory");
}

DI unsigned pk_bf16(float lo, float hi) {
  f32x2 f = {lo, hi};
  return __builtin_bit_cast(unsigned, __builtin_convertvector(f, bf16x2));
}
DI float bf_lo(unsigned v) { return __uint_as_float(v << 16); }
DI float bf_hi(unsigned v) { return __uint_as_float(v & 0xffff0000u); }
DI float round_bf(float x) { return bf_lo(pk_bf16(x, x)); }
DI float dot2bf(unsigned a, unsigned b, float c) {
  return __builtin_amdgcn_fdot2_f32_bf16(__builtin_bit_cast(bf16x2, a), __builtin_bit_cast(bf16x2, b), c, false);
}
DI s16x4 tr_read(const char* p) {
  return __builtin_amdgcn_ds_read_tr16_b64_v4i16((lds_s16x4_ptr)p);
}
DI bf16x8 cat8(s16x4 lo, s16x4 hi) { return __builtin_shufflevector(lo, hi, 0, 1, 2, 3, 4, 5, 6, 7); }
DI f32x4 mfma16(bf16x8 a, bf16x8 b, f32x4 c) { return __builtin_amdgcn_mfma_f32_16x16x32_bf16(a, b, c, 0, 0, 0); }

DI void glds16(const void* g, char* lds) {
  unsigned keep;
  const unsigned dst = __builtin_amdgcn_readfirstlane((unsigned)(size_t)lds);
  asm volatile("s_mov_b32 %0, m0\n\ts_mov_b32 m0, %2\n\ts_nop 0\n\tglobal_load_lds_dwordx4 %1, off\n\ts_mov_b32 m0, %0"
               : "=&s"(keep) : "v"(g), "s"(dst) : "memory");
}
DI void touch(const void* g, char* dummy) {
  unsigned keep;
  const unsigned dst = __builtin_amdgcn_readfirstlane((unsigned)(size_t)dummy);
  asm volatile("s_mov_b32 %0, m0\n\ts_mov_b32 m0, %2\n\ts_nop 0\n\tglobal_load_lds_dword %1, off\n\ts_mov_b32 m0, %0"
               : "=&s"(keep) : "v"(g), "s"(dst) : "memory");
}

#define STAMP_DECL do {} while (0)
#define STAMP(I) do {} while (0)
#define STAMP_FLUSH(K) do {} while (0)
#define STAMP_ARG
#define STAMP_PASS

template <int I, int N, class Fn> DI void static_for(Fn&& fn) {
  if constexpr (I < N) {
    fn(std::integral_constant<int, I>{});
    static_for<I + 1, N>(fn);
  }
}

constexpr int K1_STAGE = 49152;
constexpr int K1_DUMMY = 151808;
constexpr int K1_LDS = K1_DUMMY + 256;

constexpr int US_PITCH = 4160;
constexpr int HQ_PITCH = 4576;
constexpr int FB_PITCH = 5120;
constexpr int CV_US = 0;
constexpr int CV_HQ = 8 * US_PITCH;
constexpr int CV_FB = CV_HQ + 8 * HQ_PITCH;
static_assert(CV_FB + 16 * FB_PITCH <= K1_DUMMY && 3 * K1_STAGE <= K1_DUMMY, "lds layout");

DI void gemm1_block(char* smem, const u16* __restrict__ x, const u16* __restrict__ wg, const u16* __restrict__ wu,
                    u16* __restrict__ hbuf, float2* __restrict__ stats, int mt, int ft STAMP_ARG) {
  const int tid = threadIdx.x, lane = tid & 63;
  const int w = __builtin_amdgcn_readfirstlane(tid >> 6);
  const int wm = w >> 2, wn = w & 3;
  const int r = lane & 15, g = lane >> 4, q = (lane & 15) >> 2, p = lane & 3;
  const int row0 = mt * 128, f0 = ft * 128;

  const u16* asrc[2];
  const u16* gsrc[2];
  const u16* usrc[2];
#pragma unroll
  for (int e = 0; e < 2; ++e) {
    const int i = 2 * w + e;
    const int row = 8 * i + (lane >> 3);
    asrc[e] = x + (size_t)(row0 + row) * D_ + (((lane & 7) ^ (row & 7)) << 3);
    const int kr = 4 * i + (lane >> 4);
    const int phi = (kr & 3) | (((kr >> 3) & 1) << 2);
    const int sc = ((((lane & 15) >> 1) ^ phi) << 1) | (lane & 1);
    int col = f0 + sc * 8;
    col = col > F_ - 8 ? F_ - 8 : col;
    gsrc[e] = wg + kr * F_ + col;
    usrc[e] = wu + kr * F_ + col;
  }
  auto stage = [&](int kt, int buf) {
    char* sb = smem + buf * K1_STAGE;
#pragma unroll
    for (int e = 0; e < 2; ++e) glds16(asrc[e] + kt * 64, sb + (2 * w + e) * 1024);
#pragma unroll
    for (int e = 0; e < 2; ++e) glds16(gsrc[e] + kt * 64 * F_, sb + 16384 + (2 * w + e) * 1024);
#pragma unroll
    for (int e = 0; e < 2; ++e) glds16(usrc[e] + kt * 64 * F_, sb + 32768 + (2 * w + e) * 1024);
  };

  stage(0, 0);
  stage(1, 1);
  {
    const int kr = 64 * (mt & 7) + (tid >> 3) % 64;
    const int sel = tid & 7;
    int col = f0 + (sel & 1) * 64;
    col = col > F_ - 2 ? F_ - 2 : col;
    const u16* pw = ((sel & 2) ? wu : wg) + kr * F_ + col;
    const int t2 = (tid >> 3) * 4 + (tid & 3);
    const int xr = 3 * (t2 >> 3) + (ft % 3);
    const u16* px = x + (size_t)(row0 + xr) * D_ + (t2 & 7) * 64;
    touch((sel & 4) ? px : pw, smem + K1_DUMMY);
    const int xr2 = 96 + 3 * ((tid >> 3) % 11) + (ft % 3);
    const u16* px2 = x + (size_t)(row0 + (xr2 > 127 ? 127 : xr2)) * D_ + (tid & 7) * 64;
    touch(px2, smem + K1_DUMMY);
  }
  stage(2, 2);

  int aoff[4][2];
#pragma unroll
  for (int i = 0; i < 4; ++i)
#pragma unroll
    for (int ks = 0; ks < 2; ++ks)
      aoff[i][ks] = (64 * wm + 16 * (i ^ wn) + r) * 128 + (((4 * ks + g) ^ (r & 7)) << 4);
  const int phiL = q | ((g & 1) << 2);
  int boff[4];
#pragma unroll
  for (int T = 0; T < 4; ++T) {
    const int mat = (T >> 1) ^ wm;
    boff[T] = 16384 + mat * 16384 + (8 * g + q) * 256 + (((2 * wn + (T & 1)) ^ phiL) << 5) + p * 8;
  }

  f32x4 acc[4][4];
#pragma unroll
  for (int T = 0; T < 4; ++T)
#pragma unroll
    for (int i = 0; i < 4; ++i) acc[T][i] = f32x4{0.f, 0.f, 0.f, 0.f};
  f32x4 accS = {0.f, 0.f, 0.f, 0.f}, accR = accS, accC[2] = {accS, accS};
  const bf16x8 ones = {0x3f80, 0x3f80, 0x3f80, 0x3f80, 0x3f80, 0x3f80, 0x3f80, 0x3f80};

  struct Frags { s16x4 wl[4], wh[4]; bf16x8 xf[4]; };
  Frags fa, fb2;
  auto read_piece = [&](Frags& f, const char* sb, int ks, int n) {
    if (n < 4) f.xf[n] = *(const bf16x8*)(sb + aoff[n][ks]);
    else {
      const int T = (n - 4) >> 1;
      if ((n & 1) == 0) f.wl[T] = tr_read(sb + boff[T] + ks * 8192);
      else f.wh[T] = tr_read(sb + boff[T] + ks * 8192 + 1024);
    }
  };
  auto mma_one = [&](const Frags& f, int m) {
    const int T = m / 5, i = m % 5;
    if (i < 4) acc[T][i] = mfma16(cat8(f.wl[T], f.wh[T]), f.xf[i], acc[T][i]);
    else if (T == 0) accS = mfma16(f.xf[0], f.xf[0], accS);
    else if (T == 1) accR = mfma16(ones, f.xf[0], accR);
    else if (T == 2) accC[0] = mfma16(cat8(f.wl[0], f.wh[0]), ones, accC[0]);
    else accC[1] = mfma16(cat8(f.wl[1], f.wh[1]), ones, accC[1]);
  };
#define SB __builtin_amdgcn_sched_barrier(0)

  wait_vm<14>();
  raw_barrier();
  STAMP(1);
#pragma unroll
  for (int n = 0; n < 12; ++n) read_piece(fa, smem, 0, n);
  auto dma_piece = [&](int kt, char* sb, int n) {
    if (n == 0) glds16(asrc[0] + kt * 64, sb + (2 * w) * 1024);
    if (n == 1) glds16(asrc[1] + kt * 64, sb + (2 * w + 1) * 1024);
    if (n == 2) glds16(gsrc[0] + kt * 64 * F_, sb + 16384 + (2 * w) * 1024);
    if (n == 3) glds16(gsrc[1] + kt * 64 * F_, sb + 16384 + (2 * w + 1) * 1024);
    if (n == 4) glds16(usrc[0] + kt * 64 * F_, sb + 32768 + (2 * w) * 1024);
    if (n == 5) glds16(usrc[1] + kt * 64 * F_, sb + 32768 + (2 * w + 1) * 1024);
  };
  int buf = 0;
  char* sprev = smem;
#pragma unroll 1
  for (int kt = 0; kt < 7; ++kt) {
    char* sb = smem + buf * K1_STAGE;
    const bool late = wm == 1 && kt >= 1 && kt < 6;
    SB;
#pragma unroll
    for (int m = 0; m < 16; ++m) {
      mma_one(fa, m);
      if (m < 12) read_piece(fb2, sb, 1, m);
      if ((m == 1 || m == 3 || m == 6 || m == 8 || m == 11 || m == 13) && late)
        dma_piece(kt + 2, sprev, m == 1 ? 0 : m == 3 ? 1 : m == 6 ? 2 : m == 8 ? 3 : m == 11 ? 4 : 5);
      SB;
    }
    buf = buf == 2 ? 0 : buf + 1;
    const char* sn = smem + buf * K1_STAGE;
    if (kt < 6) wait_vm<6>(); else wait_vm<0>();
    raw_barrier();
    SB;
    const bool early = wm == 0 && kt < 5;
#pragma unroll
    for (int m = 16; m < 20; ++m) {
      mma_one(fa, m);
      if (m == 17 && early) dma_piece(kt + 3, sb, 0);
      SB;
    }
#pragma unroll
    for (int m = 0; m < 20; ++m) {
      mma_one(fb2, m);
      if (m < 12) read_piece(fa, sn, 0, m);
      if ((m == 1 || m == 5 || m == 9 || m == 13 || m == 17) && early) dma_piece(kt + 3, sb, 1 + (m >> 2));
      SB;
    }
    sprev = sb;
  }
  {
    const char* sb = smem + buf * K1_STAGE;
    SB;
#pragma unroll
    for (int m = 0; m < 20; ++m) {
      mma_one(fa, m);
      if (m < 12) read_piece(fb2, sb, 1, m);
      SB;
    }
#pragma unroll
    for (int m = 0; m < 20; ++m) mma_one(fb2, m);
  }
  __syncthreads();
  STAMP(9);
  float2* st_s = (float2*)smem;
  float* cs_s = (float*)(smem + 1024);
  if (g == (r >> 2)) {
    const int rr = r & 3;
    const float s2 = rr == 0 ? accS[0] : rr == 1 ? accS[1] : rr == 2 ? accS[2] : accS[3];
    const float mean = accR[0] * (1.f / D_);
    float var = s2 * (1.f / D_) - mean * mean;
    var = var < 0.f ? 0.f : var;
    const float mb = round_bf(mean);
    const float vb = round_bf(var);
    const float sd = round_bf(sqrtf(round_bf(vb + 1e-5f)));
    const float2 stv = make_float2(mb, 1.f / sd);
    const int row = 64 * wm + 16 * wn + r;
    st_s[row] = stv;
    if (ft == 0) stats[row0 + row] = stv;
  }
  if (r == 0) {
    *(f32x4*)(cs_s + wm * 128 + 32 * wn + 4 * g) = accC[0];
    *(f32x4*)(cs_s + wm * 128 + 32 * wn + 16 + 4 * g) = accC[1];
  }
  __syncthreads();
  const bool valid = (f0 + 32 * wn) < F_;
  f32x4 ca[2], cb[2];
#pragma unroll
  for (int T = 0; T < 2; ++T) {
    ca[T] = accC[T];
    cb[T] = *(const f32x4*)(cs_s + (wm ^ 1) * 128 + 32 * wn + 16 * T + 4 * g);
  }
#pragma unroll
  for (int i = 0; i < 4; ++i) {
    const int row = 64 * wm + 16 * (i ^ wn) + r;
    const float2 stv = st_s[row];
    const float nm = -stv.x, r2 = stv.y * stv.y;
    unsigned pk[2][2];
#pragma unroll
    for (int T = 0; T < 2; ++T) {
      const f32x4 ga = acc[T][i] + nm * ca[T];
      const f32x4 gb = acc[2 + T][i] + nm * cb[T];
      const f32x4 hv = (ga * r2) * gb;
      pk[T][0] = pk_bf16(hv[0], hv[1]);
      pk[T][1] = pk_bf16(hv[2], hv[3]);
    }
    auto r0 = __builtin_amdgcn_permlane16_swap(pk[0][0], pk[1][0], false, false);
    auto r1 = __builtin_amdgcn_permlane16_swap(pk[0][1], pk[1][1], false, false);
    u32x4 o = {r0[0], r1[0], r0[1], r1[1]};
    if (!valid) o = u32x4{0u, 0u, 0u, 0u};
    *(u32x4*)(hbuf + (size_t)(row0 + row) * FP_ + f0 + 32 * wn + 16 * (g & 1) + 8 * (g >> 1)) = o;
  }
}

DI void conv_block(char* smem, const u16* __restrict__ kin, const u16* __restrict__ vin,
                   const u16* __restrict__ shk, const u16* __restrict__ ssmk, const u16* __restrict__ ssmd,
                   u16* __restrict__ tT, int cg STAMP_ARG) {
  const int tid = threadIdx.x, lane = tid & 63;
  const int w = __builtin_amdgcn_readfirstlane(tid >> 6);
  const int r = lane & 15, g = lane >> 4;
  const int c0 = cg * 8, ch = c0 + w;

  u32x4 hx[4];
#pragma unroll
  for (int i = 0; i < 4; ++i) hx[i] = *(const u32x4*)(ssmk + (size_t)ch * S_ + (i * 64 + lane) * 8);
  touch(((tid & 256) ? vin : kin) + (size_t)(256 * (cg & 7) + (tid & 255)) * D_ + (cg >> 3) * 64, smem + K1_DUMMY);
  float wsh[8][4];
#pragma unroll
  for (int c = 0; c < 8; ++c) {
    const u32x2 t = *(const u32x2*)(shk + (c0 + c) * 4);
    wsh[c][0] = bf_lo(t[0]); wsh[c][1] = bf_hi(t[0]); wsh[c][2] = bf_lo(t[1]); wsh[c][3] = bf_hi(t[1]);
  }
  const float dch = __uint_as_float((unsigned)ssmd[ch] << 16);
  wait_vm<0>();

  u32x4 kx[4], vx[4], hal[4][3];
  auto loads = [&](int j) {
    const int s = 512 * j + tid;
    kx[j] = *(const u32x4*)(kin + (size_t)s * D_ + c0);
    vx[j] = *(const u32x4*)(vin + (size_t)s * D_ + c0);
#pragma unroll
    for (int d = 0; d < 3; ++d) {
      const int sr = 512 * j + 64 * w - 1 - d;
      hal[j][d] = *(const u32x4*)(kin + (size_t)(sr < 0 ? 0 : sr) * D_ + c0);
      if (j == 0 && sr < 0) hal[j][d] = u32x4{0u, 0u, 0u, 0u};
    }
  };
  loads(0);
  loads(1);

  char* us = smem + CV_US;
  char* hq = smem + CV_HQ + w * HQ_PITCH;
  char* fb = smem + CV_FB + w * (2 * FB_PITCH);
  if (lane < 30) *(u32x4*)(hq + lane * 16) = u32x4{0u, 0u, 0u, 0u};
  if (lane < 4) *(u32x4*)(us + w * US_PITCH + lane * 16) = u32x4{0u, 0u, 0u, 0u};
#pragma unroll
  for (int i = 0; i < 4; ++i) *(u32x4*)(hq + 480 + (i * 64 + lane) * 16) = hx[i];

  auto make_u_d = [&](int j, int d) {
    const int s = 512 * j + tid;
    const unsigned k0 = kx[j][d];
    const unsigned k1 = (unsigned)__builtin_amdgcn_update_dpp((int)hal[j][0][d], (int)k0, 0x138, 0xf, 0xf, false);
    const unsigned k2 = (unsigned)__builtin_amdgcn_update_dpp((int)hal[j][1][d], (int)k1, 0x138, 0xf, 0xf, false);
    const unsigned k3 = (unsigned)__builtin_amdgcn_update_dpp((int)hal[j][2][d], (int)k2, 0x138, 0xf, 0xf, false);
    const int ca = 2 * d, cb = 2 * d + 1;
    float a = bf_lo(k3) * wsh[ca][3];
    a = fmaf(bf_lo(k2), wsh[ca][2], a);
    a = fmaf(bf_lo(k1), wsh[ca][1], a);
    a = fmaf(bf_lo(k0), wsh[ca][0], a);
    float b = bf_hi(k3) * wsh[cb][3];
    b = fmaf(bf_hi(k2), wsh[cb][2], b);
    b = fmaf(bf_hi(k1), wsh[cb][1], b);
    b = fmaf(bf_hi(k0), wsh[cb][0], b);
    const unsigned pk = pk_bf16(a * bf_lo(vx[j][d]), b * bf_hi(vx[j][d]));
    *(u16*)(us + ca * US_PITCH + 64 + 2 * s) = (u16)(pk & 0xffffu);
    *(u16*)(us + cb * US_PITCH + 64 + 2 * s) = (u16)(pk >> 16);
  };
  const char* usw = us + w * US_PITCH;
  unsigned td[3];
  auto tab_read = [&](int j, int it) {
    const unsigned* src = (const unsigned*)(usw + 2 * (512 * j + 16 + 2 * (it * 64 + lane)));
    td[2] = src[0]; td[1] = src[-1]; td[0] = src[-2];
  };
  auto tab_write = [&](int j, int it) {
    u32x4 e;
    e[0] = (td[2] & 0xffffu) | (td[1] & 0xffff0000u);
    e[1] = (td[1] & 0xffffu) | (td[0] & 0xffff0000u);
    e[2] = (td[2] >> 16) | (td[2] << 16);
    e[3] = (td[1] >> 16) | (td[1] << 16);
    *(u32x4*)(fb + (j & 1) * FB_PITCH + (it * 64 + lane) * 16) = e;
  };

  f32x4 acc[8];
#pragma unroll
  for (int n = 0; n < 8; ++n) acc[n] = f32x4{0.f, 0.f, 0.f, 0.f};

  const char* a_lane = fb + (32 + r - 8 * g) * 8;
  const char* b_lane = hq + 448 + 32 * r + 16 * g;

  STAMP(8);
#pragma unroll
  for (int d = 0; d < 4; ++d) make_u_d(0, d);
  loads(2);
  STAMP(10);
  __syncthreads();
  STAMP(11);
  {
    unsigned t5[5][3];
#pragma unroll
    for (int it = 0; it < 5; ++it) { tab_read(0, it); t5[it][0] = td[0]; t5[it][1] = td[1]; t5[it][2] = td[2]; }
#pragma unroll
    for (int it = 0; it < 5; ++it) { td[0] = t5[it][0]; td[1] = t5[it][1]; td[2] = t5[it][2]; tab_write(0, it); }
  }
#pragma unroll
  for (int d = 0; d < 4; ++d) make_u_d(1, d);
  loads(3);
  __syncthreads();

  static_for<0, 4>([&](auto J) {
    constexpr int j = J.value;
    constexpr int NNU = 8 - 2 * j;
    constexpr int NM = 2 * NNU - 1;
    constexpr int NR = NNU + 4;
    constexpr int NS = NM > NR ? NM : NR;
    STAMP(1 + j);
    s16x4 al[2][2], ah[2][2];
    bf16x8 bfr[2][NNU];
    auto read_one = [&](int set, int k0, int n) {
      if (n < 4) {
        const char* a = a_lane + (j & 1) * FB_PITCH + (8 * (n >> 1) + k0) * 256;
        if ((n & 1) == 0) al[set][n >> 1] = *(const s16x4*)a; else ah[set][n >> 1] = *(const s16x4*)(a - 32);
      } else {
        bfr[set][n - 4] = *(const bf16x8*)(b_lane + 512 * (n - 4) - 64 * k0);
      }
    };
    auto mma_one = [&](int set, int m) {
      if (m < NNU) acc[2 * j + m] = mfma16(cat8(al[set][0], ah[set][0]), bfr[set][m], acc[2 * j + m]);
      else acc[2 * j + 1 + (m - NNU)] = mfma16(cat8(al[set][1], ah[set][1]), bfr[set][m - NNU], acc[2 * j + 1 + (m - NNU)]);
    };
#pragma unroll
    for (int n = 0; n < NR; ++n) read_one(0, 0, n);
    SB;
#pragma unroll
    for (int k0 = 0; k0 < 8; ++k0) {
#pragma unroll
      for (int m = 0; m < NS; ++m) {
        if (m < NM) mma_one(k0 & 1, m);
        if (m < NR && k0 < 7) read_one((k0 + 1) & 1, k0 + 1, m);
        if (j < 3 && k0 < 5 && m == 0) tab_read(j + 1, k0);
        if (j < 3 && k0 < 5 && m == NS - 1) tab_write(j + 1, k0);
        if (j < 2 && k0 >= 4 && m == NS / 2) make_u_d(j + 2, k0 - 4);
        SB;
      }
    }
    if constexpr (j < 2) __syncthreads();
  });
  STAMP(9);
#pragma unroll
  for (int n = 0; n < 8; ++n) {
    const int s = 256 * n + 16 * r + 4 * g;
    const u32x2 uu = *(const u32x2*)(usw + 64 + 2 * s);
    float t[4];
    t[0] = round_bf(acc[n][0]) + round_bf(bf_lo(uu[0]) * dch);
    t[1] = round_bf(acc[n][1]) + round_bf(bf_hi(uu[0]) * dch);
    t[2] = round_bf(acc[n][2]) + round_bf(bf_lo(uu[1]) * dch);
    t[3] = round_bf(acc[n][3]) + round_bf(bf_hi(uu[1]) * dch);
    u32x2 o = {pk_bf16(t[0], t[1]), pk_bf16(t[2], t[3])};
    *(u32x2*)(tT + (size_t)ch * S_ + s) = o;
  }
}

__global__ __launch_bounds__(512, 2) void k1_kernel(const u16* __restrict__ x, const u16* __restrict__ kin,
                                                     const u16* __restrict__ vin, const u16* __restrict__ shk,
                                                     const u16* __restrict__ ssmk, const u16* __restrict__ ssmd,
                                                     const u16* __restrict__ wg, const u16* __restrict__ wu,
                                                     const u16* __restrict__ wd,
                                                     u16* __restrict__ hbuf, u16* __restrict__ tT,
                                                     float2* __restrict__ stats) {
  __shared__ __attribute__((aligned(16))) char smem[K1_LDS];
  const int b = blockIdx.x, xcd = b & 7, idx = b >> 3;
  STAMP_DECL;
  STAMP(0);
  if (idx < 8) {
    conv_block(smem, kin, vin, shk, ssmk, ssmd, tT, xcd * 8 + idx STAMP_PASS);
  } else if (idx < 30) {
    const int t = 22 * (xcd & 3) + (idx - 8);
    gemm1_block(smem, x, wg, wu, hbuf, stats, 8 * (xcd >> 2) + (t & 7), t >> 3 STAMP_PASS);
  } else {
    const int part = (idx - 30) * 8 + xcd;
    for (int i = threadIdx.x; i < 688; i += 512) touch(wd + (size_t)(part * 688 + i) * 64, smem + K1_DUMMY);
    wait_vm<0>();
  }
  STAMP(15);
  STAMP_FLUSH(0);
}

constexpr int K2_NB = 4;
constexpr int K2_STAGE = 32768;
constexpr int K2_TT = K2_NB * K2_STAGE;
constexpr int K2_X = K2_TT + 8192;
constexpr int K2_ST = K2_X + 8192;
constexpr int K2_DUMMY = K2_ST + 1024;
constexpr int K2_LDS = K2_DUMMY + 256;
constexpr int K2_NT = FP_ / 128;

__global__ __launch_bounds__(512, 2) void k2_kernel(const u16* __restrict__ x, const u16* __restrict__ hbuf,
                                                     const u16* __restrict__ wd, const u16* __restrict__ tT,
                                                     const float2* __restrict__ stats, u16* __restrict__ out) {
  __shared__ __attribute__((aligned(16))) char smem[K2_LDS];
  const int tid = threadIdx.x, lane = tid & 63;
  const int w = __builtin_amdgcn_readfirstlane(tid >> 6);
  const int kg = w >> 2, wm = (w >> 1) & 1, wn = w & 1;
  const int r = lane & 15, g = lane >> 4, q = (lane & 15) >> 2, p = lane & 3;
  const int b = blockIdx.x, xcd = b & 7, idx = b >> 3;
  const int row0 = (4 * xcd + (idx >> 3)) * 64, d0 = (idx & 7) * 64;
  STAMP_DECL;
  STAMP(0);

  const u16* asrc[2];
  int wrow[2], wcol[2];
#pragma unroll
  for (int e = 0; e < 2; ++e) {
    const int ar = 8 * w + 4 * e + (lane >> 4);
    asrc[e] = hbuf + (size_t)(row0 + ar) * FP_ + (((lane & 15) ^ (ar & 15)) << 3);
    const int kr = 16 * w + 8 * e + (lane >> 3);
    const int phi_s = ((kr >> 1) & 1) | (((kr >> 3) & 1) << 1);
    wrow[e] = kr;
    wcol[e] = d0 + ((((((lane & 7) >> 1) ^ phi_s) << 1) | (lane & 1)) << 3);
  }
  auto stage = [&](int kt) {
    char* sb = smem + (kt & (K2_NB - 1)) * K2_STAGE;
#pragma unroll
    for (int e = 0; e < 2; ++e) glds16(asrc[e] + kt * 128, sb + (2 * w + e) * 1024);
#pragma unroll
    for (int e = 0; e < 2; ++e) {
      int kr = kt * 128 + wrow[e];
      kr = kr > F_ - 1 ? F_ - 1 : kr;
      glds16(wd + (size_t)kr * D_ + wcol[e], sb + 16384 + (2 * w + e) * 1024);
    }
  };
  stage(0);
  {
    const bool ish = tid < 176;
    const int hr = tid / 22, hl = tid % 22;
    int kr = 344 * (idx >> 3) + (tid - 176);
    kr = kr > F_ - 1 ? F_ - 1 : kr;
    const u16* ph = hbuf + (size_t)(row0 + 8 * (idx & 7) + (hr & 7)) * FP_ + hl * 64;
    const u16* pw = wd + (size_t)kr * D_ + d0;
    touch(ish ? ph : pw, smem + K2_DUMMY);
  }
  stage(1);
  stage(2);

  const int erow = row0 + 32 * wm + 16 * kg + r;
  const int edoff = d0 + 32 * wn + 16 * (g & 1) + 8 * (g >> 1);

  int aoff[2][2], boff[2];
#pragma unroll
  for (int i = 0; i < 2; ++i)
#pragma unroll
    for (int hs = 0; hs < 2; ++hs)
      aoff[i][hs] = (32 * wm + 16 * i + r) * 256 + (((4 * (kg + 2 * hs) + g) ^ r) << 4);
  const int phiL = ((q >> 1) & 1) | ((g & 1) << 1);
#pragma unroll
  for (int T = 0; T < 2; ++T)
    boff[T] = 16384 + (32 * kg + 8 * g + q) * 128 + (((2 * wn + T) ^ phiL) << 5) + p * 8;

  f32x4 acc[2][2];
#pragma unroll
  for (int T = 0; T < 2; ++T)
#pragma unroll
    for (int i = 0; i < 2; ++i) acc[T][i] = f32x4{0.f, 0.f, 0.f, 0.f};

  bf16x8 hA[2][2], wA[2][2], hB[2][2], wB[2][2];
  auto read_set = [&](int kt, bf16x8 (&hf)[2][2], bf16x8 (&wf)[2][2]) {
    const char* sb = smem + (kt & (K2_NB - 1)) * K2_STAGE;
#pragma unroll
    for (int hs = 0; hs < 2; ++hs) {
#pragma unroll
      for (int T = 0; T < 2; ++T) wf[hs][T] = cat8(tr_read(sb + boff[T] + hs * 8192), tr_read(sb + boff[T] + hs * 8192 + 512));
#pragma unroll
      for (int i = 0; i < 2; ++i) hf[hs][i] = *(const bf16x8*)(sb + aoff[i][hs]);
    }
  };
  auto mma_set = [&](const bf16x8 (&hf)[2][2], const bf16x8 (&wf)[2][2]) {
#pragma unroll
    for (int hs = 0; hs < 2; ++hs)
#pragma unroll
      for (int T = 0; T < 2; ++T)
#pragma unroll
        for (int i = 0; i < 2; ++i) acc[T][i] = mfma16(wf[hs][T], hf[hs][i], acc[T][i]);
  };
  wait_vm<9>();
  raw_barrier();
  STAMP(1);
  read_set(0, hA, wA);
  stage(3);
#pragma unroll 1
  for (int kt = 1; kt < 9; kt += 2) {
    wait_vm<8>();
    raw_barrier();
    read_set(kt, hB, wB);
    __builtin_amdgcn_sched_barrier(0);
    mma_set(hA, wA);
    stage(kt + 3);
    __builtin_amdgcn_sched_barrier(0);
    wait_vm<8>();
    raw_barrier();
    read_set(kt + 1, hA, wA);
    __builtin_amdgcn_sched_barrier(0);
    mma_set(hB, wB);
    if (kt < 7) stage(kt + 4);
    __builtin_amdgcn_sched_barrier(0);
  }
  glds16(tT + (size_t)(d0 + 8 * w + (lane >> 3)) * S_ + row0 + (lane & 7) * 8, smem + K2_TT + w * 1024);
  glds16(x + (size_t)(row0 + 8 * w + (lane >> 3)) * D_ + d0 + (lane & 7) * 8, smem + K2_X + w * 1024);
  glds16(stats + row0 + 2 * (lane & 31), smem + K2_ST);
  wait_vm<7>();
  raw_barrier();
  STAMP(10);
  read_set(9, hB, wB);
  __builtin_amdgcn_sched_barrier(0);
  mma_set(hA, wA);
  __builtin_amdgcn_sched_barrier(0);
  wait_vm<3>();
  raw_barrier();
  STAMP(11);
  read_set(10, hA, wA);
  __builtin_amdgcn_sched_barrier(0);
  mma_set(hB, wB);
  __builtin_amdgcn_sched_barrier(0);
  mma_set(hA, wA);
  STAMP(12);
  wait_vm<0>();
  raw_barrier();
  const int lrow = 32 * wm + 16 * kg + r;
  const u32x4 xv = *(const u32x4*)(smem + K2_X + lrow * 128 + (32 * wn + 16 * (g & 1) + 8 * (g >> 1)) * 2);
  const f32x2 stv = *(const f32x2*)(smem + K2_ST + lrow * 8);
  {
    f32x4* xb = (f32x4*)smem;
    const int wq = w & 3;
#pragma unroll
    for (int T = 0; T < 2; ++T) xb[((kg * 4 + wq) * 2 + T) * 64 + lane] = kg ? acc[T][0] : acc[T][1];
    raw_barrier();
    f32x4 fin[2];
#pragma unroll
    for (int T = 0; T < 2; ++T) {
      const f32x4 o = xb[(((kg ^ 1) * 4 + wq) * 2 + T) * 64 + lane];
      fin[T] = (kg ? acc[T][1] : acc[T][0]) + o;
    }
    unsigned pb[2][2], pt[2][2];
#pragma unroll
    for (int T = 0; T < 2; ++T) {
      pb[T][0] = pk_bf16(fin[T][0], fin[T][1]);
      pb[T][1] = pk_bf16(fin[T][2], fin[T][3]);
      const s16x4 tv = tr_read(smem + K2_TT + (32 * wn + 16 * T + 4 * g + q) * 128 + (32 * wm + 16 * kg + 4 * p) * 2);
      const u32x2 tu = __builtin_bit_cast(u32x2, tv);
      pt[T][0] = tu[0];
      pt[T][1] = tu[1];
    }
    auto b0 = __builtin_amdgcn_permlane16_swap(pb[0][0], pb[1][0], false, false);
    auto b1 = __builtin_amdgcn_permlane16_swap(pb[0][1], pb[1][1], false, false);
    auto t0 = __builtin_amdgcn_permlane16_swap(pt[0][0], pt[1][0], false, false);
    auto t1 = __builtin_amdgcn_permlane16_swap(pt[0][1], pt[1][1], false, false);
    const u32x4 bv = {b0[0], b1[0], b0[1], b1[1]};
    const u32x4 tv = {t0[0], t1[0], t0[1], t1[1]};
    u32x4 o;
#pragma unroll
    for (int j = 0; j < 4; ++j) {
      float res[2];
#pragma unroll
      for (int hh = 0; hh < 2; ++hh) {
        const float xe = hh ? bf_hi(xv[j]) : bf_lo(xv[j]);
        const float te = hh ? bf_hi(tv[j]) : bf_lo(tv[j]);
        const float be = hh ? bf_hi(bv[j]) : bf_lo(bv[j]);
        const float xn = round_bf(round_bf(xe - stv[0]) * stv[1]);
        const float a = round_bf(xn * te);
        res[hh] = round_bf(xe + a) + be;
      }
      o[j] = pk_bf16(res[0], res[1]);
    }
    *(u32x4*)(out + (size_t)erow * D_ + edoff) = o;
  }
  STAMP(15);
  STAMP_FLUSH(1);
}

extern "C" void kernel_launch(void* const* d_in, const int* in_sizes, int n_in, void* d_out, int out_size,
                              void* d_ws, size_t ws_size, hipStream_t stream) {
  const u16* q = (const u16*)d_in[0];
  const u16* k = (const u16*)d_in[1];
  const u16* v = (const u16*)d_in[2];
  const u16* shk = (const u16*)d_in[3];
  const u16* ssmk = (const u16*)d_in[4];
  const u16* ssmd = (const u16*)d_in[5];
  const u16* wg = (const u16*)d_in[7];
  const u16* wu = (const u16*)d_in[8];
  const u16* wd = (const u16*)d_in[9];
  char* ws = (char*)d_ws;
  u16* hbuf = (u16*)(ws + WS_H);
  u16* tT = (u16*)(ws + WS_T);
  float2* stats = (float2*)(ws + WS_ST);
  k1_kernel<<<256, 512, 0, stream>>>(q, k, v, shk, ssmk, ssmd, wg, wu, wd, hbuf, tT, stats);
  k2_kernel<<<256, 512, 0, stream>>>(q, hbuf, wd, tT, stats, (u16*)d_out);
}
```

```cpp
#include <hip/hip_runtime.h>
#include <hip/hip_bf16.h>
#include <stdint.h>

typedef __attribute__((ext_vector_type(8))) short bf16x8;
typedef __attribute__((ext_vector_type(4))) short s16x4;
typedef __attribute__((ext_vector_type(2))) __bf16 bf16x2;
typedef __attribute__((ext_vector_type(4))) float f32x4;
typedef __attribute__((ext_vector_type(2))) float f32x2;
typedef __attribute__((ext_vector_type(4))) unsigned u32x4;
typedef __attribute__((ext_vector_type(2))) unsigned u32x2;
typedef __attribute__((address_space(3))) s16x4* lds_s16x4_ptr;
typedef unsigned short u16;

#define DI __device__ __forceinline__

constexpr int S_ = 2048, D_ = 512, F_ = 1376, FP_ = 1408;

constexpr size_t WS_H = 0;
constexpr size_t WS_T = 6u * 1024 * 1024;
constexpr size_t WS_ST = 8u * 1024 * 1024 + 512 * 1024;

template <int N> DI void wait_vm() { asm volatile("s_waitcnt vmcnt(%0)" ::"i"(N) : "memory"); }
DI void wait_lgkm0() { asm volatile("s_waitcnt lgkmcnt(0)" ::: "memory"); }
DI void raw_barrier() {
  asm volatile("s_waitcnt lgkmcnt(0)" ::: "memory");
  __builtin_amdgcn_s_barrier();
  asm volatile("" ::: "memory");
}

DI unsigned pk_bf16(float lo, float hi) {
  f32x2 f = {lo, hi};
  return __builtin_bit_cast(unsigned, __builtin_convertvector(f, bf16x2));
}
DI float bf_lo(unsigned v) { return __uint_as_float(v << 16); }
DI float bf_hi(unsigned v) { return __uint_as_float(v & 0xffff0000u); }
DI float round_bf(float x) { return bf_lo(pk_bf16(x, x)); }
DI float dot2bf(unsigned a, unsigned b, float c) {
  return __builtin_amdgcn_fdot2_f32_bf16(__builtin_bit_cast(bf16x2, a), __builtin_bit_cast(bf16x2, b), c, false);
}
DI s16x4 tr_read(const char* p) {
  return __builtin_amdgcn_ds_read_tr16_b64_v4i16((lds_s16x4_ptr)p);
}
DI bf16x8 cat8(s16x4 lo, s16x4 hi) { return __builtin_shufflevector(lo, hi, 0, 1, 2, 3, 4, 5, 6, 7); }
DI f32x4 mfma16(bf16x8 a, bf16x8 b, f32x4 c) { return __builtin_amdgcn_mfma_f32_16x16x32_bf16(a, b, c, 0, 0, 0); }

DI void glds16(const void* g, char* lds) {
  unsigned keep;
  const unsigned dst = __builtin_amdgcn_readfirstlane((unsigned)(size_t)lds);
  asm volatile("s_mov_b32 %0, m0\n\ts_mov_b32 m0, %2\n\ts_nop 0\n\tglobal_load_lds_dwordx4 %1, off\n\ts_mov_b32 m0, %0"
               : "=&s"(keep) : "v"(g), "s"(dst) : "memory");
}
DI void glds16s(const void* sbase, unsigned voff, char* lds) {
  unsigned keep;
  const unsigned dst = __builtin_amdgcn_readfirstlane((unsigned)(size_t)lds);
  asm volatile("s_mov_b32 %0, m0\n\ts_mov_b32 m0, %3\n\ts_nop 0\n\tglobal_load_lds_dwordx4 %1, %2\n\ts_mov_b32 m0, %0"
               : "=&s"(keep) : "v"(voff), "s"(sbase), "s"(dst) : "memory");
}
DI float mul_s(float a, float s) { float d; asm("v_mul_f32 %0, %2, %1" : "=v"(d) : "v"(a), "s"(s)); return d; }
DI float fma_s(float a, float s, float c) { float d; asm("v_fma_f32 %0, %1, %2, %3" : "=v"(d) : "v"(a), "s"(s), "v"(c)); return d; }
DI float mul_v(float a, float b) { float d; asm("v_mul_f32 %0, %1, %2" : "=v"(d) : "v"(a), "v"(b)); return d; }
DI void touch(const void* g, char* dummy) {
  unsigned keep;
  const unsigned dst = __builtin_amdgcn_readfirstlane((unsigned)(size_t)dummy);
  asm volatile("s_mov_b32 %0, m0\n\ts_mov_b32 m0, %2\n\ts_nop 0\n\tglobal_load_lds_dword %1, off\n\ts_mov_b32 m0, %0"
               : "=&s"(keep) : "v"(g), "s"(dst) : "memory");
}

#define WT_MODE 1
DI void store16_wt(void* p, u32x4 v) {
  asm volatile("global_store_dwordx4 %0, %1, off sc1\n\ts_nop 1" ::"v"(p), "v"(v) : "memory");
}
DI void store8_wt(void* p, u32x2 v) {
  asm volatile("global_store_dwordx2 %0, %1, off sc1\n\ts_nop 1" ::"v"(p), "v"(v) : "memory");
}

#define STAMP_DECL do {} while (0)
#define STAMP(I) do {} while (0)
#define STAMP_FLUSH(K) do {} while (0)
#define STAMP_ARG
#define STAMP_PASS

template <int I, int N, class Fn> DI void static_for(Fn&& fn) {
  if constexpr (I < N) {
    fn(std::integral_constant<int, I>{});
    static_for<I + 1, N>(fn);
  }
}

constexpr int K1_STAGE = 49152;
constexpr int K1_DUMMY = 151808;
constexpr int K1_LDS = K1_DUMMY + 256;

constexpr int US_PITCH = 4160;
constexpr int HQ_PITCH = 4576;
constexpr int FB_PITCH = 5120;
constexpr int CV_US = 0;
constexpr int CV_HQ = 8 * US_PITCH;
constexpr int CV_FB = CV_HQ + 8 * HQ_PITCH;
static_assert(CV_FB + 16 * FB_PITCH <= K1_DUMMY && 3 * K1_STAGE <= K1_DUMMY, "lds layout");

constexpr int G1_A = 0;
constexpr int G1_W = 49152;
#define SB __builtin_amdgcn_sched_barrier(0)

DI void gemm1_block(char* smem, const u16* __restrict__ x, const u16* __restrict__ wg, const u16* __restrict__ wu,
                    u16* __restrict__ hbuf, float2* __restrict__ stats, int mt, int ft, int rank STAMP_ARG) {
  const int tid = threadIdx.x, lane = tid & 63;
  const int w = __builtin_amdgcn_readfirstlane(tid >> 6);
  const int wm = w >> 2, wn = w & 3;
  const int r = lane & 15, g = lane >> 4, q = (lane & 15) >> 2, p = lane & 3;
  const int row0 = mt * 128, f0 = ft * 128;

  unsigned aoffs[2], woffs[2];
#pragma unroll
  for (int e = 0; e < 2; ++e) {
    const int i = 2 * w + e;
    const int row = 8 * i + (lane >> 3);
    aoffs[e] = (unsigned)((row0 + row) * D_ + (((lane & 7) ^ (row & 7)) << 3)) * 2u;
    const int kr = 4 * i + (lane >> 4);
    const int phi = (kr & 3) | (((kr >> 3) & 1) << 2);
    const int sc = ((((lane & 15) >> 1) ^ phi) << 1) | (lane & 1);
    int col = f0 + sc * 8;
    col = col > F_ - 8 ? F_ - 8 : col;
    woffs[e] = (unsigned)(kr * F_ + col) * 2u;
  }
  auto dma_piece = [&](int kt, int n) {
    const int b = kt % 3, e = n & 1;
    if (n < 2) glds16s(x + kt * 64, aoffs[e], smem + G1_A + b * 16384 + (2 * w + e) * 1024);
    else if (n < 4) glds16s(wg + kt * 64 * F_, woffs[e], smem + G1_W + b * 32768 + (2 * w + e) * 1024);
    else glds16s(wu + kt * 64 * F_, woffs[e], smem + G1_W + b * 32768 + 16384 + (2 * w + e) * 1024);
  };
#pragma unroll
  for (int kt = 0; kt < 3; ++kt)
#pragma unroll
    for (int n = 0; n < 6; ++n) dma_piece(kt, n);

  int aoff[4][2];
#pragma unroll
  for (int i = 0; i < 4; ++i)
#pragma unroll
    for (int ks = 0; ks < 2; ++ks)
      aoff[i][ks] = G1_A + (64 * wm + 16 * (i ^ wn) + r) * 128 + (((4 * ks + g) ^ (r & 7)) << 4);
  const int phiL = q | ((g & 1) << 2);
  int boff[4];
#pragma unroll
  for (int T = 0; T < 4; ++T) {
    const int mat = (T >> 1) ^ wm;
    boff[T] = G1_W + mat * 16384 + (8 * g + q) * 256 + (((2 * wn + (T & 1)) ^ phiL) << 5) + p * 8;
  }

  f32x4 acc[4][4];
#pragma unroll
  for (int T = 0; T < 4; ++T)
#pragma unroll
    for (int i = 0; i < 4; ++i) acc[T][i] = f32x4{0.f, 0.f, 0.f, 0.f};
  f32x4 accS = {0.f, 0.f, 0.f, 0.f}, accR = accS, accC[2] = {accS, accS};
  const bf16x8 ones = {0x3f80, 0x3f80, 0x3f80, 0x3f80, 0x3f80, 0x3f80, 0x3f80, 0x3f80};

  struct Frags { s16x4 wl[4], wh[4]; bf16x8 xf[4]; };
  Frags fa, fb2;
  auto read_piece = [&](Frags& f, int kt, int ks, int n) {
    const int b = kt % 3;
    if (n < 4) f.xf[n] = *(const bf16x8*)(smem + b * 16384 + aoff[n][ks]);
    else {
      const int T = (n - 4) >> 1;
      if ((n & 1) == 0) f.wl[T] = tr_read(smem + b * 32768 + ks * 8192 + boff[T]);
      else f.wh[T] = tr_read(smem + b * 32768 + ks * 8192 + 1024 + boff[T]);
    }
  };
  auto mma_one = [&](const Frags& f, int m) {
    const int T = m / 5, i = m % 5;
    if (i < 4) acc[T][i] = mfma16(cat8(f.wl[T], f.wh[T]), f.xf[i], acc[T][i]);
    else if (T == 0) accS = mfma16(f.xf[0], f.xf[0], accS);
    else if (T == 1) accR = mfma16(ones, f.xf[0], accR);
    else if (T == 2) accC[0] = mfma16(cat8(f.wl[0], f.wh[0]), ones, accC[0]);
    else accC[1] = mfma16(cat8(f.wl[1], f.wh[1]), ones, accC[1]);
  };

  wait_vm<12>();
  raw_barrier();
  STAMP(1);
  {
    touch(x + (size_t)(row0 + 64 * (rank & 1) + (tid >> 3)) * D_ + (tid & 7) * 64, smem + K1_DUMMY);
    const int kr = 64 * (mt & 7) + (tid >> 3);
    const int sel = tid & 7, pos = sel % 3;
    int col = f0 + (pos == 0 ? 0 : pos == 1 ? 64 : 126);
    col = col > F_ - 2 ? F_ - 2 : col;
    touch((((sel / 3) & 1) ? wu : wg) + kr * F_ + col, smem + K1_DUMMY);
  }
#pragma unroll
  for (int n = 0; n < 12; ++n) read_piece(fa, 0, 0, n);
  static_for<0, 8>([&](auto KT) {
    constexpr int kt = KT.value;
    SB;
#pragma unroll
    for (int m = 0; m < 16; ++m) {
      mma_one(fa, m);
      if (m < 12) read_piece(fb2, kt, 1, m);
      SB;
    }
    if constexpr (kt < 7) {
      if constexpr (kt < 2) wait_vm<8>(); else if constexpr (kt < 6) wait_vm<6>(); else wait_vm<0>();
      raw_barrier();
      STAMP(2 + kt);
      SB;
    }
#pragma unroll
    for (int m = 16; m < 20; ++m) {
      mma_one(fa, m);
      if (m == 17 && kt < 5) dma_piece(kt + 3, 0);
      SB;
    }
#pragma unroll
    for (int m = 0; m < 20; ++m) {
      mma_one(fb2, m);
      if (m < 12 && kt < 7) read_piece(fa, kt + 1, 0, m);
      if ((m == 1 || m == 5 || m == 9 || m == 13 || m == 17) && kt < 5) dma_piece(kt + 3, 1 + (m >> 2));
      SB;
    }
  });
  __syncthreads();
  STAMP(9);
  float2* st_s = (float2*)smem;
  float* cs_s = (float*)(smem + 1024);
  if (g == (r >> 2)) {
    const int rr = r & 3;
    const float s2 = rr == 0 ? accS[0] : rr == 1 ? accS[1] : rr == 2 ? accS[2] : accS[3];
    const float mean = accR[0] * (1.f / D_);
    float var = s2 * (1.f / D_) - mean * mean;
    var = var < 0.f ? 0.f : var;
    const float mb = round_bf(mean);
    const float vb = round_bf(var);
    const float sd = round_bf(sqrtf(round_bf(vb + 1e-5f)));
    const float2 stv = make_float2(mb, 1.f / sd);
    const int row = 64 * wm + 16 * wn + r;
    st_s[row] = stv;
    if (ft == 0) stats[row0 + row] = stv;
  }
  if (r == 0) {
    *(f32x4*)(cs_s + wm * 128 + 32 * wn + 4 * g) = accC[0];
    *(f32x4*)(cs_s + wm * 128 + 32 * wn + 16 + 4 * g) = accC[1];
  }
  __syncthreads();
  const bool valid = (f0 + 32 * wn) < F_;
  f32x4 ca[2], cb[2];
#pragma unroll
  for (int T = 0; T < 2; ++T) {
    ca[T] = accC[T];
    cb[T] = *(const f32x4*)(cs_s + (wm ^ 1) * 128 + 32 * wn + 16 * T + 4 * g);
  }
#pragma unroll
  for (int i = 0; i < 4; ++i) {
    const int row = 64 * wm + 16 * (i ^ wn) + r;
    const float2 stv = st_s[row];
    const float nm = -stv.x, r2 = stv.y * stv.y;
    unsigned pk[2][2];
#pragma unroll
    for (int T = 0; T < 2; ++T) {
      const f32x4 ga = acc[T][i] + nm * ca[T];
      const f32x4 gb = acc[2 + T][i] + nm * cb[T];
      const f32x4 hv = (ga * r2) * gb;
      pk[T][0] = pk_bf16(hv[0], hv[1]);
      pk[T][1] = pk_bf16(hv[2], hv[3]);
    }
    auto r0 = __builtin_amdgcn_permlane16_swap(pk[0][0], pk[1][0], false, false);
    auto r1 = __builtin_amdgcn_permlane16_swap(pk[0][1], pk[1][1], false, false);
    u32x4 o = {r0[0], r1[0], r0[1], r1[1]};
    if (!valid) o = u32x4{0u, 0u, 0u, 0u};
    store16_wt(hbuf + (size_t)(row0 + row) * FP_ + f0 + 32 * wn + 16 * (g & 1) + 8 * (g >> 1), o);
  }
}

DI void conv_block(char* smem, const u16* __restrict__ kin, const u16* __restrict__ vin,
                   const u16* __restrict__ shk, const u16* __restrict__ ssmk, const u16* __restrict__ ssmd,
                   u16* __restrict__ tT, int cg STAMP_ARG) {
  const int tid = threadIdx.x, lane = tid & 63;
  const int w = __builtin_amdgcn_readfirstlane(tid >> 6);
  const int r = lane & 15, g = lane >> 4;
  const int c0 = cg * 8, ch = c0 + w;

  u32x4 hx[4];
#pragma unroll
  for (int i = 0; i < 4; ++i) hx[i] = *(const u32x4*)(ssmk + (size_t)ch * S_ + (i * 64 + lane) * 8);
  touch(((tid & 256) ? vin : kin) + (size_t)(256 * (cg & 7) + (tid & 255)) * D_ + (cg >> 3) * 64, smem + K1_DUMMY);
  float wsh[8][4];
#pragma unroll
  for (int c = 0; c < 8; ++c) {
    const u32x2 t = *(const u32x2*)(shk + (c0 + c) * 4);
    const unsigned t0 = __builtin_amdgcn_readfirstlane(t[0]), t1 = __builtin_amdgcn_readfirstlane(t[1]);
    wsh[c][0] = bf_lo(t0); wsh[c][1] = bf_hi(t0); wsh[c][2] = bf_lo(t1); wsh[c][3] = bf_hi(t1);
  }
  const float dch = __uint_as_float((unsigned)ssmd[ch] << 16);
  wait_vm<0>();

  u32x4 kx[4], vx[4], hal[4][3];
  auto loads = [&](int j) {
    const int s = 512 * j + tid;
    kx[j] = *(const u32x4*)(kin + (size_t)s * D_ + c0);
    vx[j] = *(const u32x4*)(vin + (size_t)s * D_ + c0);
#pragma unroll
    for (int d = 0; d < 3; ++d) {
      const int sr = 512 * j + 64 * w - 1 - d;
      hal[j][d] = *(const u32x4*)(kin + (size_t)(sr < 0 ? 0 : sr) * D_ + c0);
      if (j == 0 && sr < 0) hal[j][d] = u32x4{0u, 0u, 0u, 0u};
    }
  };
  loads(0);
  loads(1);

  char* us = smem + CV_US;
  char* hq = smem + CV_HQ + w * HQ_PITCH;
  char* fb = smem + CV_FB + w * (2 * FB_PITCH);
  if (lane < 30) *(u32x4*)(hq + lane * 16) = u32x4{0u, 0u, 0u, 0u};
  if (lane < 4) *(u32x4*)(us + w * US_PITCH + lane * 16) = u32x4{0u, 0u, 0u, 0u};
#pragma unroll
  for (int i = 0; i < 4; ++i) *(u32x4*)(hq + 480 + (i * 64 + lane) * 16) = hx[i];

  auto make_u_d = [&](int j, int d) {
    const int s = 512 * j + tid;
    const unsigned k0 = kx[j][d];
    const unsigned k1 = (unsigned)__builtin_amdgcn_update_dpp((int)hal[j][0][d], (int)k0, 0x138, 0xf, 0xf, false);
    const unsigned k2 = (unsigned)__builtin_amdgcn_update_dpp((int)hal[j][1][d], (int)k1, 0x138, 0xf, 0xf, false);
    const unsigned k3 = (unsigned)__builtin_amdgcn_update_dpp((int)hal[j][2][d], (int)k2, 0x138, 0xf, 0xf, false);
    const int ca = 2 * d, cb = 2 * d + 1;
    float a = mul_s(bf_lo(k3), wsh[ca][3]);
    a = fma_s(bf_lo(k2), wsh[ca][2], a);
    a = fma_s(bf_lo(k1), wsh[ca][1], a);
    a = fma_s(bf_lo(k0), wsh[ca][0], a);
    float b = mul_s(bf_hi(k3), wsh[cb][3]);
    b = fma_s(bf_hi(k2), wsh[cb][2], b);
    b = fma_s(bf_hi(k1), wsh[cb][1], b);
    b = fma_s(bf_hi(k0), wsh[cb][0], b);
    const unsigned pk = pk_bf16(mul_v(a, bf_lo(vx[j][d])), mul_v(b, bf_hi(vx[j][d])));
    *(u16*)(us + ca * US_PITCH + 64 + 2 * s) = (u16)(pk & 0xffffu);
    *(u16*)(us + cb * US_PITCH + 64 + 2 * s) = (u16)(pk >> 16);
  };
  const char* usw = us + w * US_PITCH;
  unsigned td[3];
  auto tab_read = [&](int j, int it) {
    const unsigned* src = (const unsigned*)(usw + 2 * (512 * j + 16 + 2 * (it * 64 + lane)));
    td[2] = src[0]; td[1] = src[-1]; td[0] = src[-2];
  };
  auto tab_write = [&](int j, int it) {
    u32x4 e;
    e[0] = (td[2] & 0xffffu) | (td[1] & 0xffff0000u);
    e[1] = (td[1] & 0xffffu) | (td[0] & 0xffff0000u);
    e[2] = (td[2] >> 16) | (td[2] << 16);
    e[3] = (td[1] >> 16) | (td[1] << 16);
    *(u32x4*)(fb + (j & 1) * FB_PITCH + (it * 64 + lane) * 16) = e;
  };

  f32x4 acc[8];
#pragma unroll
  for (int n = 0; n < 8; ++n) acc[n] = f32x4{0.f, 0.f, 0.f, 0.f};

  const char* a_lane = fb + (32 + r - 8 * g) * 8;
  const char* b_lane = hq + 448 + 32 * r + 16 * g;

  STAMP(8);
#pragma unroll
  for (int d = 0; d < 4; ++d) make_u_d(0, d);
  loads(2);
  STAMP(10);
  raw_barrier();
  STAMP(11);
  {
    unsigned t5[5][3];
#pragma unroll
    for (int it = 0; it < 5; ++it) { tab_read(0, it); t5[it][0] = td[0]; t5[it][1] = td[1]; t5[it][2] = td[2]; }
#pragma unroll
    for (int it = 0; it < 5; ++it) { td[0] = t5[it][0]; td[1] = t5[it][1]; td[2] = t5[it][2]; tab_write(0, it); }
  }
  loads(3);

  static_for<0, 4>([&](auto J) {
    constexpr int j = J.value;
    constexpr int NNU = 8 - 2 * j;
    constexpr int NM = 2 * NNU - 1;
    constexpr int NR = NNU + 4;
    constexpr int NS = NM > NR ? NM : NR;
    STAMP(1 + j);
    s16x4 al[2][2], ah[2][2];
    bf16x8 bfr[2][NNU];
    auto read_one = [&](int set, int k0, int n) {
      if (n < 4) {
        const char* a = a_lane + (j & 1) * FB_PITCH + (8 * (n >> 1) + k0) * 256;
        if ((n & 1) == 0) al[set][n >> 1] = *(const s16x4*)a; else ah[set][n >> 1] = *(const s16x4*)(a - 32);
      } else {
        bfr[set][n - 4] = *(const bf16x8*)(b_lane + 512 * (n - 4) - 64 * k0);
      }
    };
    auto mma_one = [&](int set, int m) {
      if (m < NNU) acc[2 * j + m] = mfma16(cat8(al[set][0], ah[set][0]), bfr[set][m], acc[2 * j + m]);
      else acc[2 * j + 1 + (m - NNU)] = mfma16(cat8(al[set][1], ah[set][1]), bfr[set][m - NNU], acc[2 * j + 1 + (m - NNU)]);
    };
#pragma unroll
    for (int n = 0; n < NR; ++n) read_one(0, 0, n);
    SB;
#pragma unroll
    for (int k0 = 0; k0 < 8; ++k0) {
#pragma unroll
      for (int m = 0; m < NS; ++m) {
        if (m < NM) mma_one(k0 & 1, m);
        if (m < NR && k0 < 7) read_one((k0 + 1) & 1, k0 + 1, m);
        if (j < 3 && k0 < 4 && m == NS / 2) make_u_d(j + 1, k0);
        if (j < 3 && k0 == 3 && m == NS - 1) raw_barrier();
        if (j < 3 && k0 >= 4 && m == 0) tab_read(j + 1, k0 - 4);
        if (j < 3 && k0 >= 4 && m == NS / 2) tab_write(j + 1, k0 - 4);
        if (j < 3 && k0 == 7 && m == NS / 2) tab_read(j + 1, 4);
        if (j < 3 && k0 == 7 && m == NS - 1) tab_write(j + 1, 4);
        SB;
      }
    }
  });
  STAMP(9);
#pragma unroll
  for (int n = 0; n < 8; ++n) {
    const int s = 256 * n + 16 * r + 4 * g;
    const u32x2 uu = *(const u32x2*)(usw + 64 + 2 * s);
    float t[4];
    t[0] = round_bf(acc[n][0]) + round_bf(bf_lo(uu[0]) * dch);
    t[1] = round_bf(acc[n][1]) + round_bf(bf_hi(uu[0]) * dch);
    t[2] = round_bf(acc[n][2]) + round_bf(bf_lo(uu[1]) * dch);
    t[3] = round_bf(acc[n][3]) + round_bf(bf_hi(uu[1]) * dch);
    u32x2 o = {pk_bf16(t[0], t[1]), pk_bf16(t[2], t[3])};
    store8_wt(tT + (size_t)ch * S_ + s, o);
  }
}

__global__ __launch_bounds__(512, 2) void k1_kernel(const u16* __restrict__ x, const u16* __restrict__ kin,
                                                     const u16* __restrict__ vin, const u16* __restrict__ shk,
                                                     const u16* __restrict__ ssmk, const u16* __restrict__ ssmd,
                                                     const u16* __restrict__ wg, const u16* __restrict__ wu,
                                                     const u16* __restrict__ wd,
                                                     u16* __restrict__ hbuf, u16* __restrict__ tT,
                                                     float2* __restrict__ stats) {
  __shared__ __attribute__((aligned(16))) char smem[K1_LDS];
  const int b = blockIdx.x, xcd = b & 7, idx = b >> 3;
  STAMP_DECL;
  STAMP(0);
  if (idx < 8) {
    conv_block(smem, kin, vin, shk, ssmk, ssmd, tT, xcd * 8 + idx STAMP_PASS);
  } else if (idx < 30) {
    const int t = 22 * (xcd & 3) + (idx - 8);
    gemm1_block(smem, x, wg, wu, hbuf, stats, 8 * (xcd >> 2) + (t & 7), t >> 3, (idx - 8) >> 3 STAMP_PASS);
  } else {
    const int part = (idx - 30) * 8 + xcd;
    __builtin_amdgcn_s_sleep(127);
    for (int i = threadIdx.x; i < 688; i += 512) touch(wd + (size_t)(part * 688 + i) * 64, smem + K1_DUMMY);
    wait_vm<0>();
  }
  STAMP(15);
  STAMP_FLUSH(0);
}

constexpr int K2_NB = 4;
constexpr int K2_STAGE = 32768;
constexpr int K2_TT = K2_NB * K2_STAGE;
constexpr int K2_X = K2_TT + 8192;
constexpr int K2_ST = K2_X + 8192;
constexpr int K2_DUMMY = K2_ST + 1024;
constexpr int K2_LDS = K2_DUMMY + 256;
constexpr int K2_NT = FP_ / 128;

__global__ __launch_bounds__(512, 2) void k2_kernel(const u16* __restrict__ x, const u16* __restrict__ hbuf,
                                                     const u16* __restrict__ wd, const u16* __restrict__ tT,
                                                     const float2* __restrict__ stats, u16* __restrict__ out) {
  __shared__ __attribute__((aligned(16))) char smem[K2_LDS];
  const int tid = threadIdx.x, lane = tid & 63;
  const int w = __builtin_amdgcn_readfirstlane(tid >> 6);
  const int kg = w >> 2, wm = (w >> 1) & 1, wn = w & 1;
  const int r = lane & 15, g = lane >> 4, q = (lane & 15) >> 2, p = lane & 3;
  const int b = blockIdx.x, xcd = b & 7, idx = b >> 3;
  const int row0 = (4 * xcd + (idx >> 3)) * 64, d0 = (idx & 7) * 64;
  STAMP_DECL;
  STAMP(0);

  const u16* asrc[2];
  int wrow[2], wcol[2];
#pragma unroll
  for (int e = 0; e < 2; ++e) {
    const int ar = 8 * w + 4 * e + (lane >> 4);
    asrc[e] = hbuf + (size_t)(row0 + ar) * FP_ + (((lane & 15) ^ (ar & 15)) << 3);
    const int kr = 16 * w + 8 * e + (lane >> 3);
    const int phi_s = ((kr >> 1) & 1) | (((kr >> 3) & 1) << 1);
    wrow[e] = kr;
    wcol[e] = d0 + ((((((lane & 7) >> 1) ^ phi_s) << 1) | (lane & 1)) << 3);
  }
  auto stage = [&](int kt) {
    char* sb = smem + (kt & (K2_NB - 1)) * K2_STAGE;
#pragma unroll
    for (int e = 0; e < 2; ++e) glds16(asrc[e] + kt * 128, sb + (2 * w + e) * 1024);
#pragma unroll
    for (int e = 0; e < 2; ++e) {
      int kr = kt * 128 + wrow[e];
      kr = kr > F_ - 1 ? F_ - 1 : kr;
      glds16(wd + (size_t)kr * D_ + wcol[e], sb + 16384 + (2 * w + e) * 1024);
    }
  };
  stage(0);
  {
    const bool ish = tid < 176;
    const int hr = tid / 22, hl = tid % 22;
    int kr = 344 * (idx >> 3) + (tid - 176);
    kr = kr > F_ - 1 ? F_ - 1 : kr;
    const u16* ph = hbuf + (size_t)(row0 + 8 * (idx & 7) + (hr & 7)) * FP_ + hl * 64;
    const u16* pw = wd + (size_t)kr * D_ + d0;
    touch(ish ? ph : pw, smem + K2_DUMMY);
  }
  stage(1);
  stage(2);

  const int erow = row0 + 32 * wm + 16 * kg + r;
  const int edoff = d0 + 32 * wn + 16 * (g & 1) + 8 * (g >> 1);

  int aoff[2][2], boff[2];
#pragma unroll
  for (int i = 0; i < 2; ++i)
#pragma unroll
    for (int hs = 0; hs < 2; ++hs)
      aoff[i][hs] = (32 * wm + 16 * i + r) * 256 + (((4 * (kg + 2 * hs) + g) ^ r) << 4);
  const int phiL = ((q >> 1) & 1) | ((g & 1) << 1);
#pragma unroll
  for (int T = 0; T < 2; ++T)
    boff[T] = 16384 + (32 * kg + 8 * g + q) * 128 + (((2 * wn + T) ^ phiL) << 5) + p * 8;

  f32x4 acc[2][2];
#pragma unroll
  for (int T = 0; T < 2; ++T)
#pragma unroll
    for (int i = 0; i < 2; ++i) acc[T][i] = f32x4{0.f, 0.f, 0.f, 0.f};

  bf16x8 hA[2][2], wA[2][2], hB[2][2], wB[2][2];
  auto read_set = [&](int kt, bf16x8 (&hf)[2][2], bf16x8 (&wf)[2][2]) {
    const char* sb = smem + (kt & (K2_NB - 1)) * K2_STAGE;
#pragma unroll
    for (int hs = 0; hs < 2; ++hs) {
#pragma unroll
      for (int T = 0; T < 2; ++T) wf[hs][T] = cat8(tr_read(sb + boff[T] + hs * 8192), tr_read(sb + boff[T] + hs * 8192 + 512));
#pragma unroll
      for (int i = 0; i < 2; ++i) hf[hs][i] = *(const bf16x8*)(sb + aoff[i][hs]);
    }
  };
  auto mma_set = [&](const bf16x8 (&hf)[2][2], const bf16x8 (&wf)[2][2]) {
#pragma unroll
    for (int hs = 0; hs < 2; ++hs)
#pragma unroll
      for (int T = 0; T < 2; ++T)
#pragma unroll
        for (int i = 0; i < 2; ++i) acc[T][i] = mfma16(wf[hs][T], hf[hs][i], acc[T][i]);
  };
  wait_vm<9>();
  raw_barrier();
  STAMP(1);
  read_set(0, hA, wA);
  stage(3);
#pragma unroll 1
  for (int kt = 1; kt < 9; kt += 2) {
    wait_vm<8>();
    raw_barrier();
    read_set(kt, hB, wB);
    __builtin_amdgcn_sched_barrier(0);
    mma_set(hA, wA);
    stage(kt + 3);
    __builtin_amdgcn_sched_barrier(0);
    wait_vm<8>();
    raw_barrier();
    read_set(kt + 1, hA, wA);
    __builtin_amdgcn_sched_barrier(0);
    mma_set(hB, wB);
    if (kt < 7) stage(kt + 4);
    __builtin_amdgcn_sched_barrier(0);
  }
  glds16(tT + (size_t)(d0 + 8 * w + (lane >> 3)) * S_ + row0 + (lane & 7) * 8, smem + K2_TT + w * 1024);
  glds16(x + (size_t)(row0 + 8 * w + (lane >> 3)) * D_ + d0 + (lane & 7) * 8, smem + K2_X + w * 1024);
  glds16(stats + row0 + 2 * (lane & 31), smem + K2_ST);
  wait_vm<7>();
  raw_barrier();
  STAMP(10);
  read_set(9, hB, wB);
  __builtin_amdgcn_sched_barrier(0);
  mma_set(hA, wA);
  __builtin_amdgcn_sched_barrier(0);
  wait_vm<3>();
  raw_barrier();
  STAMP(11);
  read_set(10, hA, wA);
  __builtin_amdgcn_sched_barrier(0);
  mma_set(hB, wB);
  __builtin_amdgcn_sched_barrier(0);
  mma_set(hA, wA);
  STAMP(12);
  wait_vm<0>();
  raw_barrier();
  const int lrow = 32 * wm + 16 * kg + r;
  const u32x4 xv = *(const u32x4*)(smem + K2_X + lrow * 128 + (32 * wn + 16 * (g & 1) + 8 * (g >> 1)) * 2);
  const f32x2 stv = *(const f32x2*)(smem + K2_ST + lrow * 8);
  {
    f32x4* xb = (f32x4*)smem;
    const int wq = w & 3;
#pragma unroll
    for (int T = 0; T < 2; ++T) xb[((kg * 4 + wq) * 2 + T) * 64 + lane] = kg ? acc[T][0] : acc[T][1];
    raw_barrier();
    f32x4 fin[2];
#pragma unroll
    for (int T = 0; T < 2; ++T) {
      const f32x4 o = xb[(((kg ^ 1) * 4 + wq) * 2 + T) * 64 + lane];
      fin[T] = (kg ? acc[T][1] : acc[T][0]) + o;
    }
    unsigned pb[2][2], pt[2][2];
#pragma unroll
    for (int T = 0; T < 2; ++T) {
      pb[T][0] = pk_bf16(fin[T][0], fin[T][1]);
      pb[T][1] = pk_bf16(fin[T][2], fin[T][3]);
      const s16x4 tv = tr_read(smem + K2_TT + (32 * wn + 16 * T + 4 * g + q) * 128 + (32 * wm + 16 * kg + 4 * p) * 2);
      const u32x2 tu = __builtin_bit_cast(u32x2, tv);
      pt[T][0] = tu[0];
      pt[T][1] = tu[1];
    }
    auto b0 = __builtin_amdgcn_permlane16_swap(pb[0][0], pb[1][0], false, false);
    auto b1 = __builtin_amdgcn_permlane16_swap(pb[0][1], pb[1][1], false, false);
    auto t0 = __builtin_amdgcn_permlane16_swap(pt[0][0], pt[1][0], false, false);
    auto t1 = __builtin_amdgcn_permlane16_swap(pt[0][1], pt[1][1], false, false);
    const u32x4 bv = {b0[0], b1[0], b0[1], b1[1]};
    const u32x4 tv = {t0[0], t1[0], t0[1], t1[1]};
    u32x4 o;
#pragma unroll
    for (int j = 0; j < 4; ++j) {
      float res[2];
#pragma unroll
      for (int hh = 0; hh < 2; ++hh) {
        const float xe = hh ? bf_hi(xv[j]) : bf_lo(xv[j]);
        const float te = hh ? bf_hi(tv[j]) : bf_lo(tv[j]);
        const float be = hh ? bf_hi(bv[j]) : bf_lo(bv[j]);
        const float xn = round_bf(round_bf(xe - stv[0]) * stv[1]);
        const float a = round_bf(xn * te);
        res[hh] = round_bf(xe + a) + be;
      }
      o[j] = pk_bf16(res[0], res[1]);
    }
    *(u32x4*)(out + (size_t)erow * D_ + edoff) = o;
  }
  STAMP(15);
  STAMP_FLUSH(1);
}

extern "C" void kernel_launch(void* const* d_in, const int* in_sizes, int n_in, void* d_out, int out_size,
                              void* d_ws, size_t ws_size, hipStream_t stream) {
  const u16* q = (const u16*)d_in[0];
  const u16* k = (const u16*)d_in[1];
  const u16* v = (const u16*)d_in[2];
  const u16* shk = (const u16*)d_in[3];
  const u16* ssmk = (const u16*)d_in[4];
  const u16* ssmd = (const u16*)d_in[5];
  const u16* wg = (const u16*)d_in[7];
  const u16* wu = (const u16*)d_in[8];
  const u16* wd = (const u16*)d_in[9];
  char* ws = (char*)d_ws;
  u16* hbuf = (u16*)(ws + WS_H);
  u16* tT = (u16*)(ws + WS_T);
  float2* stats = (float2*)(ws + WS_ST);
  k1_kernel<<<256, 512, 0, stream>>>(q, k, v, shk, ssmk, ssmd, wg, wu, wd, hbuf, tT, stats);
  k2_kernel<<<256, 512, 0, stream>>>(q, hbuf, wd, tT, stats, (u16*)d_out);
}
```

```cpp
#include <hip/hip_runtime.h>
#include <hip/hip_bf16.h>
#include <stdint.h>

typedef __attribute__((ext_vector_type(8))) short bf16x8;
typedef __attribute__((ext_vector_type(4))) short s16x4;
typedef __attribute__((ext_vector_type(2))) __bf16 bf16x2;
typedef __attribute__((ext_vector_type(4))) float f32x4;
typedef __attribute__((ext_vector_type(2))) float f32x2;
typedef __attribute__((ext_vector_type(4))) unsigned u32x4;
typedef __attribute__((ext_vector_type(2))) unsigned u32x2;
typedef __attribute__((address_space(3))) s16x4* lds_s16x4_ptr;
typedef unsigned short u16;

#define DI __device__ __forceinline__

constexpr int S_ = 2048, D_ = 512, F_ = 1376, FP_ = 1408;

constexpr size_t WS_H = 0;
constexpr size_t WS_T = 6u * 1024 * 1024;
constexpr size_t WS_ST = 8u * 1024 * 1024 + 512 * 1024;

template <int N> DI void wait_vm() { asm volatile("s_waitcnt vmcnt(%0)" ::"i"(N) : "memory"); }
DI void wait_lgkm0() { asm volatile("s_waitcnt lgkmcnt(0)" ::: "memory"); }
DI void raw_barrier() {
  asm volatile("s_waitcnt lgkmcnt(0)" ::: "memory");
  __builtin_amdgcn_s_barrier();
  asm volatile("" ::: "memory");
}

DI unsigned pk_bf16(float lo, float hi) {
  f32x2 f = {lo, hi};
  return __builtin_bit_cast(unsigned, __builtin_convertvector(f, bf16x2));
}
DI float bf_lo(unsigned v) { return __uint_as_float(v << 16); }
DI float bf_hi(unsigned v) { return __uint_as_float(v & 0xffff0000u); }
DI float round_bf(float x) { return bf_lo(pk_bf16(x, x)); }
DI float dot2bf(unsigned a, unsigned b, float c) {
  return __builtin_amdgcn_fdot2_f32_bf16(__builtin_bit_cast(bf16x2, a), __builtin_bit_cast(bf16x2, b), c, false);
}
DI s16x4 tr_read(const char* p) {
  return __builtin_amdgcn_ds_read_tr16_b64_v4i16((lds_s16x4_ptr)p);
}
DI bf16x8 cat8(s16x4 lo, s16x4 hi) { return __builtin_shufflevector(lo, hi, 0, 1, 2, 3, 4, 5, 6, 7); }
DI f32x4 mfma16(bf16x8 a, bf16x8 b, f32x4 c) { return __builtin_amdgcn_mfma_f32_16x16x32_bf16(a, b, c, 0, 0, 0); }

DI void glds16(const void* g, char* lds) {
  unsigned keep;
  const unsigned dst = __builtin_amdgcn_readfirstlane((unsigned)(size_t)lds);
  asm volatile("s_mov_b32 %0, m0\n\ts_mov_b32 m0, %2\n\ts_nop 0\n\tglobal_load_lds_dwordx4 %1, off\n\ts_mov_b32 m0, %0"
               : "=&s"(keep) : "v"(g), "s"(dst) : "memory");
}
DI void glds16s(const void* sbase, unsigned voff, char* lds) {
  unsigned keep;
  const unsigned dst = __builtin_amdgcn_readfirstlane((unsigned)(size_t)lds);
  asm volatile("s_mov_b32 %0, m0\n\ts_mov_b32 m0, %3\n\ts_nop 0\n\tglobal_load_lds_dwordx4 %1, %2\n\ts_mov_b32 m0, %0"
               : "=&s"(keep) : "v"(voff), "s"(sbase), "s"(dst) : "memory");
}
DI float mul_s(float a, float s) { float d; asm("v_mul_f32 %0, %2, %1" : "=v"(d) : "v"(a), "s"(s)); return d; }
DI float fma_s(float a, float s, float c) { float d; asm("v_fma_f32 %0, %1, %2, %3" : "=v"(d) : "v"(a), "s"(s), "v"(c)); return d; }
DI float mul_v(float a, float b) { float d; asm("v_mul_f32 %0, %1, %2" : "=v"(d) : "v"(a), "v"(b)); return d; }
DI void touch(const void* g, char* dummy) {
  unsigned keep;
  const unsigned dst = __builtin_amdgcn_readfirstlane((unsigned)(size_t)dummy);
  asm volatile("s_mov_b32 %0, m0\n\ts_mov_b32 m0, %2\n\ts_nop 0\n\tglobal_load_lds_dword %1, off\n\ts_mov_b32 m0, %0"
               : "=&s"(keep) : "v"(g), "s"(dst) : "memory");
}

#define WT_MODE 1
DI void store16_wt(void* p, u32x4 v) {
  asm volatile("global_store_dwordx4 %0, %1, off sc1\n\ts_nop 1" ::"v"(p), "v"(v) : "memory");
}
DI void store8_wt(void* p, u32x2 v) {
  asm volatile("global_store_dwordx2 %0, %1, off sc1\n\ts_nop 1" ::"v"(p), "v"(v) : "memory");
}

#define STAMP_DECL do {} while (0)
#define STAMP(I) do {} while (0)
#define STAMP_FLUSH(K) do {} while (0)
#define STAMP_ARG
#define STAMP_PASS

template <int I, int N, class Fn> DI void static_for(Fn&& fn) {
  if constexpr (I < N) {
    fn(std::integral_constant<int, I>{});
    static_for<I + 1, N>(fn);
  }
}

constexpr int K1_STAGE = 49152;
constexpr int K1_DUMMY = 151808;
constexpr int K1_LDS = K1_DUMMY + 256;

constexpr int US_PITCH = 4160;
constexpr int HQ_PITCH = 4576;
constexpr int FB_PITCH = 5120;
constexpr int CV_US = 0;
constexpr int CV_HQ = 8 * US_PITCH;
constexpr int CV_FB = CV_HQ + 8 * HQ_PITCH;
static_assert(CV_FB + 16 * FB_PITCH <= K1_DUMMY && 3 * K1_STAGE <= K1_DUMMY, "lds layout");

constexpr int G1_A = 0;
constexpr int G1_W = 49152;
#define SB __builtin_amdgcn_sched_barrier(0)

DI void gemm1_block(char* smem, const u16* __restrict__ x, const u16* __restrict__ wg, const u16* __restrict__ wu,
                    u16* __restrict__ hbuf, float2* __restrict__ stats, int mt, int ft, int rank STAMP_ARG) {
  const int tid = threadIdx.x, lane = tid & 63;
  const int w = __builtin_amdgcn_readfirstlane(tid >> 6);
  const int wm = w >> 2, wn = w & 3;
  const int r = lane & 15, g = lane >> 4, q = (lane & 15) >> 2, p = lane & 3;
  const int row0 = mt * 128, f0 = ft * 128;

  unsigned aoffs[2], woffs[2];
#pragma unroll
  for (int e = 0; e < 2; ++e) {
    const int i = 2 * w + e;
    const int row = 8 * i + (lane >> 3);
    aoffs[e] = (unsigned)((row0 + row) * D_ + (((lane & 7) ^ (row & 7)) << 3)) * 2u;
    const int kr = 4 * i + (lane >> 4);
    const int phi = (kr & 3) | (((kr >> 3) & 1) << 2);
    const int sc = ((((lane & 15) >> 1) ^ phi) << 1) | (lane & 1);
    int col = f0 + sc * 8;
    col = col > F_ - 8 ? F_ - 8 : col;
    woffs[e] = (unsigned)(kr * F_ + col) * 2u;
  }
  auto dma_piece = [&](int kt, int n) {
    const int b = kt % 3, e = n & 1;
    if (n < 2) glds16s(x + kt * 64, aoffs[e], smem + G1_A + b * 16384 + (2 * w + e) * 1024);
    else if (n < 4) glds16s(wg + kt * 64 * F_, woffs[e], smem + G1_W + b * 32768 + (2 * w + e) * 1024);
    else glds16s(wu + kt * 64 * F_, woffs[e], smem + G1_W + b * 32768 + 16384 + (2 * w + e) * 1024);
  };
#pragma unroll
  for (int kt = 0; kt < 3; ++kt)
#pragma unroll
    for (int n = 0; n < 6; ++n) dma_piece(kt, n);

  int aoff[4][2];
#pragma unroll
  for (int i = 0; i < 4; ++i)
#pragma unroll
    for (int ks = 0; ks < 2; ++ks)
      aoff[i][ks] = G1_A + (64 * wm + 16 * (i ^ wn) + r) * 128 + (((4 * ks + g) ^ (r & 7)) << 4);
  const int phiL = q | ((g & 1) << 2);
  int boff[4];
#pragma unroll
  for (int T = 0; T < 4; ++T) {
    const int mat = (T >> 1) ^ wm;
    boff[T] = G1_W + mat * 16384 + (8 * g + q) * 256 + (((2 * wn + (T & 1)) ^ phiL) << 5) + p * 8;
  }

  f32x4 acc[4][4];
#pragma unroll
  for (int T = 0; T < 4; ++T)
#pragma unroll
    for (int i = 0; i < 4; ++i) acc[T][i] = f32x4{0.f, 0.f, 0.f, 0.f};
  f32x4 accS = {0.f, 0.f, 0.f, 0.f}, accR = accS, accC[2] = {accS, accS};
  const bf16x8 ones = {0x3f80, 0x3f80, 0x3f80, 0x3f80, 0x3f80, 0x3f80, 0x3f80, 0x3f80};

  struct Frags { s16x4 wl[4], wh[4]; bf16x8 xf[4]; };
  Frags fa, fb2;
  auto read_piece = [&](Frags& f, int kt, int ks, int n) {
    const int b = kt % 3;
    if (n < 4) f.xf[n] = *(const bf16x8*)(smem + b * 16384 + aoff[n][ks]);
    else {
      const int T = (n - 4) >> 1;
      if ((n & 1) == 0) f.wl[T] = tr_read(smem + b * 32768 + ks * 8192 + boff[T]);
      else f.wh[T] = tr_read(smem + b * 32768 + ks * 8192 + 1024 + boff[T]);
    }
  };
  auto mma_one = [&](const Frags& f, int m) {
    const int T = m / 5, i = m % 5;
    if (i < 4) acc[T][i] = mfma16(cat8(f.wl[T], f.wh[T]), f.xf[i], acc[T][i]);
    else if (T == 0) accS = mfma16(f.xf[0], f.xf[0], accS);
    else if (T == 1) accR = mfma16(ones, f.xf[0], accR);
    else if (T == 2) accC[0] = mfma16(cat8(f.wl[0], f.wh[0]), ones, accC[0]);
    else accC[1] = mfma16(cat8(f.wl[1], f.wh[1]), ones, accC[1]);
  };

  wait_vm<12>();
  raw_barrier();
  STAMP(1);
  {
    touch(x + (size_t)(row0 + 64 * (rank & 1) + (tid >> 3)) * D_ + (tid & 7) * 64, smem + K1_DUMMY);
    const int kr = 64 * (mt & 7) + (tid >> 3);
    const int sel = tid & 7, pos = sel % 3;
    int col = f0 + (pos == 0 ? 0 : pos == 1 ? 64 : 126);
    col = col > F_ - 2 ? F_ - 2 : col;
    touch((((sel / 3) & 1) ? wu : wg) + kr * F_ + col, smem + K1_DUMMY);
  }
#pragma unroll
  for (int n = 0; n < 12; ++n) read_piece(fa, 0, 0, n);
  static_for<0, 8>([&](auto KT) {
    constexpr int kt = KT.value;
    SB;
#pragma unroll
    for (int m = 0; m < 16; ++m) {
      mma_one(fa, m);
      if (m < 12) read_piece(fb2, kt, 1, m);
      SB;
    }
    if constexpr (kt < 7) {
      if constexpr (kt < 2) wait_vm<8>(); else if constexpr (kt < 6) wait_vm<6>(); else wait_vm<0>();
      raw_barrier();
      STAMP(2 + kt);
      SB;
    }
#pragma unroll
    for (int m = 16; m < 20; ++m) {
      mma_one(fa, m);
      if (m == 17 && kt < 5) dma_piece(kt + 3, 0);
      SB;
    }
#pragma unroll
    for (int m = 0; m < 20; ++m) {
      mma_one(fb2, m);
      if (m < 12 && kt < 7) read_piece(fa, kt + 1, 0, m);
      if ((m == 1 || m == 5 || m == 9 || m == 13 || m == 17) && kt < 5) dma_piece(kt + 3, 1 + (m >> 2));
      SB;
    }
  });
  __syncthreads();
  STAMP(9);
  float2* st_s = (float2*)smem;
  float* cs_s = (float*)(smem + 1024);
  if (g == (r >> 2)) {
    const int rr = r & 3;
    const float s2 = rr == 0 ? accS[0] : rr == 1 ? accS[1] : rr == 2 ? accS[2] : accS[3];
    const float mean = accR[0] * (1.f / D_);
    float var = s2 * (1.f / D_) - mean * mean;
    var = var < 0.f ? 0.f : var;
    const float mb = round_bf(mean);
    const float vb = round_bf(var);
    const float sd = round_bf(sqrtf(round_bf(vb + 1e-5f)));
    const float2 stv = make_float2(mb, 1.f / sd);
    const int row = 64 * wm + 16 * wn + r;
    st_s[row] = stv;
    if (ft == 0) stats[row0 + row] = stv;
  }
  if (r == 0) {
    *(f32x4*)(cs_s + wm * 128 + 32 * wn + 4 * g) = accC[0];
    *(f32x4*)(cs_s + wm * 128 + 32 * wn + 16 + 4 * g) = accC[1];
  }
  __syncthreads();
  const bool valid = (f0 + 32 * wn) < F_;
  f32x4 ca[2], cb[2];
#pragma unroll
  for (int T = 0; T < 2; ++T) {
    ca[T] = accC[T];
    cb[T] = *(const f32x4*)(cs_s + (wm ^ 1) * 128 + 32 * wn + 16 * T + 4 * g);
  }
#pragma unroll
  for (int i = 0; i < 4; ++i) {
    const int row = 64 * wm + 16 * (i ^ wn) + r;
    const float2 stv = st_s[row];
    const float nm = -stv.x, r2 = stv.y * stv.y;
    unsigned pk[2][2];
#pragma unroll
    for (int T = 0; T < 2; ++T) {
      const f32x4 ga = acc[T][i] + nm * ca[T];
      const f32x4 gb = acc[2 + T][i] + nm * cb[T];
      const f32x4 hv = (ga * r2) * gb;
      pk[T][0] = pk_bf16(hv[0], hv[1]);
      pk[T][1] = pk_bf16(hv[2], hv[3]);
    }
    auto r0 = __builtin_amdgcn_permlane16_swap(pk[0][0], pk[1][0], false, false);
    auto r1 = __builtin_amdgcn_permlane16_swap(pk[0][1], pk[1][1], false, false);
    u32x4 o = {r0[0], r1[0], r0[1], r1[1]};
    if (!valid) o = u32x4{0u, 0u, 0u, 0u};
    store16_wt(hbuf + (size_t)(row0 + row) * FP_ + f0 + 32 * wn + 16 * (g & 1) + 8 * (g >> 1), o);
  }
}

DI void conv_block(char* smem, const u16* __restrict__ kin, const u16* __restrict__ vin,
                   const u16* __restrict__ shk, const u16* __restrict__ ssmk, const u16* __restrict__ ssmd,
                   u16* __restrict__ tT, int cg STAMP_ARG) {
  const int tid = threadIdx.x, lane = tid & 63;
  const int w = __builtin_amdgcn_readfirstlane(tid >> 6);
  const int r = lane & 15, g = lane >> 4;
  const int c0 = cg * 8, ch = c0 + w;

  u32x4 hx[4];
#pragma unroll
  for (int i = 0; i < 4; ++i) hx[i] = *(const u32x4*)(ssmk + (size_t)ch * S_ + (i * 64 + lane) * 8);
  touch(((tid & 256) ? vin : kin) + (size_t)(256 * (cg & 7) + (tid & 255)) * D_ + (cg >> 3) * 64, smem + K1_DUMMY);
  float wsh[8][4];
#pragma unroll
  for (int c = 0; c < 8; ++c) {
    const u32x2 t = *(const u32x2*)(shk + (c0 + c) * 4);
    const unsigned t0 = __builtin_amdgcn_readfirstlane(t[0]), t1 = __builtin_amdgcn_readfirstlane(t[1]);
    wsh[c][0] = bf_lo(t0); wsh[c][1] = bf_hi(t0); wsh[c][2] = bf_lo(t1); wsh[c][3] = bf_hi(t1);
  }
  const float dch = __uint_as_float((unsigned)ssmd[ch] << 16);
  wait_vm<0>();

  u32x4 kx[4], vx[4], hal[4][3];
  auto loads = [&](int j) {
    const int s = 512 * j + tid;
    kx[j] = *(const u32x4*)(kin + (size_t)s * D_ + c0);
    vx[j] = *(const u32x4*)(vin + (size_t)s * D_ + c0);
#pragma unroll
    for (int d = 0; d < 3; ++d) {
      const int sr = 512 * j + 64 * w - 1 - d;
      hal[j][d] = *(const u32x4*)(kin + (size_t)(sr < 0 ? 0 : sr) * D_ + c0);
      if (j == 0 && sr < 0) hal[j][d] = u32x4{0u, 0u, 0u, 0u};
    }
  };
  loads(0);

  char* us = smem + CV_US;
  char* hq = smem + CV_HQ + w * HQ_PITCH;
  char* fb = smem + CV_FB + w * (2 * FB_PITCH);
  if (lane < 30) *(u32x4*)(hq + lane * 16) = u32x4{0u, 0u, 0u, 0u};
  if (lane < 4) *(u32x4*)(us + w * US_PITCH + lane * 16) = u32x4{0u, 0u, 0u, 0u};
#pragma unroll
  for (int i = 0; i < 4; ++i) *(u32x4*)(hq + 480 + (i * 64 + lane) * 16) = hx[i];

  auto make_u_d = [&](int j, int d) {
    const int s = 512 * j + tid;
    const unsigned k0 = kx[j][d];
    const unsigned k1 = (unsigned)__builtin_amdgcn_update_dpp((int)hal[j][0][d], (int)k0, 0x138, 0xf, 0xf, false);
    const unsigned k2 = (unsigned)__builtin_amdgcn_update_dpp((int)hal[j][1][d], (int)k1, 0x138, 0xf, 0xf, false);
    const unsigned k3 = (unsigned)__builtin_amdgcn_update_dpp((int)hal[j][2][d], (int)k2, 0x138, 0xf, 0xf, false);
    const int ca = 2 * d, cb = 2 * d + 1;
    float a = mul_s(bf_lo(k3), wsh[ca][3]);
    a = fma_s(bf_lo(k2), wsh[ca][2], a);
    a = fma_s(bf_lo(k1), wsh[ca][1], a);
    a = fma_s(bf_lo(k0), wsh[ca][0], a);
    float b = mul_s(bf_hi(k3), wsh[cb][3]);
    b = fma_s(bf_hi(k2), wsh[cb][2], b);
    b = fma_s(bf_hi(k1), wsh[cb][1], b);
    b = fma_s(bf_hi(k0), wsh[cb][0], b);
    const unsigned pk = pk_bf16(mul_v(a, bf_lo(vx[j][d])), mul_v(b, bf_hi(vx[j][d])));
    *(u16*)(us + ca * US_PITCH + 64 + 2 * s) = (u16)(pk & 0xffffu);
    *(u16*)(us + cb * US_PITCH + 64 + 2 * s) = (u16)(pk >> 16);
  };
  const char* usw = us + w * US_PITCH;
  unsigned td[5][3];
  auto tab_read = [&](int j, int it) {
    const unsigned* src = (const unsigned*)(usw + 2 * (512 * j + 16 + 2 * (it * 64 + lane)));
    td[it][2] = src[0]; td[it][1] = src[-1]; td[it][0] = src[-2];
  };
  auto tab_write = [&](int j, int it) {
    u32x4 e;
    e[0] = (td[it][2] & 0xffffu) | (td[it][1] & 0xffff0000u);
    e[1] = (td[it][1] & 0xffffu) | (td[it][0] & 0xffff0000u);
    e[2] = (td[it][2] >> 16) | (td[it][2] << 16);
    e[3] = (td[it][1] >> 16) | (td[it][1] << 16);
    *(u32x4*)(fb + (j & 1) * FB_PITCH + (it * 64 + lane) * 16) = e;
  };

  f32x4 acc[8];
#pragma unroll
  for (int n = 0; n < 8; ++n) acc[n] = f32x4{0.f, 0.f, 0.f, 0.f};

  const char* a_lane = fb + (32 + r - 8 * g) * 8;
  const char* b_lane = hq + 448 + 32 * r + 16 * g;

  STAMP(8);
#pragma unroll
  for (int d = 0; d < 4; ++d) make_u_d(0, d);
  STAMP(10);
  raw_barrier();
  STAMP(11);
#pragma unroll
  for (int it = 0; it < 5; ++it) tab_read(0, it);
#pragma unroll
  for (int it = 0; it < 5; ++it) tab_write(0, it);

  static_for<0, 4>([&](auto J) {
    constexpr int j = J.value;
    constexpr int NNU = 8 - 2 * j;
    constexpr int NM = 2 * NNU - 1;
    constexpr int NR = NNU + 4;
    constexpr int NS = NM > NR ? NM : NR;
    STAMP(1 + j);
    s16x4 al[2][2], ah[2][2];
    bf16x8 bfr[2][NNU];
    auto read_one = [&](int set, int k0, int n) {
      if (n < 4) {
        const char* a = a_lane + (j & 1) * FB_PITCH + (8 * (n >> 1) + k0) * 256;
        if ((n & 1) == 0) al[set][n >> 1] = *(const s16x4*)a; else ah[set][n >> 1] = *(const s16x4*)(a - 32);
      } else {
        bfr[set][n - 4] = *(const bf16x8*)(b_lane + 512 * (n - 4) - 64 * k0);
      }
    };
    auto mma_one = [&](int set, int m) {
      if (m < NNU) acc[2 * j + m] = mfma16(cat8(al[set][0], ah[set][0]), bfr[set][m], acc[2 * j + m]);
      else acc[2 * j + 1 + (m - NNU)] = mfma16(cat8(al[set][1], ah[set][1]), bfr[set][m - NNU], acc[2 * j + 1 + (m - NNU)]);
    };
#pragma unroll
    for (int n = 0; n < NR; ++n) read_one(0, 0, n);
    SB;
#pragma unroll
    for (int k0 = 0; k0 < 8; ++k0) {
#pragma unroll
      for (int m = 0; m < NS; ++m) {
        if (m < NM) mma_one(k0 & 1, m);
        if (m < NR && k0 < 7) read_one((k0 + 1) & 1, k0 + 1, m);
        if (j == 0 && k0 == 0 && m == 1) loads(1);
        if (j < 2 && k0 == 1 && m == 1) loads(j + 2);
        if (j < 3 && k0 >= 2 && k0 < 6 && m == NS / 2) make_u_d(j + 1, k0 - 2);
        if (j < 3 && k0 == 5 && m == NS - 1) raw_barrier();
        if (j < 3 && k0 == 6 && m < 5) tab_read(j + 1, m);
        if (j < 3 && k0 == 7 && m < 5) tab_write(j + 1, m);
        SB;
      }
    }
  });
  STAMP(9);
#pragma unroll
  for (int n = 0; n < 8; ++n) {
    const int s = 256 * n + 16 * r + 4 * g;
    const u32x2 uu = *(const u32x2*)(usw + 64 + 2 * s);
    float t[4];
    t[0] = round_bf(acc[n][0]) + round_bf(bf_lo(uu[0]) * dch);
    t[1] = round_bf(acc[n][1]) + round_bf(bf_hi(uu[0]) * dch);
    t[2] = round_bf(acc[n][2]) + round_bf(bf_lo(uu[1]) * dch);
    t[3] = round_bf(acc[n][3]) + round_bf(bf_hi(uu[1]) * dch);
    u32x2 o = {pk_bf16(t[0], t[1]), pk_bf16(t[2], t[3])};
    store8_wt(tT + (size_t)ch * S_ + s, o);
  }
}

__global__ __launch_bounds__(512, 2) void k1_kernel(const u16* __restrict__ x, const u16* __restrict__ kin,
                                                     const u16* __restrict__ vin, const u16* __restrict__ shk,
                                                     const u16* __restrict__ ssmk, const u16* __restrict__ ssmd,
                                                     const u16* __restrict__ wg, const u16* __restrict__ wu,
                                                     const u16* __restrict__ wd,
                                                     u16* __restrict__ hbuf, u16* __restrict__ tT,
                                                     float2* __restrict__ stats) {
  __shared__ __attribute__((aligned(16))) char smem[K1_LDS];
  const int b = blockIdx.x, xcd = b & 7, idx = b >> 3;
  STAMP_DECL;
  STAMP(0);
  if (idx < 8) {
    conv_block(smem, kin, vin, shk, ssmk, ssmd, tT, xcd * 8 + idx STAMP_PASS);
  } else if (idx < 30) {
    const int t = 22 * (xcd & 3) + (idx - 8);
    gemm1_block(smem, x, wg, wu, hbuf, stats, 8 * (xcd >> 2) + (t & 7), t >> 3, (idx - 8) >> 3 STAMP_PASS);
  } else {
    const int part = (idx - 30) * 8 + xcd;
    __builtin_amdgcn_s_sleep(127);
    for (int i = threadIdx.x; i < 688; i += 512) touch(wd + (size_t)(part * 688 + i) * 64, smem + K1_DUMMY);
    wait_vm<0>();
  }
  STAMP(15);
  STAMP_FLUSH(0);
}

constexpr int K2_NB = 4;
constexpr int K2_STAGE = 32768;
constexpr int K2_TT = K2_NB * K2_STAGE;
constexpr int K2_X = K2_TT + 8192;
constexpr int K2_ST = K2_X + 8192;
constexpr int K2_DUMMY = K2_ST + 1024;
constexpr int K2_LDS = K2_DUMMY + 256;
constexpr int K2_NT = FP_ / 128;

__global__ __launch_bounds__(512, 2) void k2_kernel(const u16* __restrict__ x, const u16* __restrict__ hbuf,
                                                     const u16* __restrict__ wd, const u16* __restrict__ tT,
                                                     const float2* __restrict__ stats, u16* __restrict__ out) {
  __shared__ __attribute__((aligned(16))) char smem[K2_LDS];
  const int tid = threadIdx.x, lane = tid & 63;
  const int w = __builtin_amdgcn_readfirstlane(tid >> 6);
  const int kg = w >> 2, wm = (w >> 1) & 1, wn = w & 1;
  const int r = lane & 15, g = lane >> 4, q = (lane & 15) >> 2, p = lane & 3;
  const int b = blockIdx.x, xcd = b & 7, idx = b >> 3;
  const int row0 = (4 * xcd + (idx >> 3)) * 64, d0 = (idx & 7) * 64;
  STAMP_DECL;
  STAMP(0);

  unsigned aoffs[2], woffs[2], woffl[2];
#pragma unroll
  for (int e = 0; e < 2; ++e) {
    const int ar = 8 * w + 4 * e + (lane >> 4);
    aoffs[e] = (unsigned)((row0 + ar) * FP_ + (((lane & 15) ^ (ar & 15)) << 3)) * 2u;
    const int kr = 16 * w + 8 * e + (lane >> 3);
    const int phi_s = ((kr >> 1) & 1) | (((kr >> 3) & 1) << 1);
    const int wcol = d0 + ((((((lane & 7) >> 1) ^ phi_s) << 1) | (lane & 1)) << 3);
    woffs[e] = (unsigned)(kr * D_ + wcol) * 2u;
    const int krl = kr > F_ - 1 - 1280 ? F_ - 1 - 1280 : kr;
    woffl[e] = (unsigned)(krl * D_ + wcol) * 2u;
  }
  auto dma_piece = [&](int kt, int n) {
    char* sb = smem + (kt & (K2_NB - 1)) * K2_STAGE;
    const int e = n & 1;
    if (n < 2) glds16s(hbuf + kt * 128, aoffs[e], sb + (2 * w + e) * 1024);
    else glds16s(wd + kt * 128 * D_, kt == K2_NT - 1 ? woffl[e] : woffs[e], sb + 16384 + (2 * w + e) * 1024);
  };
#pragma unroll
  for (int n = 0; n < 4; ++n) dma_piece(0, n);
  {
    const bool ish = tid < 176;
    const int hr = tid / 22, hl = tid % 22;
    int kr = 344 * (idx >> 3) + (tid - 176);
    kr = kr > F_ - 1 ? F_ - 1 : kr;
    const u16* ph = hbuf + (size_t)(row0 + 8 * (idx & 7) + (hr & 7)) * FP_ + hl * 64;
    const u16* pw = wd + (size_t)kr * D_ + d0;
    touch(ish ? ph : pw, smem + K2_DUMMY);
  }
#pragma unroll
  for (int kt = 1; kt < 3; ++kt)
#pragma unroll
    for (int n = 0; n < 4; ++n) dma_piece(kt, n);

  const int erow = row0 + 32 * wm + 16 * kg + r;
  const int edoff = d0 + 32 * wn + 16 * (g & 1) + 8 * (g >> 1);

  int aoff[2][2], boff[2];
#pragma unroll
  for (int i = 0; i < 2; ++i)
#pragma unroll
    for (int hs = 0; hs < 2; ++hs)
      aoff[i][hs] = (32 * wm + 16 * i + r) * 256 + (((4 * (kg + 2 * hs) + g) ^ r) << 4);
  const int phiL = ((q >> 1) & 1) | ((g & 1) << 1);
#pragma unroll
  for (int T = 0; T < 2; ++T)
    boff[T] = 16384 + (32 * kg + 8 * g + q) * 128 + (((2 * wn + T) ^ phiL) << 5) + p * 8;

  f32x4 acc[2][2];
#pragma unroll
  for (int T = 0; T < 2; ++T)
#pragma unroll
    for (int i = 0; i < 2; ++i) acc[T][i] = f32x4{0.f, 0.f, 0.f, 0.f};

  struct Frags2 { bf16x8 hf[2][2]; s16x4 wl[2][2], wh[2][2]; };
  Frags2 fs[2];
  auto read_piece = [&](Frags2& f, int kt, int n) {
    const char* sb = smem + (kt & (K2_NB - 1)) * K2_STAGE;
    const int hs = n / 6, k = n % 6;
    if (k < 2) f.hf[hs][k] = *(const bf16x8*)(sb + aoff[k][hs]);
    else if ((k & 1) == 0) f.wl[hs][(k - 2) >> 1] = tr_read(sb + boff[(k - 2) >> 1] + hs * 8192);
    else f.wh[hs][(k - 2) >> 1] = tr_read(sb + boff[(k - 2) >> 1] + hs * 8192 + 512);
  };
  auto mma_one = [&](const Frags2& f, int m) {
    const int hs = m >> 2, T = (m >> 1) & 1, i = m & 1;
    acc[T][i] = mfma16(cat8(f.wl[hs][T], f.wh[hs][T]), f.hf[hs][i], acc[T][i]);
  };
  static_for<0, K2_NT + 1>([&](auto KT) {
    constexpr int kt = KT.value;
    if constexpr (kt < K2_NT) {
      if constexpr (kt == 0) wait_vm<9>();
      else if constexpr (kt <= 8) wait_vm<8>();
      else if constexpr (kt == 9) wait_vm<7>();
      else wait_vm<3>();
      raw_barrier();
      STAMP(1 + kt);
    }
    SB;
#pragma unroll
    for (int n = 0; n < 12; ++n) {
      if (kt < K2_NT) read_piece(fs[kt & 1], kt, n);
      if (kt > 0 && n < 8) mma_one(fs[(kt - 1) & 1], n);
      if (kt + 3 < K2_NT && (n % 3) == 2) dma_piece(kt + 3, n / 3);
      if (kt == 8 && n == 2) glds16(tT + (size_t)(d0 + 8 * w + (lane >> 3)) * S_ + row0 + (lane & 7) * 8, smem + K2_TT + w * 1024);
      if (kt == 8 && n == 5) glds16(x + (size_t)(row0 + 8 * w + (lane >> 3)) * D_ + d0 + (lane & 7) * 8, smem + K2_X + w * 1024);
      if (kt == 8 && n == 8) glds16(stats + row0 + 2 * (lane & 31), smem + K2_ST);
      SB;
    }
  });
  STAMP(12);
  wait_vm<0>();
  raw_barrier();
  const int lrow = 32 * wm + 16 * kg + r;
  const u32x4 xv = *(const u32x4*)(smem + K2_X + lrow * 128 + (32 * wn + 16 * (g & 1) + 8 * (g >> 1)) * 2);
  const f32x2 stv = *(const f32x2*)(smem + K2_ST + lrow * 8);
  {
    f32x4* xb = (f32x4*)smem;
    const int wq = w & 3;
#pragma unroll
    for (int T = 0; T < 2; ++T) xb[((kg * 4 + wq) * 2 + T) * 64 + lane] = kg ? acc[T][0] : acc[T][1];
    raw_barrier();
    f32x4 fin[2];
#pragma unroll
    for (int T = 0; T < 2; ++T) {
      const f32x4 o = xb[(((kg ^ 1) * 4 + wq) * 2 + T) * 64 + lane];
      fin[T] = (kg ? acc[T][1] : acc[T][0]) + o;
    }
    unsigned pb[2][2], pt[2][2];
#pragma unroll
    for (int T = 0; T < 2; ++T) {
      pb[T][0] = pk_bf16(fin[T][0], fin[T][1]);
      pb[T][1] = pk_bf16(fin[T][2], fin[T][3]);
      const s16x4 tv = tr_read(smem + K2_TT + (32 * wn + 16 * T + 4 * g + q) * 128 + (32 * wm + 16 * kg + 4 * p) * 2);
      const u32x2 tu = __builtin_bit_cast(u32x2, tv);
      pt[T][0] = tu[0];
      pt[T][1] = tu[1];
    }
    auto b0 = __builtin_amdgcn_permlane16_swap(pb[0][0], pb[1][0], false, false);
    auto b1 = __builtin_amdgcn_permlane16_swap(pb[0][1], pb[1][1], false, false);
    auto t0 = __builtin_amdgcn_permlane16_swap(pt[0][0], pt[1][0], false, false);
    auto t1 = __builtin_amdgcn_permlane16_swap(pt[0][1], pt[1][1], false, false);
    const u32x4 bv = {b0[0], b1[0], b0[1], b1[1]};
    const u32x4 tv = {t0[0], t1[0], t0[1], t1[1]};
    u32x4 o;
#pragma unroll
    for (int j = 0; j < 4; ++j) {
      float res[2];
#pragma unroll
      for (int hh = 0; hh < 2; ++hh) {
        const float xe = hh ? bf_hi(xv[j]) : bf_lo(xv[j]);
        const float te = hh ? bf_hi(tv[j]) : bf_lo(tv[j]);
        const float be = hh ? bf_hi(bv[j]) : bf_lo(bv[j]);
        const float xn = round_bf(round_bf(xe - stv[0]) * stv[1]);
        const float a = round_bf(xn * te);
        res[hh] = round_bf(xe + a) + be;
      }
      o[j] = pk_bf16(res[0], res[1]);
    }
    *(u32x4*)(out + (size_t)erow * D_ + edoff) = o;
  }
  STAMP(15);
  STAMP_FLUSH(1);
}

extern "C" void kernel_launch(void* const* d_in, const int* in_sizes, int n_in, void* d_out, int out_size,
                              void* d_ws, size_t ws_size, hipStream_t stream) {
  const u16* q = (const u16*)d_in[0];
  const u16* k = (const u16*)d_in[1];
  const u16* v = (const u16*)d_in[2];
  const u16* shk = (const u16*)d_in[3];
  const u16* ssmk = (const u16*)d_in[4];
  const u16* ssmd = (const u16*)d_in[5];
  const u16* wg = (const u16*)d_in[7];
  const u16* wu = (const u16*)d_in[8];
  const u16* wd = (const u16*)d_in[9];
  char* ws = (char*)d_ws;
  u16* hbuf = (u16*)(ws + WS_H);
  u16* tT = (u16*)(ws + WS_T);
  float2* stats = (float2*)(ws + WS_ST);
  k1_kernel<<<256, 512, 0, stream>>>(q, k, v, shk, ssmk, ssmd, wg, wu, wd, hbuf, tT, stats);
  k2_kernel<<<256, 512, 0, stream>>>(q, hbuf, wd, tT, stats, (u16*)d_out);
}
```

```cpp
#include <hip/hip_runtime.h>
#include <hip/hip_bf16.h>
#include <stdint.h>

typedef __attribute__((ext_vector_type(8))) short bf16x8;
typedef __attribute__((ext_vector_type(4))) short s16x4;
typedef __attribute__((ext_vector_type(2))) __bf16 bf16x2;
typedef __attribute__((ext_vector_type(4))) float f32x4;
typedef __attribute__((ext_vector_type(2))) float f32x2;
typedef __attribute__((ext_vector_type(4))) unsigned u32x4;
typedef __attribute__((ext_vector_type(2))) unsigned u32x2;
typedef __attribute__((address_space(3))) s16x4* lds_s16x4_ptr;
typedef unsigned short u16;

#define DI __device__ __forceinline__

constexpr int S_ = 2048, D_ = 512, F_ = 1376, FP_ = 1408;

constexpr size_t WS_H = 0;
constexpr size_t WS_T = 6u * 1024 * 1024;
constexpr size_t WS_U = 8u * 1024 * 1024;
constexpr size_t WS_ST = 10u * 1024 * 1024;
constexpr size_t WS_CNT = 10u * 1024 * 1024 + 65536;

template <int N> DI void wait_vm() { asm volatile("s_waitcnt vmcnt(%0)" ::"i"(N) : "memory"); }
DI void wait_lgkm0() { asm volatile("s_waitcnt lgkmcnt(0)" ::: "memory"); }
DI void raw_barrier() {
  asm volatile("s_waitcnt lgkmcnt(0)" ::: "memory");
  __builtin_amdgcn_s_barrier();
  asm volatile("" ::: "memory");
}

DI unsigned pk_bf16(float lo, float hi) {
  f32x2 f = {lo, hi};
  return __builtin_bit_cast(unsigned, __builtin_convertvector(f, bf16x2));
}
DI float bf_lo(unsigned v) { return __uint_as_float(v << 16); }
DI float bf_hi(unsigned v) { return __uint_as_float(v & 0xffff0000u); }
DI float round_bf(float x) { return bf_lo(pk_bf16(x, x)); }
DI float dot2bf(unsigned a, unsigned b, float c) {
  return __builtin_amdgcn_fdot2_f32_bf16(__builtin_bit_cast(bf16x2, a), __builtin_bit_cast(bf16x2, b), c, false);
}
DI s16x4 tr_read(const char* p) {
  return __builtin_amdgcn_ds_read_tr16_b64_v4i16((lds_s16x4_ptr)p);
}
DI bf16x8 cat8(s16x4 lo, s16x4 hi) { return __builtin_shufflevector(lo, hi, 0, 1, 2, 3, 4, 5, 6, 7); }
DI f32x4 mfma16(bf16x8 a, bf16x8 b, f32x4 c) { return __builtin_amdgcn_mfma_f32_16x16x32_bf16(a, b, c, 0, 0, 0); }

DI void glds16(const void* g, char* lds) {
  unsigned keep;
  const unsigned dst = __builtin_amdgcn_readfirstlane((unsigned)(size_t)lds);
  asm volatile("s_mov_b32 %0, m0\n\ts_mov_b32 m0, %2\n\ts_nop 0\n\tglobal_load_lds_dwordx4 %1, off\n\ts_mov_b32 m0, %0"
               : "=&s"(keep) : "v"(g), "s"(dst) : "memory");
}
DI void glds16s(const void* sbase, unsigned voff, char* lds) {
  unsigned keep;
  const unsigned dst = __builtin_amdgcn_readfirstlane((unsigned)(size_t)lds);
  asm volatile("s_mov_b32 %0, m0\n\ts_mov_b32 m0, %3\n\ts_nop 0\n\tglobal_load_lds_dwordx4 %1, %2\n\ts_mov_b32 m0, %0"
               : "=&s"(keep) : "v"(voff), "s"(sbase), "s"(dst) : "memory");
}
DI float mul_s(float a, float s) { float d; asm("v_mul_f32 %0, %2, %1" : "=v"(d) : "v"(a), "s"(s)); return d; }
DI float fma_s(float a, float s, float c) { float d; asm("v_fma_f32 %0, %1, %2, %3" : "=v"(d) : "v"(a), "s"(s), "v"(c)); return d; }
DI float mul_v(float a, float b) { float d; asm("v_mul_f32 %0, %1, %2" : "=v"(d) : "v"(a), "v"(b)); return d; }
DI void touch(const void* g, char* dummy) {
  unsigned keep;
  const unsigned dst = __builtin_amdgcn_readfirstlane((unsigned)(size_t)dummy);
  asm volatile("s_mov_b32 %0, m0\n\ts_mov_b32 m0, %2\n\ts_nop 0\n\tglobal_load_lds_dword %1, off\n\ts_mov_b32 m0, %0"
               : "=&s"(keep) : "v"(g), "s"(dst) : "memory");
}

#define WT_MODE 1
DI void store16_wt(void* p, u32x4 v) {
  asm volatile("global_store_dwordx4 %0, %1, off sc1\n\ts_nop 1" ::"v"(p), "v"(v) : "memory");
}
DI void store8_wt(void* p, u32x2 v) {
  asm volatile("global_store_dwordx2 %0, %1, off sc1\n\ts_nop 1" ::"v"(p), "v"(v) : "memory");
}

#define STAMP_DECL do {} while (0)
#define STAMP(I) do {} while (0)
#define STAMP_FLUSH(K) do {} while (0)
#define STAMP_ARG
#define STAMP_PASS

template <int I, int N, class Fn> DI void static_for(Fn&& fn) {
  if constexpr (I < N) {
    fn(std::integral_constant<int, I>{});
    static_for<I + 1, N>(fn);
  }
}

constexpr int K1_DUMMY = 147456;
constexpr int K1_LDS = K1_DUMMY + 256;

constexpr int G1_A = 0;
constexpr int G1_W = 49152;
#define SB __builtin_amdgcn_sched_barrier(0)

DI void gemm1_block(char* smem, const u16* __restrict__ x, const u16* __restrict__ wg, const u16* __restrict__ wu,
                    u16* __restrict__ hbuf, float2* __restrict__ stats, int mt, int ft, int rank STAMP_ARG) {
  const int tid = threadIdx.x, lane = tid & 63;
  const int w = __builtin_amdgcn_readfirstlane(tid >> 6);
  const int wm = w >> 2, wn = w & 3;
  const int r = lane & 15, g = lane >> 4, q = (lane & 15) >> 2, p = lane & 3;
  const int row0 = mt * 128, f0 = ft * 128;

  unsigned aoffs[2], woffs[2];
#pragma unroll
  for (int e = 0; e < 2; ++e) {
    const int i = 2 * w + e;
    const int row = 8 * i + (lane >> 3);
    aoffs[e] = (unsigned)((row0 + row) * D_ + (((lane & 7) ^ (row & 7)) << 3)) * 2u;
    const int kr = 4 * i + (lane >> 4);
    const int phi = (kr & 3) | (((kr >> 3) & 1) << 2);
    const int sc = ((((lane & 15) >> 1) ^ phi) << 1) | (lane & 1);
    int col = f0 + sc * 8;
    col = col > F_ - 8 ? F_ - 8 : col;
    woffs[e] = (unsigned)(kr * F_ + col) * 2u;
  }
  auto dma_piece = [&](int kt, int n) {
    const int b = kt % 3, e = n & 1;
    if (n < 2) glds16s(x + kt * 64, aoffs[e], smem + G1_A + b * 16384 + (2 * w + e) * 1024);
    else if (n < 4) glds16s(wg + kt * 64 * F_, woffs[e], smem + G1_W + b * 32768 + (2 * w + e) * 1024);
    else glds16s(wu + kt * 64 * F_, woffs[e], smem + G1_W + b * 32768 + 16384 + (2 * w + e) * 1024);
  };
#pragma unroll
  for (int kt = 0; kt < 3; ++kt)
#pragma unroll
    for (int n = 0; n < 6; ++n) dma_piece(kt, n);

  int aoff[4][2];
#pragma unroll
  for (int i = 0; i < 4; ++i)
#pragma unroll
    for (int ks = 0; ks < 2; ++ks)
      aoff[i][ks] = G1_A + (64 * wm + 16 * (i ^ wn) + r) * 128 + (((4 * ks + g) ^ (r & 7)) << 4);
  const int phiL = q | ((g & 1) << 2);
  int boff[4];
#pragma unroll
  for (int T = 0; T < 4; ++T) {
    const int mat = (T >> 1) ^ wm;
    boff[T] = G1_W + mat * 16384 + (8 * g + q) * 256 + (((2 * wn + (T & 1)) ^ phiL) << 5) + p * 8;
  }

  f32x4 acc[4][4];
#pragma unroll
  for (int T = 0; T < 4; ++T)
#pragma unroll
    for (int i = 0; i < 4; ++i) acc[T][i] = f32x4{0.f, 0.f, 0.f, 0.f};
  f32x4 accS = {0.f, 0.f, 0.f, 0.f}, accR = accS, accC[2] = {accS, accS};
  const bf16x8 ones = {0x3f80, 0x3f80, 0x3f80, 0x3f80, 0x3f80, 0x3f80, 0x3f80, 0x3f80};

  struct Frags { s16x4 wl[4], wh[4]; bf16x8 xf[4]; };
  Frags fa, fb2;
  auto read_piece = [&](Frags& f, int kt, int ks, int n) {
    const int b = kt % 3;
    if (n < 4) f.xf[n] = *(const bf16x8*)(smem + b * 16384 + aoff[n][ks]);
    else {
      const int T = (n - 4) >> 1;
      if ((n & 1) == 0) f.wl[T] = tr_read(smem + b * 32768 + ks * 8192 + boff[T]);
      else f.wh[T] = tr_read(smem + b * 32768 + ks * 8192 + 1024 + boff[T]);
    }
  };
  auto mma_one = [&](const Frags& f, int m) {
    const int T = m / 5, i = m % 5;
    if (i < 4) acc[T][i] = mfma16(cat8(f.wl[T], f.wh[T]), f.xf[i], acc[T][i]);
    else if (T == 0) accS = mfma16(f.xf[0], f.xf[0], accS);
    else if (T == 1) accR = mfma16(ones, f.xf[0], accR);
    else if (T == 2) accC[0] = mfma16(cat8(f.wl[0], f.wh[0]), ones, accC[0]);
    else accC[1] = mfma16(cat8(f.wl[1], f.wh[1]), ones, accC[1]);
  };

  wait_vm<12>();
  raw_barrier();
  STAMP(1);
  {
    touch(x + (size_t)(row0 + 64 * (rank & 1) + (tid >> 3)) * D_ + (tid & 7) * 64, smem + K1_DUMMY);
    const int kr = 64 * (mt & 7) + (tid >> 3);
    const int sel = tid & 7, pos = sel % 3;
    int col = f0 + (pos == 0 ? 0 : pos == 1 ? 64 : 126);
    col = col > F_ - 2 ? F_ - 2 : col;
    touch((((sel / 3) & 1) ? wu : wg) + kr * F_ + col, smem + K1_DUMMY);
  }
#pragma unroll
  for (int n = 0; n < 12; ++n) read_piece(fa, 0, 0, n);
  static_for<0, 8>([&](auto KT) {
    constexpr int kt = KT.value;
    SB;
#pragma unroll
    for (int m = 0; m < 16; ++m) {
      mma_one(fa, m);
      if (m < 12) read_piece(fb2, kt, 1, m);
      SB;
    }
    if constexpr (kt < 7) {
      if constexpr (kt < 2) wait_vm<8>(); else if constexpr (kt < 6) wait_vm<6>(); else wait_vm<0>();
      raw_barrier();
      STAMP(2 + kt);
      SB;
    }
#pragma unroll
    for (int m = 16; m < 20; ++m) {
      mma_one(fa, m);
      if (m == 17 && kt < 5) dma_piece(kt + 3, 0);
      SB;
    }
#pragma unroll
    for (int m = 0; m < 20; ++m) {
      mma_one(fb2, m);
      if (m < 12 && kt < 7) read_piece(fa, kt + 1, 0, m);
      if ((m == 1 || m == 5 || m == 9 || m == 13 || m == 17) && kt < 5) dma_piece(kt + 3, 1 + (m >> 2));
      SB;
    }
  });
  __syncthreads();
  STAMP(9);
  float2* st_s = (float2*)smem;
  float* cs_s = (float*)(smem + 1024);
  if (g == (r >> 2)) {
    const int rr = r & 3;
    const float s2 = rr == 0 ? accS[0] : rr == 1 ? accS[1] : rr == 2 ? accS[2] : accS[3];
    const float mean = accR[0] * (1.f / D_);
    float var = s2 * (1.f / D_) - mean * mean;
    var = var < 0.f ? 0.f : var;
    const float mb = round_bf(mean);
    const float vb = round_bf(var);
    const float sd = round_bf(sqrtf(round_bf(vb + 1e-5f)));
    const float2 stv = make_float2(mb, 1.f / sd);
    const int row = 64 * wm + 16 * wn + r;
    st_s[row] = stv;
    if (ft == 0) stats[row0 + row] = stv;
  }
  if (r == 0) {
    *(f32x4*)(cs_s + wm * 128 + 32 * wn + 4 * g) = accC[0];
    *(f32x4*)(cs_s + wm * 128 + 32 * wn + 16 + 4 * g) = accC[1];
  }
  __syncthreads();
  const bool valid = (f0 + 32 * wn) < F_;
  f32x4 ca[2], cb[2];
#pragma unroll
  for (int T = 0; T < 2; ++T) {
    ca[T] = accC[T];
    cb[T] = *(const f32x4*)(cs_s + (wm ^ 1) * 128 + 32 * wn + 16 * T + 4 * g);
  }
#pragma unroll
  for (int i = 0; i < 4; ++i) {
    const int row = 64 * wm + 16 * (i ^ wn) + r;
    const float2 stv = st_s[row];
    const float nm = -stv.x, r2 = stv.y * stv.y;
    unsigned pk[2][2];
#pragma unroll
    for (int T = 0; T < 2; ++T) {
      const f32x4 ga = acc[T][i] + nm * ca[T];
      const f32x4 gb = acc[2 + T][i] + nm * cb[T];
      const f32x4 hv = (ga * r2) * gb;
      pk[T][0] = pk_bf16(hv[0], hv[1]);
      pk[T][1] = pk_bf16(hv[2], hv[3]);
    }
    auto r0 = __builtin_amdgcn_permlane16_swap(pk[0][0], pk[1][0], false, false);
    auto r1 = __builtin_amdgcn_permlane16_swap(pk[0][1], pk[1][1], false, false);
    u32x4 o = {r0[0], r1[0], r0[1], r1[1]};
    if (!valid) o = u32x4{0u, 0u, 0u, 0u};
    store16_wt(hbuf + (size_t)(row0 + row) * FP_ + f0 + 32 * wn + 16 * (g & 1) + 8 * (g >> 1), o);
  }
}

constexpr int PU_PITCH = 520;
DI void prep_block(char* smem, const u16* __restrict__ kin, const u16* __restrict__ vin,
                   const u16* __restrict__ shk, u16* __restrict__ ur, int stripe, int rb) {
  const int tid = threadIdx.x;
  const int rg = tid >> 3, cc = tid & 7;
  const int c0 = 64 * stripe + 8 * cc, s0 = 256 * rb + 4 * rg;
  u32x4 kx[7], vx[4];
#pragma unroll
  for (int i = 0; i < 7; ++i) {
    const int sr = s0 - 3 + i;
    kx[i] = *(const u32x4*)(kin + (size_t)(sr < 0 ? 0 : sr) * D_ + c0);
    if (i < 3 && sr < 0) kx[i] = u32x4{0u, 0u, 0u, 0u};
  }
#pragma unroll
  for (int i = 0; i < 4; ++i) vx[i] = *(const u32x4*)(vin + (size_t)(s0 + i) * D_ + c0);
  u32x4 wv[4];
#pragma unroll
  for (int i = 0; i < 4; ++i) wv[i] = *(const u32x4*)(shk + c0 * 4 + 8 * i);
#pragma unroll
  for (int d = 0; d < 4; ++d) {
#pragma unroll
    for (int hh = 0; hh < 2; ++hh) {
      const float w0 = bf_lo(wv[d][2 * hh]), w1 = bf_hi(wv[d][2 * hh]);
      const float w2 = bf_lo(wv[d][2 * hh + 1]), w3 = bf_hi(wv[d][2 * hh + 1]);
      float kf[7], u[4];
#pragma unroll
      for (int i = 0; i < 7; ++i) kf[i] = hh ? bf_hi(kx[i][d]) : bf_lo(kx[i][d]);
#pragma unroll
      for (int i = 0; i < 4; ++i) {
        float a = kf[i] * w3;
        a = fmaf(kf[i + 1], w2, a);
        a = fmaf(kf[i + 2], w1, a);
        a = fmaf(kf[i + 3], w0, a);
        u[i] = a * (hh ? bf_hi(vx[i][d]) : bf_lo(vx[i][d]));
      }
      const u32x2 o = {pk_bf16(u[3], u[2]), pk_bf16(u[1], u[0])};
      *(u32x2*)(smem + (8 * cc + 2 * d + hh) * PU_PITCH + (252 - 4 * rg) * 2) = o;
    }
  }
  __syncthreads();
#pragma unroll
  for (int i = 0; i < 8; ++i) {
    const int e = i * 512 + tid, ch = e >> 6, pos = e & 63;
    const u32x2 v = *(const u32x2*)(smem + ch * PU_PITCH + pos * 8);
    store8_wt(ur + (size_t)(64 * stripe + ch) * S_ + (S_ - 256 * (rb + 1)) + pos * 4, v);
  }
}

__global__ __launch_bounds__(512, 2) void k1_kernel(const u16* __restrict__ x, const u16* __restrict__ kin,
                                                     const u16* __restrict__ vin, const u16* __restrict__ shk,
                                                     const u16* __restrict__ ssmk,
                                                     const u16* __restrict__ wg, const u16* __restrict__ wu,
                                                     const u16* __restrict__ wd,
                                                     u16* __restrict__ hbuf, u16* __restrict__ ur,
                                                     float2* __restrict__ stats, unsigned* __restrict__ cnt) {
  __shared__ __attribute__((aligned(16))) char smem[K1_LDS];
  const int b = blockIdx.x, xcd = b & 7, idx = b >> 3;
  STAMP_DECL;
  STAMP(0);
  if (idx < 8) {
    prep_block(smem, kin, vin, shk, ur, xcd, idx);
  } else if (idx < 30) {
    const int t = 22 * (xcd & 3) + (idx - 8);
    gemm1_block(smem, x, wg, wu, hbuf, stats, 8 * (xcd >> 2) + (t & 7), t >> 3, (idx - 8) >> 3 STAMP_PASS);
  } else {
    const int part = (idx - 30) * 8 + xcd;
    if (part == 0 && threadIdx.x < 8) cnt[threadIdx.x * 64] = 0u;
    __builtin_amdgcn_s_sleep(100);
    for (int i = threadIdx.x; i < 688; i += 512) touch(wd + (size_t)(part * 688 + i) * 64, smem + K1_DUMMY);
    for (int i = threadIdx.x; i < 1024; i += 512) touch(ssmk + (size_t)(part * 1024 + i) * 64, smem + K1_DUMMY);
    wait_vm<0>();
  }
  STAMP(15);
  STAMP_FLUSH(0);
}

constexpr int K2_STAGE = 32768;
constexpr int K2_NT = FP_ / 128;
constexpr int CQ_PITCH = 4576;
constexpr int CT_PITCH = 16512;
constexpr int K2_Q = 3 * K2_STAGE;
constexpr int K2_TAB = K2_Q + 2 * CQ_PITCH;
constexpr int K2_B3 = K2_TAB;
constexpr int K2_TT = K2_B3 + K2_STAGE;
constexpr int K2_X = K2_TT + 8192;
constexpr int K2_ST = K2_Q;
constexpr int K2_DUMMY = K2_X + 8192;
constexpr int K2_LDS = K2_DUMMY + 256;
DI constexpr int k2_buf(int kt) { return (kt & 3) < 3 ? (kt & 3) * K2_STAGE : K2_B3; }

typedef __attribute__((address_space(1))) unsigned gu32;

__global__ __launch_bounds__(512, 2) void k2_kernel(const u16* __restrict__ x, const u16* __restrict__ hbuf,
                                                     const u16* __restrict__ wd, const u16* __restrict__ ur,
                                                     const u16* __restrict__ ssmk, const u16* __restrict__ ssmd,
                                                     u16* tT, const float2* __restrict__ stats,
                                                     unsigned* cnt, u16* __restrict__ out) {
  __shared__ __attribute__((aligned(16))) char smem[K2_LDS];
  const int tid = threadIdx.x, lane = tid & 63;
  const int w = __builtin_amdgcn_readfirstlane(tid >> 6);
  const int kg = w >> 2, wm = (w >> 1) & 1, wn = w & 1;
  const int r = lane & 15, g = lane >> 4, q = (lane & 15) >> 2, p = lane & 3;
  const int b = blockIdx.x, xcd = b & 7, idx = b >> 3;
  const int row0 = (4 * xcd + (idx >> 3)) * 64, d0 = (idx & 7) * 64;
  STAMP_DECL;
  STAMP(0);

  const int cI = w >> 2, qd = w & 3;
  const int ch = 2 * b + cI;
  char* cq = smem + K2_Q + cI * CQ_PITCH;
  char* ct = smem + K2_TAB + cI * CT_PITCH;
  glds16(ur + (size_t)ch * S_ + (qd * 64 + lane) * 8, cq + qd * 1024);
  unsigned hd[5][3];
  {
    const unsigned* hrow = (const unsigned*)(ssmk + (size_t)ch * S_);
#pragma unroll
    for (int it = 0; it < 5; ++it) {
      const int m = it * 256 + qd * 64 + lane - 8;
#pragma unroll
      for (int k = 0; k < 3; ++k) {
        const int mm = m + k;
        const unsigned v = hrow[mm < 0 ? 0 : mm > 1023 ? 1023 : mm];
        hd[it][k] = (mm < 0 || mm > 1023) ? 0u : v;
      }
    }
  }
  const float dch = __uint_as_float((unsigned)ssmd[ch] << 16);

  unsigned aoffs[2], woffs[2], woffl[2];
#pragma unroll
  for (int e = 0; e < 2; ++e) {
    const int ar = 8 * w + 4 * e + (lane >> 4);
    aoffs[e] = (unsigned)((row0 + ar) * FP_ + (((lane & 15) ^ (ar & 15)) << 3)) * 2u;
    const int kr = 16 * w + 8 * e + (lane >> 3);
    const int phi_s = ((kr >> 1) & 1) | (((kr >> 3) & 1) << 1);
    const int wcol = d0 + ((((((lane & 7) >> 1) ^ phi_s) << 1) | (lane & 1)) << 3);
    woffs[e] = (unsigned)(kr * D_ + wcol) * 2u;
    const int krl = kr > F_ - 1 - 1280 ? F_ - 1 - 1280 : kr;
    woffl[e] = (unsigned)(krl * D_ + wcol) * 2u;
  }
  auto dma_piece = [&](int kt, int n) {
    char* sb = smem + k2_buf(kt);
    const int e = n & 1;
    if (n < 2) glds16s(hbuf + kt * 128, aoffs[e], sb + (2 * w + e) * 1024);
    else glds16s(wd + kt * 128 * D_, kt == K2_NT - 1 ? woffl[e] : woffs[e], sb + 16384 + (2 * w + e) * 1024);
  };
  if (lane < 30 && qd == 0) *(u32x4*)(cq + 4096 + lane * 16) = u32x4{0u, 0u, 0u, 0u};
#pragma unroll
  for (int it = 0; it < 5; ++it) {
    const int i = it * 256 + qd * 64 + lane;
    u32x4 e;
    e[0] = hd[it][0];
    e[1] = hd[it][1];
    e[2] = __builtin_amdgcn_alignbit(hd[it][1], hd[it][0], 16);
    e[3] = __builtin_amdgcn_alignbit(hd[it][2], hd[it][1], 16);
    if (i < 1032) *(u32x4*)(ct + i * 16) = e;
  }
  asm volatile("" ::"v"(hd[4][0]), "v"(hd[4][1]), "v"(hd[4][2]), "v"(dch));
#pragma unroll
  for (int n = 0; n < 4; ++n) dma_piece(0, n);
  {
    const bool ish = tid < 176;
    const int hr = tid / 22, hl = tid % 22;
    int kr = 344 * (idx >> 3) + (tid - 176);
    kr = kr > F_ - 1 ? F_ - 1 : kr;
    const u16* ph = hbuf + (size_t)(row0 + 8 * (idx & 7) + (hr & 7)) * FP_ + hl * 64;
    const u16* pw = wd + (size_t)kr * D_ + d0;
    touch(ish ? ph : pw, smem + K2_DUMMY);
  }
#pragma unroll
  for (int kt = 1; kt < 3; ++kt)
#pragma unroll
    for (int n = 0; n < 4; ++n) dma_piece(kt, n);

  wait_vm<13>();
  raw_barrier();
  STAMP(13);
  {
    f32x4 cacc[8];
#pragma unroll
    for (int n = 0; n < 8; ++n) cacc[n] = f32x4{0.f, 0.f, 0.f, 0.f};
    const char* a_lane = ct + (64 * qd + 1 + r + 8 * g) * 8;
    const char* b_lane = cq + 4064 + 128 * qd - 32 * r + 16 * g;
    s16x4 al[2][8], ah[2][8];
    bf16x8 bq[2][8];
    auto rd = [&](int kk, int n) {
      const int t = n / 3, k = n % 3;
      if (k == 0) al[kk][t] = *(const s16x4*)(a_lane + kk * 256 + t * 2048);
      else if (k == 1) ah[kk][t] = *(const s16x4*)(a_lane + kk * 256 + t * 2048 + 32);
      else bq[kk][t] = *(const bf16x8*)(b_lane + kk * 64 - t * 512);
    };
#pragma unroll
    for (int n = 0; n < 24; ++n) rd(0, n);
    SB;
#pragma unroll
    for (int kk = 0; kk < 2; ++kk) {
      int cnt_m = 0;
#pragma unroll
      for (int t = 0; t < 8; ++t) {
#pragma unroll
        for (int o = 0; o <= t; ++o) {
          if (t + o < 8) {
            cacc[t + o] = mfma16(cat8(al[kk][t], ah[kk][t]), bq[kk][o], cacc[t + o]);
            if (kk == 0 && cnt_m < 24) rd(1, cnt_m);
            ++cnt_m;
            SB;
            if (o < t) {
              cacc[t + o] = mfma16(cat8(al[kk][o], ah[kk][o]), bq[kk][t], cacc[t + o]);
              if (kk == 0 && cnt_m < 24) rd(1, cnt_m);
              ++cnt_m;
              SB;
            }
          }
        }
      }
    }
    raw_barrier();
    char* red = smem + K2_TAB + cI * 24576;
    f32x4 own[2];
#pragma unroll
    for (int n = 0; n < 8; ++n) {
      const int o = n >> 1;
      if (o == qd) own[n & 1] = cacc[n];
      else *(f32x4*)(red + (n * 3 + (qd < o ? qd : qd - 1)) * 1024 + lane * 16) = cacc[n];
    }
    raw_barrier();
#pragma unroll
    for (int i = 0; i < 2; ++i) {
      const int n = 2 * qd + i;
      f32x4 y = own[i];
#pragma unroll
      for (int k = 0; k < 3; ++k) y += *(const f32x4*)(red + (n * 3 + k) * 1024 + lane * 16);
      const int s = 256 * n + 16 * r + 4 * g;
      const u32x2 uu = *(const u32x2*)(cq + 2 * (2044 - s));
      const float t0 = round_bf(y[0]) + round_bf(bf_hi(uu[1]) * dch);
      const float t1 = round_bf(y[1]) + round_bf(bf_lo(uu[1]) * dch);
      const float t2 = round_bf(y[2]) + round_bf(bf_hi(uu[0]) * dch);
      const float t3 = round_bf(y[3]) + round_bf(bf_lo(uu[0]) * dch);
      const u32x2 o = {pk_bf16(t0, t1), pk_bf16(t2, t3)};
      asm volatile("global_store_dwordx2 %0, %1, off sc1\n\ts_nop 1" ::"v"(tT + (size_t)ch * S_ + s), "v"(o) : "memory");
    }
    wait_vm<0>();
    raw_barrier();
    if (tid == 0) __hip_atomic_fetch_add((gu32*)cnt + (b >> 5) * 64, 1u, __ATOMIC_RELAXED, __HIP_MEMORY_SCOPE_AGENT);
  }
  STAMP(14);

  const int erow = row0 + 32 * wm + 16 * kg + r;
  const int edoff = d0 + 32 * wn + 16 * (g & 1) + 8 * (g >> 1);

  int aoff[2][2], boff[2];
#pragma unroll
  for (int i = 0; i < 2; ++i)
#pragma unroll
    for (int hs = 0; hs < 2; ++hs)
      aoff[i][hs] = (32 * wm + 16 * i + r) * 256 + (((4 * (kg + 2 * hs) + g) ^ r) << 4);
  const int phiL = ((q >> 1) & 1) | ((g & 1) << 1);
#pragma unroll
  for (int T = 0; T < 2; ++T)
    boff[T] = 16384 + (32 * kg + 8 * g + q) * 128 + (((2 * wn + T) ^ phiL) << 5) + p * 8;

  f32x4 acc[2][2];
#pragma unroll
  for (int T = 0; T < 2; ++T)
#pragma unroll
    for (int i = 0; i < 2; ++i) acc[T][i] = f32x4{0.f, 0.f, 0.f, 0.f};

  struct Frags2 { bf16x8 hf[2][2]; s16x4 wl[2][2], wh[2][2]; };
  Frags2 fs[2];
  auto read_piece = [&](Frags2& f, int kt, int n) {
    const char* sb = smem + k2_buf(kt);
    const int hs = n / 6, k = n % 6;
    if (k < 2) f.hf[hs][k] = *(const bf16x8*)(sb + aoff[k][hs]);
    else if ((k & 1) == 0) f.wl[hs][(k - 2) >> 1] = tr_read(sb + boff[(k - 2) >> 1] + hs * 8192);
    else f.wh[hs][(k - 2) >> 1] = tr_read(sb + boff[(k - 2) >> 1] + hs * 8192 + 512);
  };
  auto mma_one = [&](const Frags2& f, int m) {
    const int hs = m >> 2, T = (m >> 1) & 1, i = m & 1;
    acc[T][i] = mfma16(cat8(f.wl[hs][T], f.wh[hs][T]), f.hf[hs][i], acc[T][i]);
  };
  u32x4 tval;
  static_for<0, K2_NT + 1>([&](auto KT) {
    constexpr int kt = KT.value;
    if constexpr (kt < K2_NT && kt > 0) {
      if constexpr (kt <= 8) wait_vm<8>();
      else if constexpr (kt == 9) wait_vm<6>();
      else wait_vm<1>();
      raw_barrier();
      STAMP(1 + kt);
    }
    if constexpr (kt == 9) {
      gu32* c = (gu32*)cnt + (idx & 7) * 64;
      for (int spin = 0; spin < (1 << 16); ++spin) {
        if (__hip_atomic_load(c, __ATOMIC_RELAXED, __HIP_MEMORY_SCOPE_AGENT) >= 32u) break;
        __builtin_amdgcn_s_sleep(2);
      }
      __builtin_amdgcn_fence(__ATOMIC_ACQUIRE, "wavefront");
      asm volatile("global_load_dwordx4 %0, %1, off sc1" : "=v"(tval)
                   : "v"(tT + (size_t)(d0 + 8 * w + (lane >> 3)) * S_ + row0 + (lane & 7) * 8) : "memory");
    }
    SB;
#pragma unroll
    for (int n = 0; n < 12; ++n) {
      if (kt < K2_NT) read_piece(fs[kt & 1], kt, n);
      if (kt > 0 && n < 8) mma_one(fs[(kt - 1) & 1], n);
      if (kt + 3 < K2_NT && (n % 3) == 2) dma_piece(kt + 3, n / 3);
      if (kt == 8 && n == 5) glds16(x + (size_t)(row0 + 8 * w + (lane >> 3)) * D_ + d0 + (lane & 7) * 8, smem + K2_X + w * 1024);
      if (kt == 8 && n == 8) glds16(stats + row0 + 2 * (lane & 31), smem + K2_ST);
      SB;
    }
  });
  STAMP(12);
  asm volatile("s_waitcnt vmcnt(0)" : "+v"(tval)::"memory");
  *(u32x4*)(smem + K2_TT + tid * 16) = tval;
  raw_barrier();
  const int lrow = 32 * wm + 16 * kg + r;
  const u32x4 xv = *(const u32x4*)(smem + K2_X + lrow * 128 + (32 * wn + 16 * (g & 1) + 8 * (g >> 1)) * 2);
  const f32x2 stv = *(const f32x2*)(smem + K2_ST + lrow * 8);
  {
    f32x4* xb = (f32x4*)smem;
    const int wq = w & 3;
#pragma unroll
    for (int T = 0; T < 2; ++T) xb[((kg * 4 + wq) * 2 + T) * 64 + lane] = kg ? acc[T][0] : acc[T][1];
    raw_barrier();
    f32x4 fin[2];
#pragma unroll
    for (int T = 0; T < 2; ++T) {
      const f32x4 o = xb[(((kg ^ 1) * 4 + wq) * 2 + T) * 64 + lane];
      fin[T] = (kg ? acc[T][1] : acc[T][0]) + o;
    }
    unsigned pb[2][2], pt[2][2];
#pragma unroll
    for (int T = 0; T < 2; ++T) {
      pb[T][0] = pk_bf16(fin[T][0], fin[T][1]);
      pb[T][1] = pk_bf16(fin[T][2], fin[T][3]);
      const s16x4 tv = tr_read(smem + K2_TT + (32 * wn + 16 * T + 4 * g + q) * 128 + (32 * wm + 16 * kg + 4 * p) * 2);
      const u32x2 tu = __builtin_bit_cast(u32x2, tv);
      pt[T][0] = tu[0];
      pt[T][1] = tu[1];
    }
    auto b0 = __builtin_amdgcn_permlane16_swap(pb[0][0], pb[1][0], false, false);
    auto b1 = __builtin_amdgcn_permlane16_swap(pb[0][1], pb[1][1], false, false);
    auto t0 = __builtin_amdgcn_permlane16_swap(pt[0][0], pt[1][0], false, false);
    auto t1 = __builtin_amdgcn_permlane16_swap(pt[0][1], pt[1][1], false, false);
    const u32x4 bv = {b0[0], b1[0], b0[1], b1[1]};
    const u32x4 tv = {t0[0], t1[0], t0[1], t1[1]};
    u32x4 o;
#pragma unroll
    for (int j = 0; j < 4; ++j) {
      float res[2];
#pragma unroll
      for (int hh = 0; hh < 2; ++hh) {
        const float xe = hh ? bf_hi(xv[j]) : bf_lo(xv[j]);
        const float te = hh ? bf_hi(tv[j]) : bf_lo(tv[j]);
        const float be = hh ? bf_hi(bv[j]) : bf_lo(bv[j]);
        const float xn = round_bf(round_bf(xe - stv[0]) * stv[1]);
        const float a = round_bf(xn * te);
        res[hh] = round_bf(xe + a) + be;
      }
      o[j] = pk_bf16(res[0], res[1]);
    }
    *(u32x4*)(out + (size_t)erow * D_ + edoff) = o;
  }
  STAMP(15);
  STAMP_FLUSH(1);
}

extern "C" void kernel_launch(void* const* d_in, const int* in_sizes, int n_in, void* d_out, int out_size,
                              void* d_ws, size_t ws_size, hipStream_t stream) {
  const u16* q = (const u16*)d_in[0];
  const u16* k = (const u16*)d_in[1];
  const u16* v = (const u16*)d_in[2];
  const u16* shk = (const u16*)d_in[3];
  const u16* ssmk = (const u16*)d_in[4];
  const u16* ssmd = (const u16*)d_in[5];
  const u16* wg = (const u16*)d_in[7];
  const u16* wu = (const u16*)d_in[8];
  const u16* wd = (const u16*)d_in[9];
  char* ws = (char*)d_ws;
  u16* hbuf = (u16*)(ws + WS_H);
  u16* tT = (u16*)(ws + WS_T);
  u16* ur = (u16*)(ws + WS_U);
  float2* stats = (float2*)(ws + WS_ST);
  unsigned* cnt = (unsigned*)(ws + WS_CNT);
  k1_kernel<<<256, 512, 0, stream>>>(q, k, v, shk, ssmk, wg, wu, wd, hbuf, ur, stats, cnt);
  k2_kernel<<<256, 512, 0, stream>>>(q, hbuf, wd, ur, ssmk, ssmd, tT, stats, cnt, (u16*)d_out);
}
```

```cpp
#include <hip/hip_runtime.h>
#include <hip/hip_bf16.h>
#include <stdint.h>

typedef __attribute__((ext_vector_type(8))) short bf16x8;
typedef __attribute__((ext_vector_type(4))) short s16x4;
typedef __attribute__((ext_vector_type(2))) __bf16 bf16x2;
typedef __attribute__((ext_vector_type(4))) float f32x4;
typedef __attribute__((ext_vector_type(2))) float f32x2;
typedef __attribute__((ext_vector_type(4))) unsigned u32x4;
typedef __attribute__((ext_vector_type(2))) unsigned u32x2;
typedef __attribute__((address_space(3))) s16x4* lds_s16x4_ptr;
typedef unsigned short u16;

#define DI __device__ __forceinline__

constexpr int S_ = 2048, D_ = 512, F_ = 1376, FP_ = 1408;

constexpr size_t WS_H = 0;
constexpr size_t WS_T = 6u * 1024 * 1024;
constexpr size_t WS_U = 8u * 1024 * 1024;
constexpr size_t WS_ST = 10u * 1024 * 1024;
constexpr size_t WS_CNT = 10u * 1024 * 1024 + 65536;

template <int N> DI void wait_vm() { asm volatile("s_waitcnt vmcnt(%0)" ::"i"(N) : "memory"); }
DI void wait_lgkm0() { asm volatile("s_waitcnt lgkmcnt(0)" ::: "memory"); }
DI void raw_barrier() {
  asm volatile("s_waitcnt lgkmcnt(0)" ::: "memory");
  __builtin_amdgcn_s_barrier();
  asm volatile("" ::: "memory");
}

DI unsigned pk_bf16(float lo, float hi) {
  f32x2 f = {lo, hi};
  return __builtin_bit_cast(unsigned, __builtin_convertvector(f, bf16x2));
}
DI float bf_lo(unsigned v) { return __uint_as_float(v << 16); }
DI float bf_hi(unsigned v) { return __uint_as_float(v & 0xffff0000u); }
DI float round_bf(float x) { return bf_lo(pk_bf16(x, x)); }
DI float dot2bf(unsigned a, unsigned b, float c) {
  return __builtin_amdgcn_fdot2_f32_bf16(__builtin_bit_cast(bf16x2, a), __builtin_bit_cast(bf16x2, b), c, false);
}
DI s16x4 tr_read(const char* p) {
  return __builtin_amdgcn_ds_read_tr16_b64_v4i16((lds_s16x4_ptr)p);
}
DI bf16x8 cat8(s16x4 lo, s16x4 hi) { return __builtin_shufflevector(lo, hi, 0, 1, 2, 3, 4, 5, 6, 7); }
DI f32x4 mfma16(bf16x8 a, bf16x8 b, f32x4 c) { return __builtin_amdgcn_mfma_f32_16x16x32_bf16(a, b, c, 0, 0, 0); }

DI void glds16(const void* g, char* lds) {
  unsigned keep;
  const unsigned dst = __builtin_amdgcn_readfirstlane((unsigned)(size_t)lds);
  asm volatile("s_mov_b32 %0, m0\n\ts_mov_b32 m0, %2\n\ts_nop 0\n\tglobal_load_lds_dwordx4 %1, off\n\ts_mov_b32 m0, %0"
               : "=&s"(keep) : "v"(g), "s"(dst) : "memory");
}
DI void glds16s(const void* sbase, unsigned voff, char* lds) {
  unsigned keep;
  const unsigned dst = __builtin_amdgcn_readfirstlane((unsigned)(size_t)lds);
  asm volatile("s_mov_b32 %0, m0\n\ts_mov_b32 m0, %3\n\ts_nop 0\n\tglobal_load_lds_dwordx4 %1, %2\n\ts_mov_b32 m0, %0"
               : "=&s"(keep) : "v"(voff), "s"(sbase), "s"(dst) : "memory");
}
DI float mul_s(float a, float s) { float d; asm("v_mul_f32 %0, %2, %1" : "=v"(d) : "v"(a), "s"(s)); return d; }
DI float fma_s(float a, float s, float c) { float d; asm("v_fma_f32 %0, %1, %2, %3" : "=v"(d) : "v"(a), "s"(s), "v"(c)); return d; }
DI float mul_v(float a, float b) { float d; asm("v_mul_f32 %0, %1, %2" : "=v"(d) : "v"(a), "v"(b)); return d; }
DI void touch(const void* g, char* dummy) {
  unsigned keep;
  const unsigned dst = __builtin_amdgcn_readfirstlane((unsigned)(size_t)dummy);
  asm volatile("s_mov_b32 %0, m0\n\ts_mov_b32 m0, %2\n\ts_nop 0\n\tglobal_load_lds_dword %1, off\n\ts_mov_b32 m0, %0"
               : "=&s"(keep) : "v"(g), "s"(dst) : "memory");
}

#define WT_MODE 1
DI void store16_wt(void* p, u32x4 v) {
  asm volatile("global_store_dwordx4 %0, %1, off sc1\n\ts_nop 1" ::"v"(p), "v"(v) : "memory");
}
DI void store8_wt(void* p, u32x2 v) {
  asm volatile("global_store_dwordx2 %0, %1, off sc1\n\ts_nop 1" ::"v"(p), "v"(v) : "memory");
}

#define STAMP_DECL do {} while (0)
#define STAMP(I) do {} while (0)
#define STAMP_FLUSH(K) do {} while (0)
#define STAMP_ARG
#define STAMP_PASS

template <int I, int N, class Fn> DI void static_for(Fn&& fn) {
  if constexpr (I < N) {
    fn(std::integral_constant<int, I>{});
    static_for<I + 1, N>(fn);
  }
}

constexpr int K1_DUMMY = 147456;
constexpr int K1_LDS = K1_DUMMY + 256;

constexpr int G1_A = 0;
constexpr int G1_W = 49152;
#define SB __builtin_amdgcn_sched_barrier(0)

DI void gemm1_block(char* smem, const u16* __restrict__ x, const u16* __restrict__ wg, const u16* __restrict__ wu,
                    u16* __restrict__ hbuf, float2* __restrict__ stats, int mt, int ft, int rank STAMP_ARG) {
  const int tid = threadIdx.x, lane = tid & 63;
  const int w = __builtin_amdgcn_readfirstlane(tid >> 6);
  const int wm = w >> 2, wn = w & 3;
  const int r = lane & 15, g = lane >> 4, q = (lane & 15) >> 2, p = lane & 3;
  const int row0 = mt * 128, f0 = ft * 128;

  unsigned aoffs[2], woffs[2];
#pragma unroll
  for (int e = 0; e < 2; ++e) {
    const int i = 2 * w + e;
    const int row = 8 * i + (lane >> 3);
    aoffs[e] = (unsigned)((row0 + row) * D_ + (((lane & 7) ^ (row & 7)) << 3)) * 2u;
    const int kr = 4 * i + (lane >> 4);
    const int phi = (kr & 3) | (((kr >> 3) & 1) << 2);
    const int sc = ((((lane & 15) >> 1) ^ phi) << 1) | (lane & 1);
    int col = f0 + sc * 8;
    col = col > F_ - 8 ? F_ - 8 : col;
    woffs[e] = (unsigned)(kr * F_ + col) * 2u;
  }
  auto dma_piece = [&](int kt, int n) {
    const int b = kt % 3, e = n & 1;
    if (n < 2) glds16s(x + kt * 64, aoffs[e], smem + G1_A + b * 16384 + (2 * w + e) * 1024);
    else if (n < 4) glds16s(wg + kt * 64 * F_, woffs[e], smem + G1_W + b * 32768 + (2 * w + e) * 1024);
    else glds16s(wu + kt * 64 * F_, woffs[e], smem + G1_W + b * 32768 + 16384 + (2 * w + e) * 1024);
  };
#pragma unroll
  for (int kt = 0; kt < 3; ++kt)
#pragma unroll
    for (int n = 0; n < 6; ++n) dma_piece(kt, n);

  int aoff[4][2];
#pragma unroll
  for (int i = 0; i < 4; ++i)
#pragma unroll
    for (int ks = 0; ks < 2; ++ks)
      aoff[i][ks] = G1_A + (64 * wm + 16 * (i ^ wn) + r) * 128 + (((4 * ks + g) ^ (r & 7)) << 4);
  const int phiL = q | ((g & 1) << 2);
  int boff[4];
#pragma unroll
  for (int T = 0; T < 4; ++T) {
    const int mat = (T >> 1) ^ wm;
    boff[T] = G1_W + mat * 16384 + (8 * g + q) * 256 + (((2 * wn + (T & 1)) ^ phiL) << 5) + p * 8;
  }

  f32x4 acc[4][4];
#pragma unroll
  for (int T = 0; T < 4; ++T)
#pragma unroll
    for (int i = 0; i < 4; ++i) acc[T][i] = f32x4{0.f, 0.f, 0.f, 0.f};
  f32x4 accS = {0.f, 0.f, 0.f, 0.f}, accR = accS, accC[2] = {accS, accS};
  const bf16x8 ones = {0x3f80, 0x3f80, 0x3f80, 0x3f80, 0x3f80, 0x3f80, 0x3f80, 0x3f80};

  struct Frags { s16x4 wl[4], wh[4]; bf16x8 xf[4]; };
  Frags fa, fb2;
  auto read_piece = [&](Frags& f, int kt, int ks, int n) {
    const int b = kt % 3;
    if (n < 4) f.xf[n] = *(const bf16x8*)(smem + b * 16384 + aoff[n][ks]);
    else {
      const int T = (n - 4) >> 1;
      if ((n & 1) == 0) f.wl[T] = tr_read(smem + b * 32768 + ks * 8192 + boff[T]);
      else f.wh[T] = tr_read(smem + b * 32768 + ks * 8192 + 1024 + boff[T]);
    }
  };
  auto mma_one = [&](const Frags& f, int m) {
    const int T = m / 5, i = m % 5;
    if (i < 4) acc[T][i] = mfma16(cat8(f.wl[T], f.wh[T]), f.xf[i], acc[T][i]);
    else if (T == 0) accS = mfma16(f.xf[0], f.xf[0], accS);
    else if (T == 1) accR = mfma16(ones, f.xf[0], accR);
    else if (T == 2) accC[0] = mfma16(cat8(f.wl[0], f.wh[0]), ones, accC[0]);
    else accC[1] = mfma16(cat8(f.wl[1], f.wh[1]), ones, accC[1]);
  };

  wait_vm<12>();
  raw_barrier();
  STAMP(1);
  {
    touch(x + (size_t)(row0 + 64 * (rank & 1) + (tid >> 3)) * D_ + (tid & 7) * 64, smem + K1_DUMMY);
    const int kr = 64 * (mt & 7) + (tid >> 3);
    const int sel = tid & 7, pos = sel % 3;
    int col = f0 + (pos == 0 ? 0 : pos == 1 ? 64 : 126);
    col = col > F_ - 2 ? F_ - 2 : col;
    touch((((sel / 3) & 1) ? wu : wg) + kr * F_ + col, smem + K1_DUMMY);
  }
#pragma unroll
  for (int n = 0; n < 12; ++n) read_piece(fa, 0, 0, n);
  static_for<0, 8>([&](auto KT) {
    constexpr int kt = KT.value;
    SB;
#pragma unroll
    for (int m = 0; m < 16; ++m) {
      mma_one(fa, m);
      if (m < 12) read_piece(fb2, kt, 1, m);
      SB;
    }
    if constexpr (kt < 7) {
      if constexpr (kt < 2) wait_vm<8>(); else if constexpr (kt < 6) wait_vm<6>(); else wait_vm<0>();
      raw_barrier();
      STAMP(2 + kt);
      SB;
    }
#pragma unroll
    for (int m = 16; m < 20; ++m) {
      mma_one(fa, m);
      if (m == 17 && kt < 5) dma_piece(kt + 3, 0);
      SB;
    }
#pragma unroll
    for (int m = 0; m < 20; ++m) {
      mma_one(fb2, m);
      if (m < 12 && kt < 7) read_piece(fa, kt + 1, 0, m);
      if ((m == 1 || m == 5 || m == 9 || m == 13 || m == 17) && kt < 5) dma_piece(kt + 3, 1 + (m >> 2));
      SB;
    }
  });
  __syncthreads();
  STAMP(9);
  float2* st_s = (float2*)smem;
  float* cs_s = (float*)(smem + 1024);
  if (g == (r >> 2)) {
    const int rr = r & 3;
    const float s2 = rr == 0 ? accS[0] : rr == 1 ? accS[1] : rr == 2 ? accS[2] : accS[3];
    const float mean = accR[0] * (1.f / D_);
    float var = s2 * (1.f / D_) - mean * mean;
    var = var < 0.f ? 0.f : var;
    const float mb = round_bf(mean);
    const float vb = round_bf(var);
    const float sd = round_bf(sqrtf(round_bf(vb + 1e-5f)));
    const float2 stv = make_float2(mb, 1.f / sd);
    const int row = 64 * wm + 16 * wn + r;
    st_s[row] = stv;
    if (ft == 0) stats[row0 + row] = stv;
  }
  if (r == 0) {
    *(f32x4*)(cs_s + wm * 128 + 32 * wn + 4 * g) = accC[0];
    *(f32x4*)(cs_s + wm * 128 + 32 * wn + 16 + 4 * g) = accC[1];
  }
  __syncthreads();
  const bool valid = (f0 + 32 * wn) < F_;
  f32x4 ca[2], cb[2];
#pragma unroll
  for (int T = 0; T < 2; ++T) {
    ca[T] = accC[T];
    cb[T] = *(const f32x4*)(cs_s + (wm ^ 1) * 128 + 32 * wn + 16 * T + 4 * g);
  }
#pragma unroll
  for (int i = 0; i < 4; ++i) {
    const int row = 64 * wm + 16 * (i ^ wn) + r;
    const float2 stv = st_s[row];
    const float nm = -stv.x, r2 = stv.y * stv.y;
    unsigned pk[2][2];
#pragma unroll
    for (int T = 0; T < 2; ++T) {
      const f32x4 ga = acc[T][i] + nm * ca[T];
      const f32x4 gb = acc[2 + T][i] + nm * cb[T];
      const f32x4 hv = (ga * r2) * gb;
      pk[T][0] = pk_bf16(hv[0], hv[1]);
      pk[T][1] = pk_bf16(hv[2], hv[3]);
    }
    auto r0 = __builtin_amdgcn_permlane16_swap(pk[0][0], pk[1][0], false, false);
    auto r1 = __builtin_amdgcn_permlane16_swap(pk[0][1], pk[1][1], false, false);
    u32x4 o = {r0[0], r1[0], r0[1], r1[1]};
    if (!valid) o = u32x4{0u, 0u, 0u, 0u};
    store16_wt(hbuf + (size_t)(row0 + row) * FP_ + f0 + 32 * wn + 16 * (g & 1) + 8 * (g >> 1), o);
  }
}

constexpr int PU_PITCH = 520;
DI void prep_block(char* smem, const u16* __restrict__ kin, const u16* __restrict__ vin,
                   const u16* __restrict__ shk, u16* __restrict__ ur, int stripe, int rb) {
  const int tid = threadIdx.x;
  const int rg = tid >> 3, cc = tid & 7;
  const int c0 = 64 * stripe + 8 * cc, s0 = 256 * rb + 4 * rg;
  u32x4 kx[7], vx[4];
#pragma unroll
  for (int i = 0; i < 7; ++i) {
    const int sr = s0 - 3 + i;
    kx[i] = *(const u32x4*)(kin + (size_t)(sr < 0 ? 0 : sr) * D_ + c0);
    if (i < 3 && sr < 0) kx[i] = u32x4{0u, 0u, 0u, 0u};
  }
#pragma unroll
  for (int i = 0; i < 4; ++i) vx[i] = *(const u32x4*)(vin + (size_t)(s0 + i) * D_ + c0);
  u32x4 wv[4];
#pragma unroll
  for (int i = 0; i < 4; ++i) wv[i] = *(const u32x4*)(shk + c0 * 4 + 8 * i);
#pragma unroll
  for (int d = 0; d < 4; ++d) {
#pragma unroll
    for (int hh = 0; hh < 2; ++hh) {
      const float w0 = bf_lo(wv[d][2 * hh]), w1 = bf_hi(wv[d][2 * hh]);
      const float w2 = bf_lo(wv[d][2 * hh + 1]), w3 = bf_hi(wv[d][2 * hh + 1]);
      float kf[7], u[4];
#pragma unroll
      for (int i = 0; i < 7; ++i) kf[i] = hh ? bf_hi(kx[i][d]) : bf_lo(kx[i][d]);
#pragma unroll
      for (int i = 0; i < 4; ++i) {
        float a = kf[i] * w3;
        a = fmaf(kf[i + 1], w2, a);
        a = fmaf(kf[i + 2], w1, a);
        a = fmaf(kf[i + 3], w0, a);
        u[i] = a * (hh ? bf_hi(vx[i][d]) : bf_lo(vx[i][d]));
      }
      const u32x2 o = {pk_bf16(u[3], u[2]), pk_bf16(u[1], u[0])};
      *(u32x2*)(smem + (8 * cc + 2 * d + hh) * PU_PITCH + (252 - 4 * rg) * 2) = o;
    }
  }
  __syncthreads();
#pragma unroll
  for (int i = 0; i < 8; ++i) {
    const int e = i * 512 + tid, ch = e >> 6, pos = e & 63;
    const u32x2 v = *(const u32x2*)(smem + ch * PU_PITCH + pos * 8);
    store8_wt(ur + (size_t)(64 * stripe + ch) * S_ + (S_ - 256 * (rb + 1)) + pos * 4, v);
  }
}

__global__ __launch_bounds__(512, 2) void k1_kernel(const u16* __restrict__ x, const u16* __restrict__ kin,
                                                     const u16* __restrict__ vin, const u16* __restrict__ shk,
                                                     const u16* __restrict__ ssmk,
                                                     const u16* __restrict__ wg, const u16* __restrict__ wu,
                                                     const u16* __restrict__ wd,
                                                     u16* __restrict__ hbuf, u16* __restrict__ ur,
                                                     float2* __restrict__ stats, unsigned* __restrict__ cnt) {
  __shared__ __attribute__((aligned(16))) char smem[K1_LDS];
  const int b = blockIdx.x, xcd = b & 7, idx = b >> 3;
  STAMP_DECL;
  STAMP(0);
  if (idx < 8) {
    prep_block(smem, kin, vin, shk, ur, xcd, idx);
  } else if (idx < 30) {
    const int t = 22 * (xcd & 3) + (idx - 8);
    gemm1_block(smem, x, wg, wu, hbuf, stats, 8 * (xcd >> 2) + (t & 7), t >> 3, (idx - 8) >> 3 STAMP_PASS);
  } else {
    const int part = (idx - 30) * 8 + xcd;
    if (part == 0 && threadIdx.x < 8) cnt[threadIdx.x * 64] = 0u;
    __builtin_amdgcn_s_sleep(100);
    for (int i = threadIdx.x; i < 688; i += 512) touch(wd + (size_t)(part * 688 + i) * 64, smem + K1_DUMMY);
    for (int i = threadIdx.x; i < 1024; i += 512) touch(ssmk + (size_t)(part * 1024 + i) * 64, smem + K1_DUMMY);
    wait_vm<0>();
  }
  STAMP(15);
  STAMP_FLUSH(0);
}

constexpr int K2_STAGE = 32768;
constexpr int K2_NT = FP_ / 128;
constexpr int CQ_PITCH = 4576;
constexpr int CT_PITCH = 16512;
constexpr int K2_Q = 3 * K2_STAGE;
constexpr int K2_TAB = K2_Q + 2 * CQ_PITCH;
constexpr int K2_B3 = K2_TAB;
constexpr int K2_TT = K2_B3 + K2_STAGE;
constexpr int K2_X = K2_TT + 8192;
constexpr int K2_ST = K2_Q;
constexpr int K2_DUMMY = K2_X + 8192;
constexpr int K2_LDS = K2_DUMMY + 256;
DI constexpr int k2_buf(int kt) { return (kt & 3) < 3 ? (kt & 3) * K2_STAGE : K2_B3; }

typedef __attribute__((address_space(1))) unsigned gu32;

constexpr int conv_pair(int m) {
  int c = 0;
  for (int t = 0; t < 8; ++t)
    for (int o = 0; o <= t; ++o)
      if (t + o < 8) {
        if (c++ == m) return t * 8 + o;
        if (o < t && c++ == m) return o * 8 + t;
      }
  return 0;
}

__global__ __launch_bounds__(512, 2) void k2_kernel(const u16* __restrict__ x, const u16* __restrict__ hbuf,
                                                     const u16* __restrict__ wd, const u16* __restrict__ ur,
                                                     const u16* __restrict__ ssmk, const u16* __restrict__ ssmd,
                                                     u16* tT, const float2* __restrict__ stats,
                                                     unsigned* cnt, u16* __restrict__ out) {
  __shared__ __attribute__((aligned(16))) char smem[K2_LDS];
  const int tid = threadIdx.x, lane = tid & 63;
  const int w = __builtin_amdgcn_readfirstlane(tid >> 6);
  const int kg = w >> 2, wm = (w >> 1) & 1, wn = w & 1;
  const int r = lane & 15, g = lane >> 4, q = (lane & 15) >> 2, p = lane & 3;
  const int b = blockIdx.x, xcd = b & 7, idx = b >> 3;
  const int row0 = (4 * xcd + (idx >> 3)) * 64, d0 = (idx & 7) * 64;
  STAMP_DECL;
  STAMP(0);

  const int cI = w >> 2, qd = w & 3;
  const int ch = 2 * b + cI;
  char* cq = smem + K2_Q + cI * CQ_PITCH;
  char* ct = smem + K2_TAB + cI * CT_PITCH;
  glds16(ur + (size_t)ch * S_ + (qd * 64 + lane) * 8, cq + qd * 1024);
  unsigned hd[5][3];
  {
    const unsigned* hrow = (const unsigned*)(ssmk + (size_t)ch * S_);
#pragma unroll
    for (int it = 0; it < 5; ++it) {
      const int m = it * 256 + qd * 64 + lane - 8;
#pragma unroll
      for (int k = 0; k < 3; ++k) {
        const int mm = m + k;
        const unsigned v = hrow[mm < 0 ? 0 : mm > 1023 ? 1023 : mm];
        hd[it][k] = (mm < 0 || mm > 1023) ? 0u : v;
      }
    }
  }
  const float dch = __uint_as_float((unsigned)ssmd[ch] << 16);

  unsigned aoffs[2], woffs[2], woffl[2];
#pragma unroll
  for (int e = 0; e < 2; ++e) {
    const int ar = 8 * w + 4 * e + (lane >> 4);
    aoffs[e] = (unsigned)((row0 + ar) * FP_ + (((lane & 15) ^ (ar & 15)) << 3)) * 2u;
    const int kr = 16 * w + 8 * e + (lane >> 3);
    const int phi_s = ((kr >> 1) & 1) | (((kr >> 3) & 1) << 1);
    const int wcol = d0 + ((((((lane & 7) >> 1) ^ phi_s) << 1) | (lane & 1)) << 3);
    woffs[e] = (unsigned)(kr * D_ + wcol) * 2u;
    const int krl = kr > F_ - 1 - 1280 ? F_ - 1 - 1280 : kr;
    woffl[e] = (unsigned)(krl * D_ + wcol) * 2u;
  }
  auto dma_piece = [&](int kt, int n) {
    char* sb = smem + k2_buf(kt);
    const int e = n & 1;
    if (n < 2) glds16s(hbuf + kt * 128, aoffs[e], sb + (2 * w + e) * 1024);
    else glds16s(wd + kt * 128 * D_, kt == K2_NT - 1 ? woffl[e] : woffs[e], sb + 16384 + (2 * w + e) * 1024);
  };
  if (lane < 30 && qd == 0) *(u32x4*)(cq + 4096 + lane * 16) = u32x4{0u, 0u, 0u, 0u};
#pragma unroll
  for (int it = 0; it < 5; ++it) {
    const int i = it * 256 + qd * 64 + lane;
    u32x4 e;
    e[0] = hd[it][0];
    e[1] = hd[it][1];
    e[2] = __builtin_amdgcn_alignbit(hd[it][1], hd[it][0], 16);
    e[3] = __builtin_amdgcn_alignbit(hd[it][2], hd[it][1], 16);
    if (i < 1032) *(u32x4*)(ct + i * 16) = e;
  }
  asm volatile("" ::"v"(hd[4][0]), "v"(hd[4][1]), "v"(hd[4][2]), "v"(dch));
  const u16* ptouch;
  {
    const int hr = tid / 22, hl = tid % 22;
    int kr = 344 * (idx >> 3) + (tid - 176);
    kr = kr > F_ - 1 ? F_ - 1 : kr;
    const u16* ph = hbuf + (size_t)(row0 + 8 * (idx & 7) + (hr & 7)) * FP_ + hl * 64;
    const u16* pw = wd + (size_t)kr * D_ + d0;
    ptouch = tid < 176 ? ph : pw;
  }
  wait_vm<0>();
  raw_barrier();
  STAMP(13);
  {
    f32x4 cacc[8];
#pragma unroll
    for (int n = 0; n < 8; ++n) cacc[n] = f32x4{0.f, 0.f, 0.f, 0.f};
    const char* a_lane = ct + (64 * qd + 1 + r + 8 * g) * 8;
    const char* b_lane = cq + 4064 + 128 * qd - 32 * r + 16 * g;
    s16x4 al[2][8], ah[2][8];
    bf16x8 bq[2][8];
    auto rd = [&](int kk, int n) {
      const int t = n / 3, k = n % 3;
      if (k == 0) al[kk][t] = *(const s16x4*)(a_lane + kk * 256 + t * 2048);
      else if (k == 1) ah[kk][t] = *(const s16x4*)(a_lane + kk * 256 + t * 2048 + 32);
      else bq[kk][t] = *(const bf16x8*)(b_lane + kk * 64 - t * 512);
    };
#pragma unroll
    for (int n = 0; n < 24; ++n) rd(0, n);
    SB;
    auto side = [&](int sl) {
      if (sl < 24) rd(1, sl);
      if (sl == 1) touch(ptouch, smem + K2_DUMMY);
      if (sl % 5 == 3 && sl / 5 < 12) dma_piece((sl / 5) >> 2, (sl / 5) & 3);
      SB;
    };
    static_for<0, 72>([&](auto SL) {
      constexpr int sl = SL.value, kk = sl / 36;
      constexpr int ai = conv_pair(sl % 36) >> 3, bi = conv_pair(sl % 36) & 7;
      cacc[ai + bi] = mfma16(cat8(al[kk][ai], ah[kk][ai]), bq[kk][bi], cacc[ai + bi]);
      side(sl);
    });
    raw_barrier();
    char* red = smem + K2_TAB + cI * 24576;
    f32x4 own[2];
#pragma unroll
    for (int n = 0; n < 8; ++n) {
      const int o = n >> 1;
      if (o == qd) own[n & 1] = cacc[n];
      else *(f32x4*)(red + (n * 3 + (qd < o ? qd : qd - 1)) * 1024 + lane * 16) = cacc[n];
    }
    raw_barrier();
#pragma unroll
    for (int i = 0; i < 2; ++i) {
      const int n = 2 * qd + i;
      f32x4 y = own[i];
#pragma unroll
      for (int k = 0; k < 3; ++k) y += *(const f32x4*)(red + (n * 3 + k) * 1024 + lane * 16);
      const int s = 256 * n + 16 * r + 4 * g;
      const u32x2 uu = *(const u32x2*)(cq + 2 * (2044 - s));
      const float t0 = round_bf(y[0]) + round_bf(bf_hi(uu[1]) * dch);
      const float t1 = round_bf(y[1]) + round_bf(bf_lo(uu[1]) * dch);
      const float t2 = round_bf(y[2]) + round_bf(bf_hi(uu[0]) * dch);
      const float t3 = round_bf(y[3]) + round_bf(bf_lo(uu[0]) * dch);
      const u32x2 o = {pk_bf16(t0, t1), pk_bf16(t2, t3)};
      asm volatile("global_store_dwordx2 %0, %1, off sc1\n\ts_nop 1" ::"v"(tT + (size_t)ch * S_ + s), "v"(o) : "memory");
    }
    raw_barrier();
  }
  STAMP(14);

  const int erow = row0 + 32 * wm + 16 * kg + r;
  const int edoff = d0 + 32 * wn + 16 * (g & 1) + 8 * (g >> 1);

  int aoff[2][2], boff[2];
#pragma unroll
  for (int i = 0; i < 2; ++i)
#pragma unroll
    for (int hs = 0; hs < 2; ++hs)
      aoff[i][hs] = (32 * wm + 16 * i + r) * 256 + (((4 * (kg + 2 * hs) + g) ^ r) << 4);
  const int phiL = ((q >> 1) & 1) | ((g & 1) << 1);
#pragma unroll
  for (int T = 0; T < 2; ++T)
    boff[T] = 16384 + (32 * kg + 8 * g + q) * 128 + (((2 * wn + T) ^ phiL) << 5) + p * 8;

  f32x4 acc[2][2];
#pragma unroll
  for (int T = 0; T < 2; ++T)
#pragma unroll
    for (int i = 0; i < 2; ++i) acc[T][i] = f32x4{0.f, 0.f, 0.f, 0.f};

  struct Frags2 { bf16x8 hf[2][2]; s16x4 wl[2][2], wh[2][2]; };
  Frags2 fs[2];
  auto read_piece = [&](Frags2& f, int kt, int n) {
    const char* sb = smem + k2_buf(kt);
    const int hs = n / 6, k = n % 6;
    if (k < 2) f.hf[hs][k] = *(const bf16x8*)(sb + aoff[k][hs]);
    else if ((k & 1) == 0) f.wl[hs][(k - 2) >> 1] = tr_read(sb + boff[(k - 2) >> 1] + hs * 8192);
    else f.wh[hs][(k - 2) >> 1] = tr_read(sb + boff[(k - 2) >> 1] + hs * 8192 + 512);
  };
  auto mma_one = [&](const Frags2& f, int m) {
    const int hs = m >> 2, T = (m >> 1) & 1, i = m & 1;
    acc[T][i] = mfma16(cat8(f.wl[hs][T], f.wh[hs][T]), f.hf[hs][i], acc[T][i]);
  };
  u32x4 tval;
  static_for<0, K2_NT + 1>([&](auto KT) {
    constexpr int kt = KT.value;
    if constexpr (kt < K2_NT) {
      if constexpr (kt == 0) wait_vm<10>();
      else if constexpr (kt == 1) wait_vm<10>();
      else if constexpr (kt <= 8) wait_vm<8>();
      else if constexpr (kt == 9) wait_vm<6>();
      else wait_vm<1>();
      raw_barrier();
      STAMP(1 + kt);
      if constexpr (kt == 2)
        if (tid == 0) __hip_atomic_fetch_add((gu32*)cnt + (b >> 5) * 64, 1u, __ATOMIC_RELAXED, __HIP_MEMORY_SCOPE_AGENT);
    }
    if constexpr (kt == 9) {
      gu32* c = (gu32*)cnt + (idx & 7) * 64;
      for (int spin = 0; spin < (1 << 16); ++spin) {
        if (__hip_atomic_load(c, __ATOMIC_RELAXED, __HIP_MEMORY_SCOPE_AGENT) >= 32u) break;
        __builtin_amdgcn_s_sleep(2);
      }
      __builtin_amdgcn_fence(__ATOMIC_ACQUIRE, "wavefront");
      asm volatile("global_load_dwordx4 %0, %1, off sc1" : "=v"(tval)
                   : "v"(tT + (size_t)(d0 + 8 * w + (lane >> 3)) * S_ + row0 + (lane & 7) * 8) : "memory");
    }
    SB;
#pragma unroll
    for (int n = 0; n < 12; ++n) {
      if (kt < K2_NT) read_piece(fs[kt & 1], kt, n);
      if (kt > 0 && n < 8) mma_one(fs[(kt - 1) & 1], n);
      if (kt + 3 < K2_NT && (n % 3) == 2) dma_piece(kt + 3, n / 3);
      if (kt == 8 && n == 5) glds16(x + (size_t)(row0 + 8 * w + (lane >> 3)) * D_ + d0 + (lane & 7) * 8, smem + K2_X + w * 1024);
      if (kt == 8 && n == 8) glds16(stats + row0 + 2 * (lane & 31), smem + K2_ST);
      SB;
    }
  });
  STAMP(12);
  asm volatile("s_waitcnt vmcnt(0)" : "+v"(tval)::"memory");
  *(u32x4*)(smem + K2_TT + tid * 16) = tval;
  raw_barrier();
  const int lrow = 32 * wm + 16 * kg + r;
  const u32x4 xv = *(const u32x4*)(smem + K2_X + lrow * 128 + (32 * wn + 16 * (g & 1) + 8 * (g >> 1)) * 2);
  const f32x2 stv = *(const f32x2*)(smem + K2_ST + lrow * 8);
  {
    f32x4* xb = (f32x4*)smem;
    const int wq = w & 3;
#pragma unroll
    for (int T = 0; T < 2; ++T) xb[((kg * 4 + wq) * 2 + T) * 64 + lane] = kg ? acc[T][0] : acc[T][1];
    raw_barrier();
    f32x4 fin[2];
#pragma unroll
    for (int T = 0; T < 2; ++T) {
      const f32x4 o = xb[(((kg ^ 1) * 4 + wq) * 2 + T) * 64 + lane];
      fin[T] = (kg ? acc[T][1] : acc[T][0]) + o;
    }
    unsigned pb[2][2], pt[2][2];
#pragma unroll
    for (int T = 0; T < 2; ++T) {
      pb[T][0] = pk_bf16(fin[T][0], fin[T][1]);
      pb[T][1] = pk_bf16(fin[T][2], fin[T][3]);
      const s16x4 tv = tr_read(smem + K2_TT + (32 * wn + 16 * T + 4 * g + q) * 128 + (32 * wm + 16 * kg + 4 * p) * 2);
      const u32x2 tu = __builtin_bit_cast(u32x2, tv);
      pt[T][0] = tu[0];
      pt[T][1] = tu[1];
    }
    auto b0 = __builtin_amdgcn_permlane16_swap(pb[0][0], pb[1][0], false, false);
    auto b1 = __builtin_amdgcn_permlane16_swap(pb[0][1], pb[1][1], false, false);
    auto t0 = __builtin_amdgcn_permlane16_swap(pt[0][0], pt[1][0], false, false);
    auto t1 = __builtin_amdgcn_permlane16_swap(pt[0][1], pt[1][1], false, false);
    const u32x4 bv = {b0[0], b1[0], b0[1], b1[1]};
    const u32x4 tv = {t0[0], t1[0], t0[1], t1[1]};
    u32x4 o;
#pragma unroll
    for (int j = 0; j < 4; ++j) {
      float res[2];
#pragma unroll
      for (int hh = 0; hh < 2; ++hh) {
        const float xe = hh ? bf_hi(xv[j]) : bf_lo(xv[j]);
        const float te = hh ? bf_hi(tv[j]) : bf_lo(tv[j]);
        const float be = hh ? bf_hi(bv[j]) : bf_lo(bv[j]);
        const float xn = round_bf(round_bf(xe - stv[0]) * stv[1]);
        const float a = round_bf(xn * te);
        res[hh] = round_bf(xe + a) + be;
      }
      o[j] = pk_bf16(res[0], res[1]);
    }
    *(u32x4*)(out + (size_t)erow * D_ + edoff) = o;
  }
  STAMP(15);
  STAMP_FLUSH(1);
}

extern "C" void kernel_launch(void* const* d_in, const int* in_sizes, int n_in, void* d_out, int out_size,
                              void* d_ws, size_t ws_size, hipStream_t stream) {
  const u16* q = (const u16*)d_in[0];
  const u16* k = (const u16*)d_in[1];
  const u16* v = (const u16*)d_in[2];
  const u16* shk = (const u16*)d_in[3];
  const u16* ssmk = (const u16*)d_in[4];
  const u16* ssmd = (const u16*)d_in[5];
  const u16* wg = (const u16*)d_in[7];
  const u16* wu = (const u16*)d_in[8];
  const u16* wd = (const u16*)d_in[9];
  char* ws = (char*)d_ws;
  u16* hbuf = (u16*)(ws + WS_H);
  u16* tT = (u16*)(ws + WS_T);
  u16* ur = (u16*)(ws + WS_U);
  float2* stats = (float2*)(ws + WS_ST);
  unsigned* cnt = (unsigned*)(ws + WS_CNT);
  k1_kernel<<<256, 512, 0, stream>>>(q, k, v, shk, ssmk, wg, wu, wd, hbuf, ur, stats, cnt);
  k2_kernel<<<256, 512, 0, stream>>>(q, hbuf, wd, ur, ssmk, ssmd, tT, stats, cnt, (u16*)d_out);
}
```

```cpp
#include <hip/hip_runtime.h>
#include <hip/hip_bf16.h>
#include <stdint.h>

typedef __attribute__((ext_vector_type(8))) short bf16x8;
typedef __attribute__((ext_vector_type(4))) short s16x4;
typedef __attribute__((ext_vector_type(2))) __bf16 bf16x2;
typedef __attribute__((ext_vector_type(4))) float f32x4;
typedef __attribute__((ext_vector_type(2))) float f32x2;
typedef __attribute__((ext_vector_type(4))) unsigned u32x4;
typedef __attribute__((ext_vector_type(2))) unsigned u32x2;
typedef __attribute__((address_space(3))) s16x4* lds_s16x4_ptr;
typedef unsigned short u16;

#define DI __device__ __forceinline__

constexpr int S_ = 2048, D_ = 512, F_ = 1376, FP_ = 1408;

constexpr size_t WS_H = 0;
constexpr size_t WS_T = 6u * 1024 * 1024;
constexpr size_t WS_U = 8u * 1024 * 1024;
constexpr size_t WS_ST = 10u * 1024 * 1024;
constexpr size_t WS_CNT = 10u * 1024 * 1024 + 65536;

template <int N> DI void wait_vm() { asm volatile("s_waitcnt vmcnt(%0)" ::"i"(N) : "memory"); }
DI void wait_lgkm0() { asm volatile("s_waitcnt lgkmcnt(0)" ::: "memory"); }
DI void raw_barrier() {
  asm volatile("s_waitcnt lgkmcnt(0)" ::: "memory");
  __builtin_amdgcn_s_barrier();
  asm volatile("" ::: "memory");
}

DI unsigned pk_bf16(float lo, float hi) {
  f32x2 f = {lo, hi};
  return __builtin_bit_cast(unsigned, __builtin_convertvector(f, bf16x2));
}
DI float bf_lo(unsigned v) { return __uint_as_float(v << 16); }
DI float bf_hi(unsigned v) { return __uint_as_float(v & 0xffff0000u); }
DI float round_bf(float x) { return bf_lo(pk_bf16(x, x)); }
DI float dot2bf(unsigned a, unsigned b, float c) {
  return __builtin_amdgcn_fdot2_f32_bf16(__builtin_bit_cast(bf16x2, a), __builtin_bit_cast(bf16x2, b), c, false);
}
DI s16x4 tr_read(const char* p) {
  return __builtin_amdgcn_ds_read_tr16_b64_v4i16((lds_s16x4_ptr)p);
}
DI bf16x8 cat8(s16x4 lo, s16x4 hi) { return __builtin_shufflevector(lo, hi, 0, 1, 2, 3, 4, 5, 6, 7); }
DI f32x4 mfma16(bf16x8 a, bf16x8 b, f32x4 c) { return __builtin_amdgcn_mfma_f32_16x16x32_bf16(a, b, c, 0, 0, 0); }

DI void glds16(const void* g, char* lds) {
  unsigned keep;
  const unsigned dst = __builtin_amdgcn_readfirstlane((unsigned)(size_t)lds);
  asm volatile("s_mov_b32 %0, m0\n\ts_mov_b32 m0, %2\n\ts_nop 0\n\tglobal_load_lds_dwordx4 %1, off\n\ts_mov_b32 m0, %0"
               : "=&s"(keep) : "v"(g), "s"(dst) : "memory");
}
DI void glds16s(const void* sbase, unsigned voff, char* lds) {
  unsigned keep;
  const unsigned dst = __builtin_amdgcn_readfirstlane((unsigned)(size_t)lds);
  asm volatile("s_mov_b32 %0, m0\n\ts_mov_b32 m0, %3\n\ts_nop 0\n\tglobal_load_lds_dwordx4 %1, %2\n\ts_mov_b32 m0, %0"
               : "=&s"(keep) : "v"(voff), "s"(sbase), "s"(dst) : "memory");
}
DI float mul_s(float a, float s) { float d; asm("v_mul_f32 %0, %2, %1" : "=v"(d) : "v"(a), "s"(s)); return d; }
DI float fma_s(float a, float s, float c) { float d; asm("v_fma_f32 %0, %1, %2, %3" : "=v"(d) : "v"(a), "s"(s), "v"(c)); return d; }
DI float mul_v(float a, float b) { float d; asm("v_mul_f32 %0, %1, %2" : "=v"(d) : "v"(a), "v"(b)); return d; }
DI void touch(const void* g, char* dummy) {
  unsigned keep;
  const unsigned dst = __builtin_amdgcn_readfirstlane((unsigned)(size_t)dummy);
  asm volatile("s_mov_b32 %0, m0\n\ts_mov_b32 m0, %2\n\ts_nop 0\n\tglobal_load_lds_dword %1, off\n\ts_mov_b32 m0, %0"
               : "=&s"(keep) : "v"(g), "s"(dst) : "memory");
}

#define WT_MODE 1
DI void store16_wt(void* p, u32x4 v) {
  asm volatile("global_store_dwordx4 %0, %1, off sc1\n\ts_nop 1" ::"v"(p), "v"(v) : "memory");
}
DI void store8_wt(void* p, u32x2 v) {
  asm volatile("global_store_dwordx2 %0, %1, off sc1\n\ts_nop 1" ::"v"(p), "v"(v) : "memory");
}

#define STAMP_DECL do {} while (0)
#define STAMP(I) do {} while (0)
#define STAMP_FLUSH(K) do {} while (0)
#define STAMP_ARG
#define STAMP_PASS

template <int I, int N, class Fn> DI void static_for(Fn&& fn) {
  if constexpr (I < N) {
    fn(std::integral_constant<int, I>{});
    static_for<I + 1, N>(fn);
  }
}

constexpr int K1_DUMMY = 147456;
constexpr int K1_LDS = K1_DUMMY + 256;

constexpr int G1_A = 0;
constexpr int G1_W = 49152;
#define SB __builtin_amdgcn_sched_barrier(0)

DI void gemm1_block(char* smem, const u16* __restrict__ x, const u16* __restrict__ wg, const u16* __restrict__ wu,
                    u16* __restrict__ hbuf, float2* __restrict__ stats, int mt, int ft, int rank STAMP_ARG) {
  const int tid = threadIdx.x, lane = tid & 63;
  const int w = __builtin_amdgcn_readfirstlane(tid >> 6);
  const int wm = w >> 2, wn = w & 3;
  const int r = lane & 15, g = lane >> 4, q = (lane & 15) >> 2, p = lane & 3;
  const int row0 = mt * 128, f0 = ft * 128;

  unsigned aoffs[2], woffs[2];
#pragma unroll
  for (int e = 0; e < 2; ++e) {
    const int i = 2 * w + e;
    const int row = 8 * i + (lane >> 3);
    aoffs[e] = (unsigned)((row0 + row) * D_ + (((lane & 7) ^ (row & 7)) << 3)) * 2u;
    const int kr = 4 * i + (lane >> 4);
    const int phi = (kr & 3) | (((kr >> 3) & 1) << 2);
    const int sc = ((((lane & 15) >> 1) ^ phi) << 1) | (lane & 1);
    int col = f0 + sc * 8;
    col = col > F_ - 8 ? F_ - 8 : col;
    woffs[e] = (unsigned)(kr * F_ + col) * 2u;
  }
  auto dma_piece = [&](int kt, int n) {
    const int b = kt % 3, e = n & 1;
    if (n < 2) glds16s(x + kt * 64, aoffs[e], smem + G1_A + b * 16384 + (2 * w + e) * 1024);
    else if (n < 4) glds16s(wg + kt * 64 * F_, woffs[e], smem + G1_W + b * 32768 + (2 * w + e) * 1024);
    else glds16s(wu + kt * 64 * F_, woffs[e], smem + G1_W + b * 32768 + 16384 + (2 * w + e) * 1024);
  };
#pragma unroll
  for (int kt = 0; kt < 3; ++kt)
#pragma unroll
    for (int n = 0; n < 6; ++n) dma_piece(kt, n);

  int aoff[4][2];
#pragma unroll
  for (int i = 0; i < 4; ++i)
#pragma unroll
    for (int ks = 0; ks < 2; ++ks)
      aoff[i][ks] = G1_A + (64 * wm + 16 * (i ^ wn) + r) * 128 + (((4 * ks + g) ^ (r & 7)) << 4);
  const int phiL = q | ((g & 1) << 2);
  int boff[4];
#pragma unroll
  for (int T = 0; T < 4; ++T) {
    const int mat = (T >> 1) ^ wm;
    boff[T] = G1_W + mat * 16384 + (8 * g + q) * 256 + (((2 * wn + (T & 1)) ^ phiL) << 5) + p * 8;
  }

  f32x4 acc[4][4];
#pragma unroll
  for (int T = 0; T < 4; ++T)
#pragma unroll
    for (int i = 0; i < 4; ++i) acc[T][i] = f32x4{0.f, 0.f, 0.f, 0.f};
  f32x4 accS = {0.f, 0.f, 0.f, 0.f}, accR = accS, accC[2] = {accS, accS};
  const bf16x8 ones = {0x3f80, 0x3f80, 0x3f80, 0x3f80, 0x3f80, 0x3f80, 0x3f80, 0x3f80};

  struct Frags { s16x4 wl[4], wh[4]; bf16x8 xf[4]; };
  Frags fa, fb2;
  auto read_piece = [&](Frags& f, int kt, int ks, int n) {
    const int b = kt % 3;
    if (n < 4) f.xf[n] = *(const bf16x8*)(smem + b * 16384 + aoff[n][ks]);
    else {
      const int T = (n - 4) >> 1;
      if ((n & 1) == 0) f.wl[T] = tr_read(smem + b * 32768 + ks * 8192 + boff[T]);
      else f.wh[T] = tr_read(smem + b * 32768 + ks * 8192 + 1024 + boff[T]);
    }
  };
  auto mma_one = [&](const Frags& f, int m) {
    const int T = m / 5, i = m % 5;
    if (i < 4) acc[T][i] = mfma16(cat8(f.wl[T], f.wh[T]), f.xf[i], acc[T][i]);
    else if (T == 0) accS = mfma16(f.xf[0], f.xf[0], accS);
    else if (T == 1) accR = mfma16(ones, f.xf[0], accR);
    else if (T == 2) accC[0] = mfma16(cat8(f.wl[0], f.wh[0]), ones, accC[0]);
    else accC[1] = mfma16(cat8(f.wl[1], f.wh[1]), ones, accC[1]);
  };

  wait_vm<12>();
  raw_barrier();
  STAMP(1);
  {
    touch(x + (size_t)(row0 + 64 * (rank & 1) + (tid >> 3)) * D_ + (tid & 7) * 64, smem + K1_DUMMY);
    const int kr = 64 * (mt & 7) + (tid >> 3);
    const int sel = tid & 7, pos = sel % 3;
    int col = f0 + (pos == 0 ? 0 : pos == 1 ? 64 : 126);
    col = col > F_ - 2 ? F_ - 2 : col;
    touch((((sel / 3) & 1) ? wu : wg) + kr * F_ + col, smem + K1_DUMMY);
  }
#pragma unroll
  for (int n = 0; n < 12; ++n) read_piece(fa, 0, 0, n);
  static_for<0, 8>([&](auto KT) {
    constexpr int kt = KT.value;
    SB;
#pragma unroll
    for (int m = 0; m < 16; ++m) {
      mma_one(fa, m);
      if (m < 12) read_piece(fb2, kt, 1, m);
      SB;
    }
    if constexpr (kt < 7) {
      if constexpr (kt < 2) wait_vm<8>(); else if constexpr (kt < 6) wait_vm<6>(); else wait_vm<0>();
      raw_barrier();
      STAMP(2 + kt);
      SB;
    }
#pragma unroll
    for (int m = 16; m < 20; ++m) {
      mma_one(fa, m);
      if (m == 17 && kt < 5) dma_piece(kt + 3, 0);
      SB;
    }
#pragma unroll
    for (int m = 0; m < 20; ++m) {
      mma_one(fb2, m);
      if (m < 12 && kt < 7) read_piece(fa, kt + 1, 0, m);
      if ((m == 1 || m == 5 || m == 9 || m == 13 || m == 17) && kt < 5) dma_piece(kt + 3, 1 + (m >> 2));
      SB;
    }
  });
  __syncthreads();
  STAMP(9);
  float2* st_s = (float2*)smem;
  float* cs_s = (float*)(smem + 1024);
  if (g == (r >> 2)) {
    const int rr = r & 3;
    const float s2 = rr == 0 ? accS[0] : rr == 1 ? accS[1] : rr == 2 ? accS[2] : accS[3];
    const float mean = accR[0] * (1.f / D_);
    float var = s2 * (1.f / D_) - mean * mean;
    var = var < 0.f ? 0.f : var;
    const float mb = round_bf(mean);
    const float vb = round_bf(var);
    const float sd = round_bf(sqrtf(round_bf(vb + 1e-5f)));
    const float2 stv = make_float2(mb, 1.f / sd);
    const int row = 64 * wm + 16 * wn + r;
    st_s[row] = stv;
    if (ft == 0) stats[row0 + row] = stv;
  }
  if (r == 0) {
    *(f32x4*)(cs_s + wm * 128 + 32 * wn + 4 * g) = accC[0];
    *(f32x4*)(cs_s + wm * 128 + 32 * wn + 16 + 4 * g) = accC[1];
  }
  __syncthreads();
  const bool valid = (f0 + 32 * wn) < F_;
  f32x4 ca[2], cb[2];
#pragma unroll
  for (int T = 0; T < 2; ++T) {
    ca[T] = accC[T];
    cb[T] = *(const f32x4*)(cs_s + (wm ^ 1) * 128 + 32 * wn + 16 * T + 4 * g);
  }
#pragma unroll
  for (int i = 0; i < 4; ++i) {
    const int row = 64 * wm + 16 * (i ^ wn) + r;
    const float2 stv = st_s[row];
    const float nm = -stv.x, r2 = stv.y * stv.y;
    unsigned pk[2][2];
#pragma unroll
    for (int T = 0; T < 2; ++T) {
      const f32x4 ga = acc[T][i] + nm * ca[T];
      const f32x4 gb = acc[2 + T][i] + nm * cb[T];
      const f32x4 hv = (ga * r2) * gb;
      pk[T][0] = pk_bf16(hv[0], hv[1]);
      pk[T][1] = pk_bf16(hv[2], hv[3]);
    }
    auto r0 = __builtin_amdgcn_permlane16_swap(pk[0][0], pk[1][0], false, false);
    auto r1 = __builtin_amdgcn_permlane16_swap(pk[0][1], pk[1][1], false, false);
    u32x4 o = {r0[0], r1[0], r0[1], r1[1]};
    if (!valid) o = u32x4{0u, 0u, 0u, 0u};
    store16_wt(hbuf + (size_t)(row0 + row) * FP_ + f0 + 32 * wn + 16 * (g & 1) + 8 * (g >> 1), o);
  }
}

constexpr int PU_PITCH = 520;
DI void prep_block(char* smem, const u16* __restrict__ kin, const u16* __restrict__ vin,
                   const u16* __restrict__ shk, u16* __restrict__ ur, int stripe, int rb) {
  const int tid = threadIdx.x;
  const int rg = tid >> 3, cc = tid & 7;
  const int c0 = 64 * stripe + 8 * cc, s0 = 256 * rb + 4 * rg;
  u32x4 kx[7], vx[4];
#pragma unroll
  for (int i = 0; i < 7; ++i) {
    const int sr = s0 - 3 + i;
    kx[i] = *(const u32x4*)(kin + (size_t)(sr < 0 ? 0 : sr) * D_ + c0);
    if (i < 3 && sr < 0) kx[i] = u32x4{0u, 0u, 0u, 0u};
  }
#pragma unroll
  for (int i = 0; i < 4; ++i) vx[i] = *(const u32x4*)(vin + (size_t)(s0 + i) * D_ + c0);
  u32x4 wv[4];
#pragma unroll
  for (int i = 0; i < 4; ++i) wv[i] = *(const u32x4*)(shk + c0 * 4 + 8 * i);
#pragma unroll
  for (int d = 0; d < 4; ++d) {
#pragma unroll
    for (int hh = 0; hh < 2; ++hh) {
      const float w0 = bf_lo(wv[d][2 * hh]), w1 = bf_hi(wv[d][2 * hh]);
      const float w2 = bf_lo(wv[d][2 * hh + 1]), w3 = bf_hi(wv[d][2 * hh + 1]);
      float kf[7], u[4];
#pragma unroll
      for (int i = 0; i < 7; ++i) kf[i] = hh ? bf_hi(kx[i][d]) : bf_lo(kx[i][d]);
#pragma unroll
      for (int i = 0; i < 4; ++i) {
        float a = kf[i] * w3;
        a = fmaf(kf[i + 1], w2, a);
        a = fmaf(kf[i + 2], w1, a);
        a = fmaf(kf[i + 3], w0, a);
        u[i] = a * (hh ? bf_hi(vx[i][d]) : bf_lo(vx[i][d]));
      }
      const u32x2 o = {pk_bf16(u[3], u[2]), pk_bf16(u[1], u[0])};
      *(u32x2*)(smem + (8 * cc + 2 * d + hh) * PU_PITCH + (252 - 4 * rg) * 2) = o;
    }
  }
  __syncthreads();
#pragma unroll
  for (int i = 0; i < 8; ++i) {
    const int e = i * 512 + tid, ch = e >> 6, pos = e & 63;
    const u32x2 v = *(const u32x2*)(smem + ch * PU_PITCH + pos * 8);
    store8_wt(ur + (size_t)(64 * stripe + ch) * S_ + (S_ - 256 * (rb + 1)) + pos * 4, v);
  }
}

__global__ __launch_bounds__(512, 2) void k1_kernel(const u16* __restrict__ x, const u16* __restrict__ kin,
                                                     const u16* __restrict__ vin, const u16* __restrict__ shk,
                                                     const u16* __restrict__ ssmk,
                                                     const u16* __restrict__ wg, const u16* __restrict__ wu,
                                                     const u16* __restrict__ wd,
                                                     u16* __restrict__ hbuf, u16* __restrict__ ur,
                                                     float2* __restrict__ stats, unsigned* __restrict__ cnt) {
  __shared__ __attribute__((aligned(16))) char smem[K1_LDS];
  const int b = blockIdx.x, xcd = b & 7, idx = b >> 3;
  STAMP_DECL;
  STAMP(0);
  if (idx < 8) {
    prep_block(smem, kin, vin, shk, ur, xcd, idx);
  } else if (idx < 30) {
    const int t = 22 * (xcd & 3) + (idx - 8);
    gemm1_block(smem, x, wg, wu, hbuf, stats, 8 * (xcd >> 2) + (t & 7), t >> 3, (idx - 8) >> 3 STAMP_PASS);
  } else {
    const int part = (idx - 30) * 8 + xcd;
    if (part == 0 && threadIdx.x < 8) cnt[threadIdx.x * 64] = 0u;
    __builtin_amdgcn_s_sleep(100);
    for (int i = threadIdx.x; i < 688; i += 512) touch(wd + (size_t)(part * 688 + i) * 64, smem + K1_DUMMY);
    for (int i = threadIdx.x; i < 1024; i += 512) touch(ssmk + (size_t)(part * 1024 + i) * 64, smem + K1_DUMMY);
    wait_vm<0>();
  }
  STAMP(15);
  STAMP_FLUSH(0);
}

constexpr int K2_STAGE = 32768;
constexpr int K2_NT = FP_ / 128;
constexpr int CQ_PITCH = 4576;
constexpr int CT_PITCH = 16512;
constexpr int K2_Q = 3 * K2_STAGE;
constexpr int K2_TAB = K2_Q + 2 * CQ_PITCH;
constexpr int K2_B3 = K2_TAB;
constexpr int K2_TT = K2_B3 + K2_STAGE;
constexpr int K2_X = K2_TT + 8192;
constexpr int K2_ST = K2_Q;
constexpr int K2_DUMMY = K2_X + 8192;
constexpr int K2_LDS = K2_DUMMY + 256;
DI constexpr int k2_buf(int kt) { return (kt & 3) < 3 ? (kt & 3) * K2_STAGE : K2_B3; }

typedef __attribute__((address_space(1))) unsigned gu32;

constexpr int conv_pair(int m) {
  int c = 0;
  for (int t = 0; t < 8; ++t)
    for (int o = 0; o <= t; ++o)
      if (t + o < 8) {
        if (c++ == m) return t * 8 + o;
        if (o < t && c++ == m) return o * 8 + t;
      }
  return 0;
}

__global__ __launch_bounds__(512, 2) void k2_kernel(const u16* __restrict__ x, const u16* __restrict__ hbuf,
                                                     const u16* __restrict__ wd, const u16* __restrict__ ur,
                                                     const u16* __restrict__ ssmk, const u16* __restrict__ ssmd,
                                                     u16* tT, const float2* __restrict__ stats,
                                                     unsigned* cnt, u16* __restrict__ out) {
  __shared__ __attribute__((aligned(16))) char smem[K2_LDS];
  const int tid = threadIdx.x, lane = tid & 63;
  const int w = __builtin_amdgcn_readfirstlane(tid >> 6);
  const int kg = w >> 2, wm = (w >> 1) & 1, wn = w & 1;
  const int r = lane & 15, g = lane >> 4, q = (lane & 15) >> 2, p = lane & 3;
  const int b = blockIdx.x, xcd = b & 7, idx = b >> 3;
  const int row0 = (4 * xcd + (idx >> 3)) * 64, d0 = (idx & 7) * 64;
  STAMP_DECL;
  STAMP(0);

  const int cI = w >> 2, qd = w & 3;
  const int ch = 2 * b + cI;
  char* cq = smem + K2_Q + cI * CQ_PITCH;
  char* ct = smem + K2_TAB + cI * CT_PITCH;
  glds16(ur + (size_t)ch * S_ + (qd * 64 + lane) * 8, cq + qd * 1024);
  typedef __attribute__((ext_vector_type(3))) unsigned u32x3;
  unsigned he[2][3], dbits;
  u32x3 hm[3];
  const int mb = qd * 64 + lane - 8;
  {
    const unsigned* hrow = (const unsigned*)(ssmk + (size_t)ch * S_);
#pragma unroll
    for (int k = 0; k < 3; ++k) {
      const int m0 = mb + k;
      asm volatile("global_load_dword %0, %1, off" : "=v"(he[0][k]) : "v"(hrow + (m0 < 0 ? 0 : m0)) : "memory");
    }
#pragma unroll
    for (int it = 1; it < 4; ++it)
      asm volatile("global_load_dwordx3 %0, %1, off" : "=v"(hm[it - 1]) : "v"(hrow + it * 256 + mb) : "memory");
#pragma unroll
    for (int k = 0; k < 3; ++k) {
      const int m4 = 1024 + mb + k;
      asm volatile("global_load_dword %0, %1, off" : "=v"(he[1][k]) : "v"(hrow + (m4 > 1023 ? 1023 : m4)) : "memory");
    }
    asm volatile("global_load_ushort %0, %1, off" : "=v"(dbits) : "v"(ssmd + ch) : "memory");
  }

  unsigned aoffs[2], woffs[2], woffl[2];
#pragma unroll
  for (int e = 0; e < 2; ++e) {
    const int ar = 8 * w + 4 * e + (lane >> 4);
    aoffs[e] = (unsigned)((row0 + ar) * FP_ + (((lane & 15) ^ (ar & 15)) << 3)) * 2u;
    const int kr = 16 * w + 8 * e + (lane >> 3);
    const int phi_s = ((kr >> 1) & 1) | (((kr >> 3) & 1) << 1);
    const int wcol = d0 + ((((((lane & 7) >> 1) ^ phi_s) << 1) | (lane & 1)) << 3);
    woffs[e] = (unsigned)(kr * D_ + wcol) * 2u;
    const int krl = kr > F_ - 1 - 1280 ? F_ - 1 - 1280 : kr;
    woffl[e] = (unsigned)(krl * D_ + wcol) * 2u;
  }
  auto dma_piece = [&](int kt, int n) {
    char* sb = smem + k2_buf(kt);
    const int e = n & 1;
    if (n < 2) glds16s(hbuf + kt * 128, aoffs[e], sb + (2 * w + e) * 1024);
    else glds16s(wd + kt * 128 * D_, kt == K2_NT - 1 ? woffl[e] : woffs[e], sb + 16384 + (2 * w + e) * 1024);
  };
  {
    const int hr = tid / 22, hl = tid % 22;
    int kr = 344 * (idx >> 3) + (tid - 176);
    kr = kr > F_ - 1 ? F_ - 1 : kr;
    const u16* ph = hbuf + (size_t)(row0 + 8 * (idx & 7) + (hr & 7)) * FP_ + hl * 64;
    const u16* pw = wd + (size_t)kr * D_ + d0;
    touch(tid < 176 ? ph : pw, smem + K2_DUMMY);
  }
#pragma unroll
  for (int kt = 0; kt < 3; ++kt)
#pragma unroll
    for (int n = 0; n < 4; ++n) dma_piece(kt, n);

  if (lane < 30 && qd == 0) *(u32x4*)(cq + 4096 + lane * 16) = u32x4{0u, 0u, 0u, 0u};
  asm volatile("s_waitcnt vmcnt(13)"
               : "+v"(he[0][0]), "+v"(he[0][1]), "+v"(he[0][2]), "+v"(he[1][0]), "+v"(he[1][1]), "+v"(he[1][2]),
                 "+v"(hm[0]), "+v"(hm[1]), "+v"(hm[2]), "+v"(dbits)::"memory");
  const float dch = __uint_as_float(dbits << 16);
#pragma unroll
  for (int it = 0; it < 5; ++it) {
    const int i = it * 256 + qd * 64 + lane;
    unsigned d[3];
#pragma unroll
    for (int k = 0; k < 3; ++k) {
      if (it == 0) d[k] = mb + k < 0 ? 0u : he[0][k];
      else if (it == 4) d[k] = 1024 + mb + k > 1023 ? 0u : he[1][k];
      else d[k] = hm[it - 1][k];
    }
    u32x4 e;
    e[0] = d[0];
    e[1] = d[1];
    e[2] = __builtin_amdgcn_alignbit(d[1], d[0], 16);
    e[3] = __builtin_amdgcn_alignbit(d[2], d[1], 16);
    if (i < 1032) *(u32x4*)(ct + i * 16) = e;
  }
  raw_barrier();
  STAMP(13);
  {
    f32x4 cacc[8];
#pragma unroll
    for (int n = 0; n < 8; ++n) cacc[n] = f32x4{0.f, 0.f, 0.f, 0.f};
    const char* a_lane = ct + (64 * qd + 1 + r + 8 * g) * 8;
    const char* b_lane = cq + 4064 + 128 * qd - 32 * r + 16 * g;
    s16x4 al[2][8], ah[2][8];
    bf16x8 bq[2][8];
    auto rd = [&](int kk, int n) {
      const int t = n / 3, k = n % 3;
      if (k == 0) al[kk][t] = *(const s16x4*)(a_lane + kk * 256 + t * 2048);
      else if (k == 1) ah[kk][t] = *(const s16x4*)(a_lane + kk * 256 + t * 2048 + 32);
      else bq[kk][t] = *(const bf16x8*)(b_lane + kk * 64 - t * 512);
    };
#pragma unroll
    for (int n = 0; n < 24; ++n) rd(0, n);
    SB;
    auto side = [&](int sl) {
      if (sl < 24) rd(1, sl);
      SB;
    };
    static_for<0, 72>([&](auto SL) {
      constexpr int sl = SL.value, kk = sl / 36;
      constexpr int ai = conv_pair(sl % 36) >> 3, bi = conv_pair(sl % 36) & 7;
      cacc[ai + bi] = mfma16(cat8(al[kk][ai], ah[kk][ai]), bq[kk][bi], cacc[ai + bi]);
      side(sl);
    });
    raw_barrier();
    char* red = smem + K2_TAB + cI * 24576;
    f32x4 own[2];
#pragma unroll
    for (int n = 0; n < 8; ++n) {
      const int o = n >> 1;
      if (o == qd) own[n & 1] = cacc[n];
      else *(f32x4*)(red + (n * 3 + (qd < o ? qd : qd - 1)) * 1024 + lane * 16) = cacc[n];
    }
    raw_barrier();
#pragma unroll
    for (int i = 0; i < 2; ++i) {
      const int n = 2 * qd + i;
      f32x4 y = own[i];
#pragma unroll
      for (int k = 0; k < 3; ++k) y += *(const f32x4*)(red + (n * 3 + k) * 1024 + lane * 16);
      const int s = 256 * n + 16 * r + 4 * g;
      const u32x2 uu = *(const u32x2*)(cq + 2 * (2044 - s));
      const float t0 = round_bf(y[0]) + round_bf(bf_hi(uu[1]) * dch);
      const float t1 = round_bf(y[1]) + round_bf(bf_lo(uu[1]) * dch);
      const float t2 = round_bf(y[2]) + round_bf(bf_hi(uu[0]) * dch);
      const float t3 = round_bf(y[3]) + round_bf(bf_lo(uu[0]) * dch);
      const u32x2 o = {pk_bf16(t0, t1), pk_bf16(t2, t3)};
      asm volatile("global_store_dwordx2 %0, %1, off sc1\n\ts_nop 1" ::"v"(tT + (size_t)ch * S_ + s), "v"(o) : "memory");
    }
    raw_barrier();
  }
  STAMP(14);

  const int erow = row0 + 32 * wm + 16 * kg + r;
  const int edoff = d0 + 32 * wn + 16 * (g & 1) + 8 * (g >> 1);

  int aoff[2][2], boff[2];
#pragma unroll
  for (int i = 0; i < 2; ++i)
#pragma unroll
    for (int hs = 0; hs < 2; ++hs)
      aoff[i][hs] = (32 * wm + 16 * i + r) * 256 + (((4 * (kg + 2 * hs) + g) ^ r) << 4);
  const int phiL = ((q >> 1) & 1) | ((g & 1) << 1);
#pragma unroll
  for (int T = 0; T < 2; ++T)
    boff[T] = 16384 + (32 * kg + 8 * g + q) * 128 + (((2 * wn + T) ^ phiL) << 5) + p * 8;

  f32x4 acc[2][2];
#pragma unroll
  for (int T = 0; T < 2; ++T)
#pragma unroll
    for (int i = 0; i < 2; ++i) acc[T][i] = f32x4{0.f, 0.f, 0.f, 0.f};

  struct Frags2 { bf16x8 hf[2][2]; s16x4 wl[2][2], wh[2][2]; };
  Frags2 fs[2];
  auto read_piece = [&](Frags2& f, int kt, int n) {
    const char* sb = smem + k2_buf(kt);
    const int hs = n / 6, k = n % 6;
    if (k < 2) f.hf[hs][k] = *(const bf16x8*)(sb + aoff[k][hs]);
    else if ((k & 1) == 0) f.wl[hs][(k - 2) >> 1] = tr_read(sb + boff[(k - 2) >> 1] + hs * 8192);
    else f.wh[hs][(k - 2) >> 1] = tr_read(sb + boff[(k - 2) >> 1] + hs * 8192 + 512);
  };
  auto mma_one = [&](const Frags2& f, int m) {
    const int hs = m >> 2, T = (m >> 1) & 1, i = m & 1;
    acc[T][i] = mfma16(cat8(f.wl[hs][T], f.wh[hs][T]), f.hf[hs][i], acc[T][i]);
  };
  u32x4 tval;
  static_for<0, K2_NT + 1>([&](auto KT) {
    constexpr int kt = KT.value;
    if constexpr (kt < K2_NT) {
      if constexpr (kt == 0) wait_vm<10>();
      else if constexpr (kt == 1) wait_vm<10>();
      else if constexpr (kt <= 8) wait_vm<8>();
      else if constexpr (kt == 9) wait_vm<6>();
      else wait_vm<1>();
      raw_barrier();
      STAMP(1 + kt);
      if constexpr (kt == 2)
        if (tid == 0) __hip_atomic_fetch_add((gu32*)cnt + (b >> 5) * 64, 1u, __ATOMIC_RELAXED, __HIP_MEMORY_SCOPE_AGENT);
    }
    if constexpr (kt == 9) {
      gu32* c = (gu32*)cnt + (idx & 7) * 64;
      for (int spin = 0; spin < (1 << 16); ++spin) {
        if (__hip_atomic_load(c, __ATOMIC_RELAXED, __HIP_MEMORY_SCOPE_AGENT) >= 32u) break;
        __builtin_amdgcn_s_sleep(2);
      }
      __builtin_amdgcn_fence(__ATOMIC_ACQUIRE, "wavefront");
      asm volatile("global_load_dwordx4 %0, %1, off sc1" : "=v"(tval)
                   : "v"(tT + (size_t)(d0 + 8 * w + (lane >> 3)) * S_ + row0 + (lane & 7) * 8) : "memory");
    }
    SB;
#pragma unroll
    for (int n = 0; n < 12; ++n) {
      if (kt < K2_NT) read_piece(fs[kt & 1], kt, n);
      if (kt > 0 && n < 8) mma_one(fs[(kt - 1) & 1], n);
      if (kt + 3 < K2_NT && (n % 3) == 2) dma_piece(kt + 3, n / 3);
      if (kt == 8 && n == 5) glds16(x + (size_t)(row0 + 8 * w + (lane >> 3)) * D_ + d0 + (lane & 7) * 8, smem + K2_X + w * 1024);
      if (kt == 8 && n == 8) glds16(stats + row0 + 2 * (lane & 31), smem + K2_ST);
      SB;
    }
  });
  STAMP(12);
  asm volatile("s_waitcnt vmcnt(0)" : "+v"(tval)::"memory");
  *(u32x4*)(smem + K2_TT + tid * 16) = tval;
  raw_barrier();
  const int lrow = 32 * wm + 16 * kg + r;
  const u32x4 xv = *(const u32x4*)(smem + K2_X + lrow * 128 + (32 * wn + 16 * (g & 1) + 8 * (g >> 1)) * 2);
  const f32x2 stv = *(const f32x2*)(smem + K2_ST + lrow * 8);
  {
    f32x4* xb = (f32x4*)smem;
    const int wq = w & 3;
#pragma unroll
    for (int T = 0; T < 2; ++T) xb[((kg * 4 + wq) * 2 + T) * 64 + lane] = kg ? acc[T][0] : acc[T][1];
    raw_barrier();
    f32x4 fin[2];
#pragma unroll
    for (int T = 0; T < 2; ++T) {
      const f32x4 o = xb[(((kg ^ 1) * 4 + wq) * 2 + T) * 64 + lane];
      fin[T] = (kg ? acc[T][1] : acc[T][0]) + o;
    }
    unsigned pb[2][2], pt[2][2];
#pragma unroll
    for (int T = 0; T < 2; ++T) {
      pb[T][0] = pk_bf16(fin[T][0], fin[T][1]);
      pb[T][1] = pk_bf16(fin[T][2], fin[T][3]);
      const s16x4 tv = tr_read(smem + K2_TT + (32 * wn + 16 * T + 4 * g + q) * 128 + (32 * wm + 16 * kg + 4 * p) * 2);
      const u32x2 tu = __builtin_bit_cast(u32x2, tv);
      pt[T][0] = tu[0];
      pt[T][1] = tu[1];
    }
    auto b0 = __builtin_amdgcn_permlane16_swap(pb[0][0], pb[1][0], false, false);
    auto b1 = __builtin_amdgcn_permlane16_swap(pb[0][1], pb[1][1], false, false);
    auto t0 = __builtin_amdgcn_permlane16_swap(pt[0][0], pt[1][0], false, false);
    auto t1 = __builtin_amdgcn_permlane16_swap(pt[0][1], pt[1][1], false, false);
    const u32x4 bv = {b0[0], b1[0], b0[1], b1[1]};
    const u32x4 tv = {t0[0], t1[0], t0[1], t1[1]};
    u32x4 o;
#pragma unroll
    for (int j = 0; j < 4; ++j) {
      float res[2];
#pragma unroll
      for (int hh = 0; hh < 2; ++hh) {
        const float xe = hh ? bf_hi(xv[j]) : bf_lo(xv[j]);
        const float te = hh ? bf_hi(tv[j]) : bf_lo(tv[j]);
        const float be = hh ? bf_hi(bv[j]) : bf_lo(bv[j]);
        const float xn = round_bf(round_bf(xe - stv[0]) * stv[1]);
        const float a = round_bf(xn * te);
        res[hh] = round_bf(xe + a) + be;
      }
      o[j] = pk_bf16(res[0], res[1]);
    }
    *(u32x4*)(out + (size_t)erow * D_ + edoff) = o;
  }
  STAMP(15);
  STAMP_FLUSH(1);
}

extern "C" void kernel_launch(void* const* d_in, const int* in_sizes, int n_in, void* d_out, int out_size,
                              void* d_ws, size_t ws_size, hipStream_t stream) {
  const u16* q = (const u16*)d_in[0];
  const u16* k = (const u16*)d_in[1];
  const u16* v = (const u16*)d_in[2];
  const u16* shk = (const u16*)d_in[3];
  const u16* ssmk = (const u16*)d_in[4];
  const u16* ssmd = (const u16*)d_in[5];
  const u16* wg = (const u16*)d_in[7];
  const u16* wu = (const u16*)d_in[8];
  const u16* wd = (const u16*)d_in[9];
  char* ws = (char*)d_ws;
  u16* hbuf = (u16*)(ws + WS_H);
  u16* tT = (u16*)(ws + WS_T);
  u16* ur = (u16*)(ws + WS_U);
  float2* stats = (float2*)(ws + WS_ST);
  unsigned* cnt = (unsigned*)(ws + WS_CNT);
  k1_kernel<<<256, 512, 0, stream>>>(q, k, v, shk, ssmk, wg, wu, wd, hbuf, ur, stats, cnt);
  k2_kernel<<<256, 512, 0, stream>>>(q, hbuf, wd, ur, ssmk, ssmd, tT, stats, cnt, (u16*)d_out);
}
```

```cpp
#include <hip/hip_runtime.h>
#include <hip/hip_bf16.h>
#include <stdint.h>

typedef __attribute__((ext_vector_type(8))) short bf16x8;
typedef __attribute__((ext_vector_type(4))) short s16x4;
typedef __attribute__((ext_vector_type(2))) __bf16 bf16x2;
typedef __attribute__((ext_vector_type(4))) float f32x4;
typedef __attribute__((ext_vector_type(2))) float f32x2;
typedef __attribute__((ext_vector_type(4))) unsigned u32x4;
typedef __attribute__((ext_vector_type(2))) unsigned u32x2;
typedef __attribute__((address_space(3))) s16x4* lds_s16x4_ptr;
typedef unsigned short u16;

#define DI __device__ __forceinline__
#define HELPER_PF 1

constexpr int S_ = 2048, D_ = 512, F_ = 1376, FP_ = 1408;

constexpr size_t WS_H = 0;
constexpr size_t WS_T = 6u * 1024 * 1024;
constexpr size_t WS_U = 8u * 1024 * 1024;
constexpr size_t WS_ST = 10u * 1024 * 1024;
constexpr size_t WS_CNT = 10u * 1024 * 1024 + 65536;

template <int N> DI void wait_vm() { asm volatile("s_waitcnt vmcnt(%0)" ::"i"(N) : "memory"); }
DI void wait_lgkm0() { asm volatile("s_waitcnt lgkmcnt(0)" ::: "memory"); }
DI void raw_barrier() {
  asm volatile("s_waitcnt lgkmcnt(0)" ::: "memory");
  __builtin_amdgcn_s_barrier();
  asm volatile("" ::: "memory");
}

DI unsigned pk_bf16(float lo, float hi) {
  f32x2 f = {lo, hi};
  return __builtin_bit_cast(unsigned, __builtin_convertvector(f, bf16x2));
}
DI float bf_lo(unsigned v) { return __uint_as_float(v << 16); }
DI float bf_hi(unsigned v) { return __uint_as_float(v & 0xffff0000u); }
DI float round_bf(float x) { return bf_lo(pk_bf16(x, x)); }
DI float dot2bf(unsigned a, unsigned b, float c) {
  return __builtin_amdgcn_fdot2_f32_bf16(__builtin_bit_cast(bf16x2, a), __builtin_bit_cast(bf16x2, b), c, false);
}
DI s16x4 tr_read(const char* p) {
  return __builtin_amdgcn_ds_read_tr16_b64_v4i16((lds_s16x4_ptr)p);
}
DI bf16x8 cat8(s16x4 lo, s16x4 hi) { return __builtin_shufflevector(lo, hi, 0, 1, 2, 3, 4, 5, 6, 7); }
DI f32x4 mfma16(bf16x8 a, bf16x8 b, f32x4 c) { return __builtin_amdgcn_mfma_f32_16x16x32_bf16(a, b, c, 0, 0, 0); }

DI void glds16(const void* g, char* lds) {
  unsigned keep;
  const unsigned dst = __builtin_amdgcn_readfirstlane((unsigned)(size_t)lds);
  asm volatile("s_mov_b32 %0, m0\n\ts_mov_b32 m0, %2\n\ts_nop 0\n\tglobal_load_lds_dwordx4 %1, off\n\ts_mov_b32 m0, %0"
               : "=&s"(keep) : "v"(g), "s"(dst) : "memory");
}
DI void glds16s(const void* sbase, unsigned voff, char* lds) {
  unsigned keep;
  const unsigned dst = __builtin_amdgcn_readfirstlane((unsigned)(size_t)lds);
  asm volatile("s_mov_b32 %0, m0\n\ts_mov_b32 m0, %3\n\ts_nop 0\n\tglobal_load_lds_dwordx4 %1, %2\n\ts_mov_b32 m0, %0"
               : "=&s"(keep) : "v"(voff), "s"(sbase), "s"(dst) : "memory");
}
DI float mul_s(float a, float s) { float d; asm("v_mul_f32 %0, %2, %1" : "=v"(d) : "v"(a), "s"(s)); return d; }
DI float fma_s(float a, float s, float c) { float d; asm("v_fma_f32 %0, %1, %2, %3" : "=v"(d) : "v"(a), "s"(s), "v"(c)); return d; }
DI float mul_v(float a, float b) { float d; asm("v_mul_f32 %0, %1, %2" : "=v"(d) : "v"(a), "v"(b)); return d; }
DI void touch(const void* g, char* dummy) {
  unsigned keep;
  const unsigned dst = __builtin_amdgcn_readfirstlane((unsigned)(size_t)dummy);
  asm volatile("s_mov_b32 %0, m0\n\ts_mov_b32 m0, %2\n\ts_nop 0\n\tglobal_load_lds_dword %1, off\n\ts_mov_b32 m0, %0"
               : "=&s"(keep) : "v"(g), "s"(dst) : "memory");
}

#define WT_MODE 1
DI void store16_wt(void* p, u32x4 v) {
  asm volatile("global_store_dwordx4 %0, %1, off sc1\n\ts_nop 1" ::"v"(p), "v"(v) : "memory");
}
DI void store8_wt(void* p, u32x2 v) {
  asm volatile("global_store_dwordx2 %0, %1, off sc1\n\ts_nop 1" ::"v"(p), "v"(v) : "memory");
}

#define STAMP_DECL do {} while (0)
#define STAMP(I) do {} while (0)
#define STAMP_FLUSH(K) do {} while (0)
#define STAMP_ARG
#define STAMP_PASS

template <int I, int N, class Fn> DI void static_for(Fn&& fn) {
  if constexpr (I < N) {
    fn(std::integral_constant<int, I>{});
    static_for<I + 1, N>(fn);
  }
}

constexpr int K1_DUMMY = 147456;
constexpr int K1_LDS = K1_DUMMY + 256;

constexpr int G1_A = 0;
constexpr int G1_W = 49152;
#define SB __builtin_amdgcn_sched_barrier(0)

DI void gemm1_block(char* smem, const u16* __restrict__ x, const u16* __restrict__ wg, const u16* __restrict__ wu,
                    u16* __restrict__ hbuf, float2* __restrict__ stats, int mt, int ft, int rank STAMP_ARG) {
  const int tid = threadIdx.x, lane = tid & 63;
  const int w = __builtin_amdgcn_readfirstlane(tid >> 6);
  const int wm = w >> 2, wn = w & 3;
  const int r = lane & 15, g = lane >> 4, q = (lane & 15) >> 2, p = lane & 3;
  const int row0 = mt * 128, f0 = ft * 128;

  unsigned aoffs[2], woffs[2];
#pragma unroll
  for (int e = 0; e < 2; ++e) {
    const int i = 2 * w + e;
    const int row = 8 * i + (lane >> 3);
    aoffs[e] = (unsigned)((row0 + row) * D_ + (((lane & 7) ^ (row & 7)) << 3)) * 2u;
    const int kr = 4 * i + (lane >> 4);
    const int phi = (kr & 3) | (((kr >> 3) & 1) << 2);
    const int sc = ((((lane & 15) >> 1) ^ phi) << 1) | (lane & 1);
    int col = f0 + sc * 8;
    col = col > F_ - 8 ? F_ - 8 : col;
    woffs[e] = (unsigned)(kr * F_ + col) * 2u;
  }
  auto dma_piece = [&](int kt, int n) {
    const int b = kt % 3, e = n & 1;
    if (n < 2) glds16s(x + kt * 64, aoffs[e], smem + G1_A + b * 16384 + (2 * w + e) * 1024);
    else if (n < 4) glds16s(wg + kt * 64 * F_, woffs[e], smem + G1_W + b * 32768 + (2 * w + e) * 1024);
    else glds16s(wu + kt * 64 * F_, woffs[e], smem + G1_W + b * 32768 + 16384 + (2 * w + e) * 1024);
  };
#pragma unroll
  for (int kt = 0; kt < 3; ++kt)
#pragma unroll
    for (int n = 0; n < 6; ++n) dma_piece(kt, n);

  int aoff[4][2];
#pragma unroll
  for (int i = 0; i < 4; ++i)
#pragma unroll
    for (int ks = 0; ks < 2; ++ks)
      aoff[i][ks] = G1_A + (64 * wm + 16 * (i ^ wn) + r) * 128 + (((4 * ks + g) ^ (r & 7)) << 4);
  const int phiL = q | ((g & 1) << 2);
  int boff[4];
#pragma unroll
  for (int T = 0; T < 4; ++T) {
    const int mat = (T >> 1) ^ wm;
    boff[T] = G1_W + mat * 16384 + (8 * g + q) * 256 + (((2 * wn + (T & 1)) ^ phiL) << 5) + p * 8;
  }

  f32x4 acc[4][4];
#pragma unroll
  for (int T = 0; T < 4; ++T)
#pragma unroll
    for (int i = 0; i < 4; ++i) acc[T][i] = f32x4{0.f, 0.f, 0.f, 0.f};
  f32x4 accS = {0.f, 0.f, 0.f, 0.f}, accR = accS, accC[2] = {accS, accS};
  const bf16x8 ones = {0x3f80, 0x3f80, 0x3f80, 0x3f80, 0x3f80, 0x3f80, 0x3f80, 0x3f80};

  struct Frags { s16x4 wl[4], wh[4]; bf16x8 xf[4]; };
  Frags fa, fb2;
  auto read_piece = [&](Frags& f, int kt, int ks, int n) {
    const int b = kt % 3;
    if (n < 4) f.xf[n] = *(const bf16x8*)(smem + b * 16384 + aoff[n][ks]);
    else {
      const int T = (n - 4) >> 1;
      if ((n & 1) == 0) f.wl[T] = tr_read(smem + b * 32768 + ks * 8192 + boff[T]);
      else f.wh[T] = tr_read(smem + b * 32768 + ks * 8192 + 1024 + boff[T]);
    }
  };
  auto mma_one = [&](const Frags& f, int m) {
    const int T = m / 5, i = m % 5;
    if (i < 4) acc[T][i] = mfma16(cat8(f.wl[T], f.wh[T]), f.xf[i], acc[T][i]);
    else if (T == 0) accS = mfma16(f.xf[0], f.xf[0], accS);
    else if (T == 1) accR = mfma16(ones, f.xf[0], accR);
    else if (T == 2) accC[0] = mfma16(cat8(f.wl[0], f.wh[0]), ones, accC[0]);
    else accC[1] = mfma16(cat8(f.wl[1], f.wh[1]), ones, accC[1]);
  };

  wait_vm<12>();
  raw_barrier();
  STAMP(1);
#pragma unroll
  for (int n = 0; n < 12; ++n) read_piece(fa, 0, 0, n);
  static_for<0, 8>([&](auto KT) {
    constexpr int kt = KT.value;
    SB;
#pragma unroll
    for (int m = 0; m < 16; ++m) {
      mma_one(fa, m);
      if (m < 12) read_piece(fb2, kt, 1, m);
      SB;
    }
    if constexpr (kt < 7) {
      if constexpr (kt < 6) wait_vm<6>(); else wait_vm<0>();
      raw_barrier();
      STAMP(2 + kt);
      SB;
    }
#pragma unroll
    for (int m = 16; m < 20; ++m) {
      mma_one(fa, m);
      if (m == 17 && kt < 5) dma_piece(kt + 3, 0);
      SB;
    }
#pragma unroll
    for (int m = 0; m < 20; ++m) {
      mma_one(fb2, m);
      if (m < 12 && kt < 7) read_piece(fa, kt + 1, 0, m);
      if ((m == 1 || m == 5 || m == 9 || m == 13 || m == 17) && kt < 5) dma_piece(kt + 3, 1 + (m >> 2));
      SB;
    }
  });
  __syncthreads();
  STAMP(9);
  float2* st_s = (float2*)smem;
  float* cs_s = (float*)(smem + 1024);
  if (g == (r >> 2)) {
    const int rr = r & 3;
    const float s2 = rr == 0 ? accS[0] : rr == 1 ? accS[1] : rr == 2 ? accS[2] : accS[3];
    const float mean = accR[0] * (1.f / D_);
    float var = s2 * (1.f / D_) - mean * mean;
    var = var < 0.f ? 0.f : var;
    const float mb = round_bf(mean);
    const float vb = round_bf(var);
    const float sd = round_bf(sqrtf(round_bf(vb + 1e-5f)));
    const float2 stv = make_float2(mb, 1.f / sd);
    const int row = 64 * wm + 16 * wn + r;
    st_s[row] = stv;
    if (ft == 0) stats[row0 + row] = stv;
  }
  if (r == 0) {
    *(f32x4*)(cs_s + wm * 128 + 32 * wn + 4 * g) = accC[0];
    *(f32x4*)(cs_s + wm * 128 + 32 * wn + 16 + 4 * g) = accC[1];
  }
  __syncthreads();
  const bool valid = (f0 + 32 * wn) < F_;
  f32x4 ca[2], cb[2];
#pragma unroll
  for (int T = 0; T < 2; ++T) {
    ca[T] = accC[T];
    cb[T] = *(const f32x4*)(cs_s + (wm ^ 1) * 128 + 32 * wn + 16 * T + 4 * g);
  }
#pragma unroll
  for (int i = 0; i < 4; ++i) {
    const int row = 64 * wm + 16 * (i ^ wn) + r;
    const float2 stv = st_s[row];
    const float nm = -stv.x, r2 = stv.y * stv.y;
    unsigned pk[2][2];
#pragma unroll
    for (int T = 0; T < 2; ++T) {
      const f32x4 ga = acc[T][i] + nm * ca[T];
      const f32x4 gb = acc[2 + T][i] + nm * cb[T];
      const f32x4 hv = (ga * r2) * gb;
      pk[T][0] = pk_bf16(hv[0], hv[1]);
      pk[T][1] = pk_bf16(hv[2], hv[3]);
    }
    auto r0 = __builtin_amdgcn_permlane16_swap(pk[0][0], pk[1][0], false, false);
    auto r1 = __builtin_amdgcn_permlane16_swap(pk[0][1], pk[1][1], false, false);
    u32x4 o = {r0[0], r1[0], r0[1], r1[1]};
    if (!valid) o = u32x4{0u, 0u, 0u, 0u};
    store16_wt(hbuf + (size_t)(row0 + row) * FP_ + f0 + 32 * wn + 16 * (g & 1) + 8 * (g >> 1), o);
  }
}

constexpr int PU_PITCH = 520;
DI void prep_block(char* smem, const u16* __restrict__ kin, const u16* __restrict__ vin,
                   const u16* __restrict__ shk, u16* __restrict__ ur, int stripe, int rb) {
  const int tid = threadIdx.x;
  const int rg = tid >> 3, cc = tid & 7;
  const int c0 = 64 * stripe + 8 * cc, s0 = 256 * rb + 4 * rg;
  u32x4 kx[7], vx[4];
#pragma unroll
  for (int i = 0; i < 7; ++i) {
    const int sr = s0 - 3 + i;
    kx[i] = *(const u32x4*)(kin + (size_t)(sr < 0 ? 0 : sr) * D_ + c0);
    if (i < 3 && sr < 0) kx[i] = u32x4{0u, 0u, 0u, 0u};
  }
#pragma unroll
  for (int i = 0; i < 4; ++i) vx[i] = *(const u32x4*)(vin + (size_t)(s0 + i) * D_ + c0);
  u32x4 wv[4];
#pragma unroll
  for (int i = 0; i < 4; ++i) wv[i] = *(const u32x4*)(shk + c0 * 4 + 8 * i);
#pragma unroll
  for (int d = 0; d < 4; ++d) {
#pragma unroll
    for (int hh = 0; hh < 2; ++hh) {
      const float w0 = bf_lo(wv[d][2 * hh]), w1 = bf_hi(wv[d][2 * hh]);
      const float w2 = bf_lo(wv[d][2 * hh + 1]), w3 = bf_hi(wv[d][2 * hh + 1]);
      float kf[7], u[4];
#pragma unroll
      for (int i = 0; i < 7; ++i) kf[i] = hh ? bf_hi(kx[i][d]) : bf_lo(kx[i][d]);
#pragma unroll
      for (int i = 0; i < 4; ++i) {
        float a = kf[i] * w3;
        a = fmaf(kf[i + 1], w2, a);
        a = fmaf(kf[i + 2], w1, a);
        a = fmaf(kf[i + 3], w0, a);
        u[i] = a * (hh ? bf_hi(vx[i][d]) : bf_lo(vx[i][d]));
      }
      const u32x2 o = {pk_bf16(u[3], u[2]), pk_bf16(u[1], u[0])};
      *(u32x2*)(smem + (8 * cc + 2 * d + hh) * PU_PITCH + (252 - 4 * rg) * 2) = o;
    }
  }
  __syncthreads();
#pragma unroll
  for (int i = 0; i < 8; ++i) {
    const int e = i * 512 + tid, ch = e >> 6, pos = e & 63;
    const u32x2 v = *(const u32x2*)(smem + ch * PU_PITCH + pos * 8);
    store8_wt(ur + (size_t)(64 * stripe + ch) * S_ + (S_ - 256 * (rb + 1)) + pos * 4, v);
  }
}

__global__ __launch_bounds__(512, 2) void k1_kernel(const u16* __restrict__ x, const u16* __restrict__ kin,
                                                     const u16* __restrict__ vin, const u16* __restrict__ shk,
                                                     const u16* __restrict__ ssmk,
                                                     const u16* __restrict__ wg, const u16* __restrict__ wu,
                                                     const u16* __restrict__ wd,
                                                     u16* __restrict__ hbuf, u16* __restrict__ ur,
                                                     float2* __restrict__ stats, unsigned* __restrict__ cnt) {
  __shared__ __attribute__((aligned(16))) char smem[K1_LDS];
  const int b = blockIdx.x, xcd = b & 7, idx = b >> 3;
  STAMP_DECL;
  STAMP(0);
  if (idx < 8 || idx >= 30) {
    const int hi = idx < 8 ? idx : idx - 22, tid = threadIdx.x;
    const int q4 = xcd & 3, clo = 128 * ((22 * q4) >> 3);
    int chi = 128 * (((22 * q4 + 21) >> 3) + 1) - 2;
    chi = chi > F_ - 2 ? F_ - 2 : chi;
    const int e = hi * 256 + (tid & 255), e2 = e - 1024;
    int col = clo + 64 * (e2 % 9);
    col = col > chi ? chi : col;
    const int krl = e2 / 18 > 63 ? 63 : e2 / 18;
    const u16* pb = e < 1024 ? x + (size_t)(1024 * (xcd >> 2) + e) * D_ : (((e2 % 18) / 9) ? wu : wg) + krl * F_ + col;
    const int step = e < 1024 ? 64 : 64 * F_;
    for (int st = 3 + (tid >> 8); st < 8; st += 2) touch(pb + st * step, smem + K1_DUMMY);
  }
  if (idx < 8) {
#define PREP_DELAY 40
    if (PREP_DELAY) __builtin_amdgcn_s_sleep(PREP_DELAY);
    prep_block(smem, kin, vin, shk, ur, xcd, idx);
#define PREP_SLEEP 1
    __builtin_amdgcn_s_sleep(PREP_SLEEP);
    for (int i = threadIdx.x; i < 1376; i += 512) touch(wd + (size_t)(idx * 1376 + i) * 64, smem + K1_DUMMY);
    wait_vm<0>();
  } else if (idx < 30) {
    const int t = 22 * (xcd & 3) + (idx - 8);
    gemm1_block(smem, x, wg, wu, hbuf, stats, 8 * (xcd >> 2) + (t & 7), t >> 3, (idx - 8) >> 3 STAMP_PASS);
  } else {
    const int part = (idx - 30) * 8 + xcd;
    if (part == 0 && threadIdx.x < 8) cnt[threadIdx.x * 64] = 0u;
    __builtin_amdgcn_s_sleep(100);
    for (int i = threadIdx.x; i < 1024; i += 512) {
      const int l = (idx - 30) * 1024 + i;
      touch(ssmk + (size_t)(2 * (8 * (l >> 6) + xcd)) * S_ + (l & 63) * 64, smem + K1_DUMMY);
    }
    wait_vm<0>();
  }
  STAMP(15);
  STAMP_FLUSH(0);
}

constexpr int K2_STAGE = 32768;
constexpr int K2_NT = FP_ / 128;
constexpr int CQ_PITCH = 4576;
constexpr int CT_PITCH = 16512;
constexpr int K2_Q = 3 * K2_STAGE;
constexpr int K2_TAB = K2_Q + 2 * CQ_PITCH;
constexpr int K2_B3 = K2_TAB;
constexpr int K2_TT = K2_B3 + K2_STAGE;
constexpr int K2_X = K2_TT + 8192;
constexpr int K2_ST = K2_Q;
constexpr int K2_DUMMY = K2_X + 8192;
constexpr int K2_LDS = K2_DUMMY + 256;
DI constexpr int k2_buf(int kt) { return (kt & 3) < 3 ? (kt & 3) * K2_STAGE : K2_B3; }

typedef __attribute__((address_space(1))) unsigned gu32;

constexpr int conv_pair(int m) {
  int c = 0;
  for (int t = 0; t < 8; ++t)
    for (int o = 0; o <= t; ++o)
      if (t + o < 8) {
        if (c++ == m) return t * 8 + o;
        if (o < t && c++ == m) return o * 8 + t;
      }
  return 0;
}

__global__ __launch_bounds__(512, 2) void k2_kernel(const u16* __restrict__ x, const u16* __restrict__ hbuf,
                                                     const u16* __restrict__ wd, const u16* __restrict__ ur,
                                                     const u16* __restrict__ ssmk, const u16* __restrict__ ssmd,
                                                     u16* tT, const float2* __restrict__ stats,
                                                     unsigned* cnt, u16* __restrict__ out) {
  __shared__ __attribute__((aligned(16))) char smem[K2_LDS];
  const int tid = threadIdx.x, lane = tid & 63;
  const int w = __builtin_amdgcn_readfirstlane(tid >> 6);
  const int kg = w >> 2, wm = (w >> 1) & 1, wn = w & 1;
  const int r = lane & 15, g = lane >> 4, q = (lane & 15) >> 2, p = lane & 3;
  const int b = blockIdx.x, xcd = b & 7, idx = b >> 3;
  const int row0 = (4 * xcd + (idx >> 3)) * 64, d0 = (idx & 7) * 64;
  STAMP_DECL;
  STAMP(0);

  const int cI = w >> 2, qd = w & 3;
  const int ch = 2 * b + cI;
  char* cq = smem + K2_Q + cI * CQ_PITCH;
  char* ct = smem + K2_TAB + cI * CT_PITCH;
  glds16(ur + (size_t)ch * S_ + (qd * 64 + lane) * 8, cq + qd * 1024);
  typedef __attribute__((ext_vector_type(3))) unsigned u32x3;
  unsigned he[2][3], dbits;
  u32x3 hm[3];
  const int mb = qd * 64 + lane - 8;
  {
    const unsigned* hrow = (const unsigned*)(ssmk + (size_t)ch * S_);
#pragma unroll
    for (int k = 0; k < 3; ++k) {
      const int m0 = mb + k;
      asm volatile("global_load_dword %0, %1, off" : "=v"(he[0][k]) : "v"(hrow + (m0 < 0 ? 0 : m0)) : "memory");
    }
#pragma unroll
    for (int it = 1; it < 4; ++it)
      asm volatile("global_load_dwordx3 %0, %1, off" : "=v"(hm[it - 1]) : "v"(hrow + it * 256 + mb) : "memory");
#pragma unroll
    for (int k = 0; k < 3; ++k) {
      const int m4 = 1024 + mb + k;
      asm volatile("global_load_dword %0, %1, off" : "=v"(he[1][k]) : "v"(hrow + (m4 > 1023 ? 1023 : m4)) : "memory");
    }
    asm volatile("global_load_ushort %0, %1, off" : "=v"(dbits) : "v"(ssmd + ch) : "memory");
  }

  unsigned aoffs[2], woffs[2], woffl[2];
#pragma unroll
  for (int e = 0; e < 2; ++e) {
    const int ar = 8 * w + 4 * e + (lane >> 4);
    aoffs[e] = (unsigned)((row0 + ar) * FP_ + (((lane & 15) ^ (ar & 15)) << 3)) * 2u;
    const int kr = 16 * w + 8 * e + (lane >> 3);
    const int phi_s = ((kr >> 1) & 1) | (((kr >> 3) & 1) << 1);
    const int wcol = d0 + ((((((lane & 7) >> 1) ^ phi_s) << 1) | (lane & 1)) << 3);
    woffs[e] = (unsigned)(kr * D_ + wcol) * 2u;
    const int krl = kr > F_ - 1 - 1280 ? F_ - 1 - 1280 : kr;
    woffl[e] = (unsigned)(krl * D_ + wcol) * 2u;
  }
  auto dma_piece = [&](int kt, int n) {
    char* sb = smem + k2_buf(kt);
    const int e = n & 1;
    if (n < 2) glds16s(hbuf + kt * 128, aoffs[e], sb + (2 * w + e) * 1024);
    else glds16s(wd + kt * 128 * D_, kt == K2_NT - 1 ? woffl[e] : woffs[e], sb + 16384 + (2 * w + e) * 1024);
  };
  const u16* ptouch;
  {
    const int hr = tid / 22, hl = tid % 22;
    int kr = 344 * (idx >> 3) + (tid - 176);
    kr = kr > F_ - 1 ? F_ - 1 : kr;
    const u16* ph = hbuf + (size_t)(row0 + 8 * (idx & 7) + (hr & 7)) * FP_ + hl * 64;
    const u16* pw = wd + (size_t)kr * D_ + d0;
    ptouch = tid < 176 ? ph : pw;
  }
#define K2_EARLY 0
#pragma unroll
  for (int kt = 0; kt < K2_EARLY; ++kt)
#pragma unroll
    for (int n = 0; n < 4; ++n) dma_piece(kt, n);

  if (lane < 30 && qd == 0) *(u32x4*)(cq + 4096 + lane * 16) = u32x4{0u, 0u, 0u, 0u};
  asm volatile("s_waitcnt vmcnt(%10)"
               : "+v"(he[0][0]), "+v"(he[0][1]), "+v"(he[0][2]), "+v"(he[1][0]), "+v"(he[1][1]), "+v"(he[1][2]),
                 "+v"(hm[0]), "+v"(hm[1]), "+v"(hm[2]), "+v"(dbits) : "i"(4 * K2_EARLY) : "memory");
  const float dch = __uint_as_float(dbits << 16);
#pragma unroll
  for (int it = 0; it < 5; ++it) {
    const int i = it * 256 + qd * 64 + lane;
    unsigned d[3];
#pragma unroll
    for (int k = 0; k < 3; ++k) {
      if (it == 0) d[k] = mb + k < 0 ? 0u : he[0][k];
      else if (it == 4) d[k] = 1024 + mb + k > 1023 ? 0u : he[1][k];
      else d[k] = hm[it - 1][k];
    }
    u32x4 e;
    e[0] = d[0];
    e[1] = d[1];
    e[2] = __builtin_amdgcn_alignbit(d[1], d[0], 16);
    e[3] = __builtin_amdgcn_alignbit(d[2], d[1], 16);
    if (i < 1032) *(u32x4*)(ct + i * 16) = e;
  }
  raw_barrier();
  STAMP(13);
  {
    f32x4 cacc[8];
#pragma unroll
    for (int n = 0; n < 8; ++n) cacc[n] = f32x4{0.f, 0.f, 0.f, 0.f};
    const char* a_lane = ct + (64 * qd + 1 + r + 8 * g) * 8;
    const char* b_lane = cq + 4064 + 128 * qd - 32 * r + 16 * g;
    s16x4 al[2][8], ah[2][8];
    bf16x8 bq[2][8];
    auto rd = [&](int kk, int n) {
      const int t = n / 3, k = n % 3;
      if (k == 0) al[kk][t] = *(const s16x4*)(a_lane + kk * 256 + t * 2048);
      else if (k == 1) ah[kk][t] = *(const s16x4*)(a_lane + kk * 256 + t * 2048 + 32);
      else bq[kk][t] = *(const bf16x8*)(b_lane + kk * 64 - t * 512);
    };
#pragma unroll
    for (int n = 0; n < 24; ++n) rd(0, n);
    SB;
    auto side = [&](int sl) {
      if (sl < 24) rd(1, sl);
      if (sl == 1) touch(ptouch, smem + K2_DUMMY);
      SB;
    };
    static_for<0, 72>([&](auto SL) {
      constexpr int sl = SL.value, kk = sl / 36;
      constexpr int ai = conv_pair(sl % 36) >> 3, bi = conv_pair(sl % 36) & 7;
      cacc[ai + bi] = mfma16(cat8(al[kk][ai], ah[kk][ai]), bq[kk][bi], cacc[ai + bi]);
      side(sl);
    });
#pragma unroll
    for (int kt = K2_EARLY; kt < 3; ++kt)
#pragma unroll
      for (int n = 0; n < 4; ++n) dma_piece(kt, n);
    raw_barrier();
    char* red = smem + K2_TAB + cI * 24576;
    f32x4 own[2];
#pragma unroll
    for (int n = 0; n < 8; ++n) {
      const int o = n >> 1;
      if (o == qd) own[n & 1] = cacc[n];
      else *(f32x4*)(red + (n * 3 + (qd < o ? qd : qd - 1)) * 1024 + lane * 16) = cacc[n];
    }
    raw_barrier();
#pragma unroll
    for (int i = 0; i < 2; ++i) {
      const int n = 2 * qd + i;
      f32x4 y = own[i];
#pragma unroll
      for (int k = 0; k < 3; ++k) y += *(const f32x4*)(red + (n * 3 + k) * 1024 + lane * 16);
      const int s = 256 * n + 16 * r + 4 * g;
      const u32x2 uu = *(const u32x2*)(cq + 2 * (2044 - s));
      const float t0 = fmaf(bf_hi(uu[1]), dch, y[0]);
      const float t1 = fmaf(bf_lo(uu[1]), dch, y[1]);
      const float t2 = fmaf(bf_hi(uu[0]), dch, y[2]);
      const float t3 = fmaf(bf_lo(uu[0]), dch, y[3]);
      const u32x2 o = {pk_bf16(t0, t1), pk_bf16(t2, t3)};
      asm volatile("global_store_dwordx2 %0, %1, off sc1\n\ts_nop 1" ::"v"(tT + (size_t)ch * S_ + s), "v"(o) : "memory");
    }
    raw_barrier();
  }
  STAMP(14);

  const int erow = row0 + 32 * wm + 16 * kg + r;
  const int edoff = d0 + 32 * wn + 16 * (g & 1) + 8 * (g >> 1);

  int aoff[2][2], boff[2];
#pragma unroll
  for (int i = 0; i < 2; ++i)
#pragma unroll
    for (int hs = 0; hs < 2; ++hs)
      aoff[i][hs] = (32 * wm + 16 * i + r) * 256 + (((4 * (kg + 2 * hs) + g) ^ r) << 4);
  const int phiL = ((q >> 1) & 1) | ((g & 1) << 1);
#pragma unroll
  for (int T = 0; T < 2; ++T)
    boff[T] = 16384 + (32 * kg + 8 * g + q) * 128 + (((2 * wn + T) ^ phiL) << 5) + p * 8;

  f32x4 acc[2][2];
#pragma unroll
  for (int T = 0; T < 2; ++T)
#pragma unroll
    for (int i = 0; i < 2; ++i) acc[T][i] = f32x4{0.f, 0.f, 0.f, 0.f};

  struct Frags2 { bf16x8 hf[2][2]; s16x4 wl[2][2], wh[2][2]; };
  Frags2 fs[2];
  auto read_piece = [&](Frags2& f, int kt, int n) {
    const char* sb = smem + k2_buf(kt);
    const int hs = n / 6, k = n % 6;
    if (k < 2) f.hf[hs][k] = *(const bf16x8*)(sb + aoff[k][hs]);
    else if ((k & 1) == 0) f.wl[hs][(k - 2) >> 1] = tr_read(sb + boff[(k - 2) >> 1] + hs * 8192);
    else f.wh[hs][(k - 2) >> 1] = tr_read(sb + boff[(k - 2) >> 1] + hs * 8192 + 512);
  };
  auto mma_one = [&](const Frags2& f, int m) {
    const int hs = m >> 2, T = (m >> 1) & 1, i = m & 1;
    acc[T][i] = mfma16(cat8(f.wl[hs][T], f.wh[hs][T]), f.hf[hs][i], acc[T][i]);
  };
  u32x4 tval;
  static_for<0, K2_NT + 1>([&](auto KT) {
    constexpr int kt = KT.value;
    if constexpr (kt < K2_NT) {
      if constexpr (kt == 0) wait_vm<10>();
      else if constexpr (kt == 1) wait_vm<10>();
      else if constexpr (kt <= 8) wait_vm<8>();
      else if constexpr (kt == 9) wait_vm<6>();
      else wait_vm<1>();
      raw_barrier();
      STAMP(1 + kt);
      if constexpr (kt == 2)
        if (tid == 0) __hip_atomic_fetch_add((gu32*)cnt + (b >> 5) * 64, 1u, __ATOMIC_RELAXED, __HIP_MEMORY_SCOPE_AGENT);
    }
    if constexpr (kt == 9) {
      gu32* c = (gu32*)cnt + (idx & 7) * 64;
      for (int spin = 0; spin < (1 << 16); ++spin) {
        if (__hip_atomic_load(c, __ATOMIC_RELAXED, __HIP_MEMORY_SCOPE_AGENT) >= 32u) break;
        __builtin_amdgcn_s_sleep(2);
      }
      __builtin_amdgcn_fence(__ATOMIC_ACQUIRE, "wavefront");
      asm volatile("global_load_dwordx4 %0, %1, off sc1" : "=v"(tval)
                   : "v"(tT + (size_t)(d0 + 8 * w + (lane >> 3)) * S_ + row0 + (lane & 7) * 8) : "memory");
    }
    SB;
#pragma unroll
    for (int n = 0; n < 12; ++n) {
      if (kt < K2_NT) read_piece(fs[kt & 1], kt, n);
      if (kt > 0 && n < 8) mma_one(fs[(kt - 1) & 1], n);
      if (kt + 3 < K2_NT && (n % 3) == 2) dma_piece(kt + 3, n / 3);
      if (kt == 8 && n == 5) glds16(x + (size_t)(row0 + 8 * w + (lane >> 3)) * D_ + d0 + (lane & 7) * 8, smem + K2_X + w * 1024);
      if (kt == 8 && n == 8) glds16(stats + row0 + 2 * (lane & 31), smem + K2_ST);
      SB;
    }
  });
  STAMP(12);
  asm volatile("s_waitcnt vmcnt(0)" : "+v"(tval)::"memory");
  *(u32x4*)(smem + K2_TT + tid * 16) = tval;
  raw_barrier();
  const int lrow = 32 * wm + 16 * kg + r;
  const u32x4 xv = *(const u32x4*)(smem + K2_X + lrow * 128 + (32 * wn + 16 * (g & 1) + 8 * (g >> 1)) * 2);
  const f32x2 stv = *(const f32x2*)(smem + K2_ST + lrow * 8);
  {
    f32x4* xb = (f32x4*)smem;
    const int wq = w & 3;
#pragma unroll
    for (int T = 0; T < 2; ++T) xb[((kg * 4 + wq) * 2 + T) * 64 + lane] = kg ? acc[T][0] : acc[T][1];
    raw_barrier();
    f32x4 fin[2];
#pragma unroll
    for (int T = 0; T < 2; ++T) {
      const f32x4 o = xb[(((kg ^ 1) * 4 + wq) * 2 + T) * 64 + lane];
      fin[T] = (kg ? acc[T][1] : acc[T][0]) + o;
    }
    unsigned pb[2][2], pt[2][2];
#pragma unroll
    for (int T = 0; T < 2; ++T) {
      pb[T][0] = pk_bf16(fin[T][0], fin[T][1]);
      pb[T][1] = pk_bf16(fin[T][2], fin[T][3]);
      const s16x4 tv = tr_read(smem + K2_TT + (32 * wn + 16 * T + 4 * g + q) * 128 + (32 * wm + 16 * kg + 4 * p) * 2);
      const u32x2 tu = __builtin_bit_cast(u32x2, tv);
      pt[T][0] = tu[0];
      pt[T][1] = tu[1];
    }
    auto b0 = __builtin_amdgcn_permlane16_swap(pb[0][0], pb[1][0], false, false);
    auto b1 = __builtin_amdgcn_permlane16_swap(pb[0][1], pb[1][1], false, false);
    auto t0 = __builtin_amdgcn_permlane16_swap(pt[0][0], pt[1][0], false, false);
    auto t1 = __builtin_amdgcn_permlane16_swap(pt[0][1], pt[1][1], false, false);
    const u32x4 bv = {b0[0], b1[0], b0[1], b1[1]};
    const u32x4 tv = {t0[0], t1[0], t0[1], t1[1]};
    u32x4 o;
#pragma unroll
    for (int j = 0; j < 4; ++j) {
      float res[2];
#pragma unroll
      for (int hh = 0; hh < 2; ++hh) {
        const float xe = hh ? bf_hi(xv[j]) : bf_lo(xv[j]);
        const float te = hh ? bf_hi(tv[j]) : bf_lo(tv[j]);
        const float be = hh ? bf_hi(bv[j]) : bf_lo(bv[j]);
        const float xn = round_bf(round_bf(xe - stv[0]) * stv[1]);
        const float a = round_bf(xn * te);
        res[hh] = round_bf(xe + a) + be;
      }
      o[j] = pk_bf16(res[0], res[1]);
    }
    *(u32x4*)(out + (size_t)erow * D_ + edoff) = o;
  }
  STAMP(15);
  STAMP_FLUSH(1);
}

extern "C" void kernel_launch(void* const* d_in, const int* in_sizes, int n_in, void* d_out, int out_size,
                              void* d_ws, size_t ws_size, hipStream_t stream) {
  const u16* q = (const u16*)d_in[0];
  const u16* k = (const u16*)d_in[1];
  const u16* v = (const u16*)d_in[2];
  const u16* shk = (const u16*)d_in[3];
  const u16* ssmk = (const u16*)d_in[4];
  const u16* ssmd = (const u16*)d_in[5];
  const u16* wg = (const u16*)d_in[7];
  const u16* wu = (const u16*)d_in[8];
  const u16* wd = (const u16*)d_in[9];
  char* ws = (char*)d_ws;
  u16* hbuf = (u16*)(ws + WS_H);
  u16* tT = (u16*)(ws + WS_T);
  u16* ur = (u16*)(ws + WS_U);
  float2* stats = (float2*)(ws + WS_ST);
  unsigned* cnt = (unsigned*)(ws + WS_CNT);
  k1_kernel<<<256, 512, 0, stream>>>(q, k, v, shk, ssmk, wg, wu, wd, hbuf, ur, stats, cnt);
  k2_kernel<<<256, 512, 0, stream>>>(q, hbuf, wd, ur, ssmk, ssmd, tT, stats, cnt, (u16*)d_out);
}
```

```cpp
#include <hip/hip_runtime.h>
#include <hip/hip_bf16.h>
#include <stdint.h>

typedef __attribute__((ext_vector_type(8))) short bf16x8;
typedef __attribute__((ext_vector_type(4))) short s16x4;
typedef __attribute__((ext_vector_type(2))) __bf16 bf16x2;
typedef __attribute__((ext_vector_type(4))) float f32x4;
typedef __attribute__((ext_vector_type(2))) float f32x2;
typedef __attribute__((ext_vector_type(4))) unsigned u32x4;
typedef __attribute__((ext_vector_type(2))) unsigned u32x2;
typedef __attribute__((address_space(3))) s16x4* lds_s16x4_ptr;
typedef unsigned short u16;

#define DI __device__ __forceinline__
#define HELPER_PF 1

constexpr int S_ = 2048, D_ = 512, F_ = 1376, FP_ = 1408;

constexpr size_t WS_H = 0;
constexpr size_t WS_T = 6u * 1024 * 1024;
constexpr size_t WS_U = 8u * 1024 * 1024;
constexpr size_t WS_ST = 10u * 1024 * 1024;
constexpr size_t WS_CNT = 10u * 1024 * 1024 + 65536;

template <int N> DI void wait_vm() { asm volatile("s_waitcnt vmcnt(%0)" ::"i"(N) : "memory"); }
DI void wait_lgkm0() { asm volatile("s_waitcnt lgkmcnt(0)" ::: "memory"); }
DI void raw_barrier() {
  asm volatile("s_waitcnt lgkmcnt(0)" ::: "memory");
  __builtin_amdgcn_s_barrier();
  asm volatile("" ::: "memory");
}

DI unsigned pk_bf16(float lo, float hi) {
  f32x2 f = {lo, hi};
  return __builtin_bit_cast(unsigned, __builtin_convertvector(f, bf16x2));
}
DI float bf_lo(unsigned v) { return __uint_as_float(v << 16); }
DI float bf_hi(unsigned v) { return __uint_as_float(v & 0xffff0000u); }
DI float round_bf(float x) { return bf_lo(pk_bf16(x, x)); }
DI float dot2bf(unsigned a, unsigned b, float c) {
  return __builtin_amdgcn_fdot2_f32_bf16(__builtin_bit_cast(bf16x2, a), __builtin_bit_cast(bf16x2, b), c, false);
}
DI s16x4 tr_read(const char* p) {
  return __builtin_amdgcn_ds_read_tr16_b64_v4i16((lds_s16x4_ptr)p);
}
DI bf16x8 cat8(s16x4 lo, s16x4 hi) { return __builtin_shufflevector(lo, hi, 0, 1, 2, 3, 4, 5, 6, 7); }
DI f32x4 mfma16(bf16x8 a, bf16x8 b, f32x4 c) { return __builtin_amdgcn_mfma_f32_16x16x32_bf16(a, b, c, 0, 0, 0); }

DI void glds16(const void* g, char* lds) {
  unsigned keep;
  const unsigned dst = __builtin_amdgcn_readfirstlane((unsigned)(size_t)lds);
  asm volatile("s_mov_b32 %0, m0\n\ts_mov_b32 m0, %2\n\ts_nop 0\n\tglobal_load_lds_dwordx4 %1, off\n\ts_mov_b32 m0, %0"
               : "=&s"(keep) : "v"(g), "s"(dst) : "memory");
}
DI void glds16s(const void* sbase, unsigned voff, char* lds) {
  unsigned keep;
  const unsigned dst = __builtin_amdgcn_readfirstlane((unsigned)(size_t)lds);
  asm volatile("s_mov_b32 %0, m0\n\ts_mov_b32 m0, %3\n\ts_nop 0\n\tglobal_load_lds_dwordx4 %1, %2\n\ts_mov_b32 m0, %0"
               : "=&s"(keep) : "v"(voff), "s"(sbase), "s"(dst) : "memory");
}
DI float mul_s(float a, float s) { float d; asm("v_mul_f32 %0, %2, %1" : "=v"(d) : "v"(a), "s"(s)); return d; }
DI float fma_s(float a, float s, float c) { float d; asm("v_fma_f32 %0, %1, %2, %3" : "=v"(d) : "v"(a), "s"(s), "v"(c)); return d; }
DI float mul_v(float a, float b) { float d; asm("v_mul_f32 %0, %1, %2" : "=v"(d) : "v"(a), "v"(b)); return d; }
DI void touch(const void* g, char* dummy) {
  unsigned keep;
  const unsigned dst = __builtin_amdgcn_readfirstlane((unsigned)(size_t)dummy);
  asm volatile("s_mov_b32 %0, m0\n\ts_mov_b32 m0, %2\n\ts_nop 0\n\tglobal_load_lds_dword %1, off\n\ts_mov_b32 m0, %0"
               : "=&s"(keep) : "v"(g), "s"(dst) : "memory");
}

#define WT_MODE 1
DI void store16_wt(void* p, u32x4 v) {
  asm volatile("global_store_dwordx4 %0, %1, off sc1\n\ts_nop 1" ::"v"(p), "v"(v) : "memory");
}
DI void store8_wt(void* p, u32x2 v) {
  asm volatile("global_store_dwordx2 %0, %1, off sc1\n\ts_nop 1" ::"v"(p), "v"(v) : "memory");
}

#define STAMP_DECL do {} while (0)
#define STAMP(I) do {} while (0)
#define STAMP_FLUSH(K) do {} while (0)
#define STAMP_ARG
#define STAMP_PASS

template <int I, int N, class Fn> DI void static_for(Fn&& fn) {
  if constexpr (I < N) {
    fn(std::integral_constant<int, I>{});
    static_for<I + 1, N>(fn);
  }
}

constexpr int K1_DUMMY = 147456;
constexpr int K1_LDS = K1_DUMMY + 256;

constexpr int G1_A = 0;
constexpr int G1_W = 49152;
#define SB __builtin_amdgcn_sched_barrier(0)

DI void gemm1_block(char* smem, const u16* __restrict__ x, const u16* __restrict__ wg, const u16* __restrict__ wu,
                    u16* __restrict__ hbuf, float2* __restrict__ stats, int mt, int ft, int rank STAMP_ARG) {
  const int tid = threadIdx.x, lane = tid & 63;
  const int w = __builtin_amdgcn_readfirstlane(tid >> 6);
  const int wm = w >> 2, wn = w & 3;
  const int r = lane & 15, g = lane >> 4, q = (lane & 15) >> 2, p = lane & 3;
  const int row0 = mt * 128, f0 = ft * 128;

  unsigned aoffs[2], woffs[2];
#pragma unroll
  for (int e = 0; e < 2; ++e) {
    const int i = 2 * w + e;
    const int row = 8 * i + (lane >> 3);
    aoffs[e] = (unsigned)((row0 + row) * D_ + (((lane & 7) ^ (row & 7)) << 3)) * 2u;
    const int kr = 4 * i + (lane >> 4);
    const int phi = (kr & 3) | (((kr >> 3) & 1) << 2);
    const int sc = ((((lane & 15) >> 1) ^ phi) << 1) | (lane & 1);
    int col = f0 + sc * 8;
    col = col > F_ - 8 ? F_ - 8 : col;
    woffs[e] = (unsigned)(kr * F_ + col) * 2u;
  }
  auto dma_piece = [&](int kt, int n) {
    const int b = kt % 3, e = n & 1;
    if (n < 2) glds16s(x + kt * 64, aoffs[e], smem + G1_A + b * 16384 + (2 * w + e) * 1024);
    else if (n < 4) glds16s(wg + kt * 64 * F_, woffs[e], smem + G1_W + b * 32768 + (2 * w + e) * 1024);
    else glds16s(wu + kt * 64 * F_, woffs[e], smem + G1_W + b * 32768 + 16384 + (2 * w + e) * 1024);
  };
#pragma unroll
  for (int kt = 0; kt < 3; ++kt)
#pragma unroll
    for (int n = 0; n < 6; ++n) dma_piece(kt, n);

  int aoff[4][2];
#pragma unroll
  for (int i = 0; i < 4; ++i)
#pragma unroll
    for (int ks = 0; ks < 2; ++ks)
      aoff[i][ks] = G1_A + (64 * wm + 16 * (i ^ wn) + r) * 128 + (((4 * ks + g) ^ (r & 7)) << 4);
  const int phiL = q | ((g & 1) << 2);
  int boff[4];
#pragma unroll
  for (int T = 0; T < 4; ++T) {
    const int mat = (T >> 1) ^ wm;
    boff[T] = G1_W + mat * 16384 + (8 * g + q) * 256 + (((2 * wn + (T & 1)) ^ phiL) << 5) + p * 8;
  }

  f32x4 acc[4][4];
#pragma unroll
  for (int T = 0; T < 4; ++T)
#pragma unroll
    for (int i = 0; i < 4; ++i) acc[T][i] = f32x4{0.f, 0.f, 0.f, 0.f};
  f32x4 accS = {0.f, 0.f, 0.f, 0.f}, accR = accS, accC[2] = {accS, accS};
  const bf16x8 ones = {0x3f80, 0x3f80, 0x3f80, 0x3f80, 0x3f80, 0x3f80, 0x3f80, 0x3f80};

  struct Frags { s16x4 wl[4], wh[4]; bf16x8 xf[4]; };
  Frags fa, fb2;
  auto read_piece = [&](Frags& f, int kt, int ks, int n) {
    const int b = kt % 3;
    if (n < 4) f.xf[n] = *(const bf16x8*)(smem + b * 16384 + aoff[n][ks]);
    else {
      const int T = (n - 4) >> 1;
      if ((n & 1) == 0) f.wl[T] = tr_read(smem + b * 32768 + ks * 8192 + boff[T]);
      else f.wh[T] = tr_read(smem + b * 32768 + ks * 8192 + 1024 + boff[T]);
    }
  };
  auto mma_one = [&](const Frags& f, int m) {
    const int T = m / 5, i = m % 5;
    if (i < 4) acc[T][i] = mfma16(cat8(f.wl[T], f.wh[T]), f.xf[i], acc[T][i]);
    else if (T == 0) accS = mfma16(f.xf[0], f.xf[0], accS);
    else if (T == 1) accR = mfma16(ones, f.xf[0], accR);
    else if (T == 2) accC[0] = mfma16(cat8(f.wl[0], f.wh[0]), ones, accC[0]);
    else accC[1] = mfma16(cat8(f.wl[1], f.wh[1]), ones, accC[1]);
  };

  wait_vm<12>();
  raw_barrier();
  STAMP(1);
#pragma unroll
  for (int n = 0; n < 12; ++n) read_piece(fa, 0, 0, n);
  static_for<0, 8>([&](auto KT) {
    constexpr int kt = KT.value;
    SB;
#pragma unroll
    for (int m = 0; m < 16; ++m) {
      mma_one(fa, m);
      if (m < 12) read_piece(fb2, kt, 1, m);
      SB;
    }
    if constexpr (kt < 7) {
      if constexpr (kt < 6) wait_vm<6>(); else wait_vm<0>();
      raw_barrier();
      STAMP(2 + kt);
      SB;
    }
#pragma unroll
    for (int m = 16; m < 20; ++m) {
      mma_one(fa, m);
      if (m == 17 && kt < 5) dma_piece(kt + 3, 0);
      SB;
    }
#pragma unroll
    for (int m = 0; m < 20; ++m) {
      mma_one(fb2, m);
      if (m < 12 && kt < 7) read_piece(fa, kt + 1, 0, m);
      if ((m == 1 || m == 5 || m == 9 || m == 13 || m == 17) && kt < 5) dma_piece(kt + 3, 1 + (m >> 2));
      SB;
    }
  });
  __syncthreads();
  STAMP(9);
  float2* st_s = (float2*)smem;
  float* cs_s = (float*)(smem + 1024);
  if (g == (r >> 2)) {
    const int rr = r & 3;
    const float s2 = rr == 0 ? accS[0] : rr == 1 ? accS[1] : rr == 2 ? accS[2] : accS[3];
    const float mean = accR[0] * (1.f / D_);
    float var = s2 * (1.f / D_) - mean * mean;
    var = var < 0.f ? 0.f : var;
    const float mb = round_bf(mean);
    const float vb = round_bf(var);
    const float sd = round_bf(sqrtf(round_bf(vb + 1e-5f)));
    const float2 stv = make_float2(mb, 1.f / sd);
    const int row = 64 * wm + 16 * wn + r;
    st_s[row] = stv;
    if (ft == 0) stats[row0 + row] = stv;
  }
  if (r == 0) {
    *(f32x4*)(cs_s + wm * 128 + 32 * wn + 4 * g) = accC[0];
    *(f32x4*)(cs_s + wm * 128 + 32 * wn + 16 + 4 * g) = accC[1];
  }
  __syncthreads();
  const bool valid = (f0 + 32 * wn) < F_;
  f32x4 ca[2], cb[2];
#pragma unroll
  for (int T = 0; T < 2; ++T) {
    ca[T] = accC[T];
    cb[T] = *(const f32x4*)(cs_s + (wm ^ 1) * 128 + 32 * wn + 16 * T + 4 * g);
  }
#pragma unroll
  for (int i = 0; i < 4; ++i) {
    const int row = 64 * wm + 16 * (i ^ wn) + r;
    const float2 stv = st_s[row];
    const float nm = -stv.x, r2 = stv.y * stv.y;
    unsigned pk[2][2];
#pragma unroll
    for (int T = 0; T < 2; ++T) {
      const f32x4 ga = acc[T][i] + nm * ca[T];
      const f32x4 gb = acc[2 + T][i] + nm * cb[T];
      const f32x4 hv = (ga * r2) * gb;
      pk[T][0] = pk_bf16(hv[0], hv[1]);
      pk[T][1] = pk_bf16(hv[2], hv[3]);
    }
    auto r0 = __builtin_amdgcn_permlane16_swap(pk[0][0], pk[1][0], false, false);
    auto r1 = __builtin_amdgcn_permlane16_swap(pk[0][1], pk[1][1], false, false);
    u32x4 o = {r0[0], r1[0], r0[1], r1[1]};
    if (!valid) o = u32x4{0u, 0u, 0u, 0u};
    store16_wt(hbuf + (size_t)(row0 + row) * FP_ + f0 + 32 * wn + 16 * (g & 1) + 8 * (g >> 1), o);
  }
}

constexpr int PU_PITCH = 520;
DI void prep_block(char* smem, const u16* __restrict__ kin, const u16* __restrict__ vin,
                   const u16* __restrict__ shk, u16* __restrict__ ur, int stripe, int rb) {
  const int tid = threadIdx.x;
  const int rg = tid >> 3, cc = tid & 7;
  const int c0 = 64 * stripe + 8 * cc, s0 = 256 * rb + 4 * rg;
  u32x4 kx[7], vx[4];
#pragma unroll
  for (int i = 0; i < 7; ++i) {
    const int sr = s0 - 3 + i;
    kx[i] = *(const u32x4*)(kin + (size_t)(sr < 0 ? 0 : sr) * D_ + c0);
    if (i < 3 && sr < 0) kx[i] = u32x4{0u, 0u, 0u, 0u};
  }
#pragma unroll
  for (int i = 0; i < 4; ++i) vx[i] = *(const u32x4*)(vin + (size_t)(s0 + i) * D_ + c0);
  u32x4 wv[4];
#pragma unroll
  for (int i = 0; i < 4; ++i) wv[i] = *(const u32x4*)(shk + c0 * 4 + 8 * i);
#pragma unroll
  for (int d = 0; d < 4; ++d) {
#pragma unroll
    for (int hh = 0; hh < 2; ++hh) {
      const float w0 = bf_lo(wv[d][2 * hh]), w1 = bf_hi(wv[d][2 * hh]);
      const float w2 = bf_lo(wv[d][2 * hh + 1]), w3 = bf_hi(wv[d][2 * hh + 1]);
      float kf[7], u[4];
#pragma unroll
      for (int i = 0; i < 7; ++i) kf[i] = hh ? bf_hi(kx[i][d]) : bf_lo(kx[i][d]);
#pragma unroll
      for (int i = 0; i < 4; ++i) {
        float a = kf[i] * w3;
        a = fmaf(kf[i + 1], w2, a);
        a = fmaf(kf[i + 2], w1, a);
        a = fmaf(kf[i + 3], w0, a);
        u[i] = a * (hh ? bf_hi(vx[i][d]) : bf_lo(vx[i][d]));
      }
      const u32x2 o = {pk_bf16(u[3], u[2]), pk_bf16(u[1], u[0])};
      *(u32x2*)(smem + (8 * cc + 2 * d + hh) * PU_PITCH + (252 - 4 * rg) * 2) = o;
    }
  }
  __syncthreads();
#pragma unroll
  for (int i = 0; i < 8; ++i) {
    const int e = i * 512 + tid, ch = e >> 6, pos = e & 63;
    const u32x2 v = *(const u32x2*)(smem + ch * PU_PITCH + pos * 8);
    store8_wt(ur + (size_t)(64 * stripe + ch) * S_ + (S_ - 256 * (rb + 1)) + pos * 4, v);
  }
}

__global__ __launch_bounds__(512, 2) void k1_kernel(const u16* __restrict__ x, const u16* __restrict__ kin,
                                                     const u16* __restrict__ vin, const u16* __restrict__ shk,
                                                     const u16* __restrict__ ssmk,
                                                     const u16* __restrict__ wg, const u16* __restrict__ wu,
                                                     const u16* __restrict__ wd,
                                                     u16* __restrict__ hbuf, u16* __restrict__ ur,
                                                     float2* __restrict__ stats, unsigned* __restrict__ cnt) {
  __shared__ __attribute__((aligned(16))) char smem[K1_LDS];
  const int b = blockIdx.x, xcd = b & 7, idx = b >> 3;
  STAMP_DECL;
  STAMP(0);
  if (idx < 8 || idx >= 30) {
    const int hi = idx < 8 ? idx : idx - 22, tid = threadIdx.x;
    const int q4 = xcd & 3, clo = 128 * ((22 * q4) >> 3);
    int chi = 128 * (((22 * q4 + 21) >> 3) + 1) - 2;
    chi = chi > F_ - 2 ? F_ - 2 : chi;
    const int e = hi * 256 + (tid & 255), e2 = e - 1024;
    int col = clo + 64 * (e2 % 9);
    col = col > chi ? chi : col;
    const int krl = e2 / 18 > 63 ? 63 : e2 / 18;
    const u16* pb = e < 1024 ? x + (size_t)(1024 * (xcd >> 2) + e) * D_ : (((e2 % 18) / 9) ? wu : wg) + krl * F_ + col;
    const int step = e < 1024 ? 64 : 64 * F_;
    for (int st = 3 + (tid >> 8); st < 8; st += 2) touch(pb + st * step, smem + K1_DUMMY);
  }
  if (idx < 8) {
#define PREP_DELAY 40
    if (PREP_DELAY) __builtin_amdgcn_s_sleep(PREP_DELAY);
    prep_block(smem, kin, vin, shk, ur, xcd, idx);
#define PREP_SLEEP 1
    __builtin_amdgcn_s_sleep(PREP_SLEEP);
    for (int i = threadIdx.x; i < 1376; i += 512) touch(wd + (size_t)(idx * 1376 + i) * 64, smem + K1_DUMMY);
    wait_vm<0>();
  } else if (idx < 30) {
    const int t = 22 * (xcd & 3) + (idx - 8);
    gemm1_block(smem, x, wg, wu, hbuf, stats, 8 * (xcd >> 2) + (t & 7), t >> 3, (idx - 8) >> 3 STAMP_PASS);
  } else {
    const int part = (idx - 30) * 8 + xcd;
    if (part == 0 && threadIdx.x < 8) cnt[threadIdx.x * 64] = 0u;
    __builtin_amdgcn_s_sleep(100);
    for (int i = threadIdx.x; i < 1024; i += 512) {
      const int l = (idx - 30) * 1024 + i;
      touch(ssmk + (size_t)(2 * (8 * (l >> 6) + xcd)) * S_ + (l & 63) * 64, smem + K1_DUMMY);
    }
    wait_vm<0>();
  }
  STAMP(15);
  STAMP_FLUSH(0);
}

constexpr int K2_STAGE = 32768;
constexpr int K2_NT = FP_ / 128;
constexpr int CQ_PITCH = 4576;
constexpr int CT_PITCH = 16512;
constexpr int K2_Q = 3 * K2_STAGE;
constexpr int K2_TAB = K2_Q + 2 * CQ_PITCH;
constexpr int K2_B3 = K2_TAB;
constexpr int K2_TT = K2_B3 + K2_STAGE;
constexpr int K2_X = K2_TT + 8192;
constexpr int K2_ST = K2_Q;
constexpr int K2_DUMMY = K2_X + 8192;
constexpr int K2_LDS = K2_DUMMY + 256;
DI constexpr int k2_buf(int kt) { return (kt & 3) < 3 ? (kt & 3) * K2_STAGE : K2_B3; }

typedef __attribute__((address_space(1))) unsigned gu32;

constexpr int conv_pair(int m) {
  int c = 0;
  for (int t = 0; t < 8; ++t)
    for (int o = 0; o <= t; ++o)
      if (t + o < 8) {
        if (c++ == m) return t * 8 + o;
        if (o < t && c++ == m) return o * 8 + t;
      }
  return 0;
}

__global__ __launch_bounds__(512, 2) void k2_kernel(const u16* __restrict__ x, const u16* __restrict__ hbuf,
                                                     const u16* __restrict__ wd, const u16* __restrict__ ur,
                                                     const u16* __restrict__ ssmk, const u16* __restrict__ ssmd,
                                                     u16* tT, const float2* __restrict__ stats,
                                                     unsigned* cnt, u16* __restrict__ out) {
  __shared__ __attribute__((aligned(16))) char smem[K2_LDS];
  const int tid = threadIdx.x, lane = tid & 63;
  const int w = __builtin_amdgcn_readfirstlane(tid >> 6);
  const int kg = w >> 2, wm = (w >> 1) & 1, wn = w & 1;
  const int r = lane & 15, g = lane >> 4, q = (lane & 15) >> 2, p = lane & 3;
  const int b = blockIdx.x, xcd = b & 7, idx = b >> 3;
  const int row0 = (4 * xcd + (idx >> 3)) * 64, d0 = (idx & 7) * 64;
  STAMP_DECL;
  STAMP(0);

  const int cI = w >> 2, qd = w & 3;
  const int ch = 2 * b + cI;
  char* cq = smem + K2_Q + cI * CQ_PITCH;
  char* ct = smem + K2_TAB + cI * CT_PITCH;
  glds16(ur + (size_t)ch * S_ + (qd * 64 + lane) * 8, cq + qd * 1024);
  typedef __attribute__((ext_vector_type(3))) unsigned u32x3;
  unsigned he[2][3], dbits;
  u32x3 hm[3];
  const int mb = qd * 64 + lane - 8;
  {
    const unsigned* hrow = (const unsigned*)(ssmk + (size_t)ch * S_);
#pragma unroll
    for (int k = 0; k < 3; ++k) {
      const int m0 = mb + k;
      asm volatile("global_load_dword %0, %1, off" : "=v"(he[0][k]) : "v"(hrow + (m0 < 0 ? 0 : m0)) : "memory");
    }
#pragma unroll
    for (int it = 1; it < 4; ++it)
      asm volatile("global_load_dwordx3 %0, %1, off" : "=v"(hm[it - 1]) : "v"(hrow + it * 256 + mb) : "memory");
#pragma unroll
    for (int k = 0; k < 3; ++k) {
      const int m4 = 1024 + mb + k;
      asm volatile("global_load_dword %0, %1, off" : "=v"(he[1][k]) : "v"(hrow + (m4 > 1023 ? 1023 : m4)) : "memory");
    }
    asm volatile("global_load_ushort %0, %1, off" : "=v"(dbits) : "v"(ssmd + ch) : "memory");
  }

  unsigned aoffs[2], woffs[2], woffl[2];
#pragma unroll
  for (int e = 0; e < 2; ++e) {
    const int ar = 8 * w + 4 * e + (lane >> 4);
    aoffs[e] = (unsigned)((row0 + ar) * FP_ + (((lane & 15) ^ (ar & 15)) << 3)) * 2u;
    const int kr = 16 * w + 8 * e + (lane >> 3);
    const int phi_s = ((kr >> 1) & 1) | (((kr >> 3) & 1) << 1);
    const int wcol = d0 + ((((((lane & 7) >> 1) ^ phi_s) << 1) | (lane & 1)) << 3);
    woffs[e] = (unsigned)(kr * D_ + wcol) * 2u;
    const int krl = kr > F_ - 1 - 1280 ? F_ - 1 - 1280 : kr;
    woffl[e] = (unsigned)(krl * D_ + wcol) * 2u;
  }
  auto dma_piece = [&](int kt, int n) {
    char* sb = smem + k2_buf(kt);
    const int e = n & 1;
    if (n < 2) glds16s(hbuf + kt * 128, aoffs[e], sb + (2 * w + e) * 1024);
    else glds16s(wd + kt * 128 * D_, kt == K2_NT - 1 ? woffl[e] : woffs[e], sb + 16384 + (2 * w + e) * 1024);
  };
  const u16* ptouch;
  {
    const int hr = tid / 22, hl = tid % 22;
    int kr = 344 * (idx >> 3) + (tid - 176);
    kr = kr > F_ - 1 ? F_ - 1 : kr;
    const u16* ph = hbuf + (size_t)(row0 + 8 * (idx & 7) + (hr & 7)) * FP_ + hl * 64;
    const u16* pw = wd + (size_t)kr * D_ + d0;
    ptouch = tid < 176 ? ph : pw;
  }
#define K2_EARLY 0
#pragma unroll
  for (int kt = 0; kt < K2_EARLY; ++kt)
#pragma unroll
    for (int n = 0; n < 4; ++n) dma_piece(kt, n);

  if (lane < 30 && qd == 0) *(u32x4*)(cq + 4096 + lane * 16) = u32x4{0u, 0u, 0u, 0u};
  asm volatile("s_waitcnt vmcnt(%10)"
               : "+v"(he[0][0]), "+v"(he[0][1]), "+v"(he[0][2]), "+v"(he[1][0]), "+v"(he[1][1]), "+v"(he[1][2]),
                 "+v"(hm[0]), "+v"(hm[1]), "+v"(hm[2]), "+v"(dbits) : "i"(4 * K2_EARLY) : "memory");
  const float dch = __uint_as_float(dbits << 16);
#pragma unroll
  for (int it = 0; it < 5; ++it) {
    const int i = it * 256 + qd * 64 + lane;
    unsigned d[3];
#pragma unroll
    for (int k = 0; k < 3; ++k) {
      if (it == 0) d[k] = mb + k < 0 ? 0u : he[0][k];
      else if (it == 4) d[k] = 1024 + mb + k > 1023 ? 0u : he[1][k];
      else d[k] = hm[it - 1][k];
    }
    u32x4 e;
    e[0] = d[0];
    e[1] = d[1];
    e[2] = __builtin_amdgcn_alignbit(d[1], d[0], 16);
    e[3] = __builtin_amdgcn_alignbit(d[2], d[1], 16);
    if (i < 1032) *(u32x4*)(ct + i * 16) = e;
  }
  raw_barrier();
  STAMP(13);
  {
    f32x4 cacc[8];
#pragma unroll
    for (int n = 0; n < 8; ++n) cacc[n] = f32x4{0.f, 0.f, 0.f, 0.f};
    const char* a_lane = ct + (64 * qd + 1 + r + 8 * g) * 8;
    const char* b_lane = cq + 4064 + 128 * qd - 32 * r + 16 * g;
    s16x4 al[2][8], ah[2][8];
    bf16x8 bq[2][8];
    auto rd = [&](int kk, int n) {
      const int t = n / 3, k = n % 3;
      if (k == 0) al[kk][t] = *(const s16x4*)(a_lane + kk * 256 + t * 2048);
      else if (k == 1) ah[kk][t] = *(const s16x4*)(a_lane + kk * 256 + t * 2048 + 32);
      else bq[kk][t] = *(const bf16x8*)(b_lane + kk * 64 - t * 512);
    };
#pragma unroll
    for (int n = 0; n < 24; ++n) rd(0, n);
    SB;
    auto side = [&](int sl) {
      if (sl < 24) rd(1, sl);
      if (sl == 1) touch(ptouch, smem + K2_DUMMY);
      SB;
    };
    static_for<0, 72>([&](auto SL) {
      constexpr int sl = SL.value, kk = sl / 36;
      constexpr int ai = conv_pair(sl % 36) >> 3, bi = conv_pair(sl % 36) & 7;
      cacc[ai + bi] = mfma16(cat8(al[kk][ai], ah[kk][ai]), bq[kk][bi], cacc[ai + bi]);
      side(sl);
    });
    if (kg == 0) {
#pragma unroll
      for (int kt = 0; kt < 3; ++kt)
#pragma unroll
        for (int n = 0; n < 4; ++n) {
          dma_piece(kt, n);
          char* sb = smem + k2_buf(kt);
          const int e = n & 1;
          if (n < 2) glds16s(hbuf + kt * 128 + 32 * FP_, aoffs[e], sb + (2 * w + e) * 1024 + 8192);
          else glds16s(wd + kt * 128 * D_ + 64 * D_, woffs[e], sb + 16384 + (2 * w + e) * 1024 + 8192);
        }
    }
    raw_barrier();
    char* red = smem + K2_TAB + cI * 24576;
    f32x4 own[2];
#pragma unroll
    for (int n = 0; n < 8; ++n) {
      const int o = n >> 1;
      if (o == qd) own[n & 1] = cacc[n];
      else *(f32x4*)(red + (n * 3 + (qd < o ? qd : qd - 1)) * 1024 + lane * 16) = cacc[n];
    }
    raw_barrier();
#pragma unroll
    for (int i = 0; i < 2; ++i) {
      const int n = 2 * qd + i;
      f32x4 y = own[i];
#pragma unroll
      for (int k = 0; k < 3; ++k) y += *(const f32x4*)(red + (n * 3 + k) * 1024 + lane * 16);
      const int s = 256 * n + 16 * r + 4 * g;
      const u32x2 uu = *(const u32x2*)(cq + 2 * (2044 - s));
      const float t0 = fmaf(bf_hi(uu[1]), dch, y[0]);
      const float t1 = fmaf(bf_lo(uu[1]), dch, y[1]);
      const float t2 = fmaf(bf_hi(uu[0]), dch, y[2]);
      const float t3 = fmaf(bf_lo(uu[0]), dch, y[3]);
      const u32x2 o = {pk_bf16(t0, t1), pk_bf16(t2, t3)};
      asm volatile("global_store_dwordx2 %0, %1, off sc1\n\ts_nop 1" ::"v"(tT + (size_t)ch * S_ + s), "v"(o) : "memory");
    }
    raw_barrier();
  }
  STAMP(14);

  const int erow = row0 + 32 * wm + 16 * kg + r;
  const int edoff = d0 + 32 * wn + 16 * (g & 1) + 8 * (g >> 1);

  int aoff[2][2], boff[2];
#pragma unroll
  for (int i = 0; i < 2; ++i)
#pragma unroll
    for (int hs = 0; hs < 2; ++hs)
      aoff[i][hs] = (32 * wm + 16 * i + r) * 256 + (((4 * (kg + 2 * hs) + g) ^ r) << 4);
  const int phiL = ((q >> 1) & 1) | ((g & 1) << 1);
#pragma unroll
  for (int T = 0; T < 2; ++T)
    boff[T] = 16384 + (32 * kg + 8 * g + q) * 128 + (((2 * wn + T) ^ phiL) << 5) + p * 8;

  f32x4 acc[2][2];
#pragma unroll
  for (int T = 0; T < 2; ++T)
#pragma unroll
    for (int i = 0; i < 2; ++i) acc[T][i] = f32x4{0.f, 0.f, 0.f, 0.f};

  struct Frags2 { bf16x8 hf[2][2]; s16x4 wl[2][2], wh[2][2]; };
  Frags2 fs[2];
  auto read_piece = [&](Frags2& f, int kt, int n) {
    const char* sb = smem + k2_buf(kt);
    const int hs = n / 6, k = n % 6;
    if (k < 2) f.hf[hs][k] = *(const bf16x8*)(sb + aoff[k][hs]);
    else if ((k & 1) == 0) f.wl[hs][(k - 2) >> 1] = tr_read(sb + boff[(k - 2) >> 1] + hs * 8192);
    else f.wh[hs][(k - 2) >> 1] = tr_read(sb + boff[(k - 2) >> 1] + hs * 8192 + 512);
  };
  auto mma_one = [&](const Frags2& f, int m) {
    const int hs = m >> 2, T = (m >> 1) & 1, i = m & 1;
    acc[T][i] = mfma16(cat8(f.wl[hs][T], f.wh[hs][T]), f.hf[hs][i], acc[T][i]);
  };
  u32x4 tval;
  unsigned cval;
  gu32* const cptr = (gu32*)cnt + (idx & 7) * 64;
  static_for<0, K2_NT + 1>([&](auto KT) {
    constexpr int kt = KT.value;
    if constexpr (kt < K2_NT) {
      if constexpr (kt == 0) wait_vm<18>();
      else if constexpr (kt == 1) wait_vm<14>();
      else if constexpr (kt <= 7) wait_vm<8>();
      else if constexpr (kt == 8) wait_vm<9>();
      else if constexpr (kt == 9) wait_vm<6>();
      else wait_vm<1>();
      raw_barrier();
      STAMP(1 + kt);
      if constexpr (kt == 2)
        if (tid == 0) __hip_atomic_fetch_add((gu32*)cnt + (b >> 5) * 64, 1u, __ATOMIC_RELAXED, __HIP_MEMORY_SCOPE_AGENT);
    }
    if constexpr (kt == 9) {
      asm volatile("" : "+v"(cval));
      if (__builtin_amdgcn_readfirstlane(cval) < 32u) {
        for (int spin = 0; spin < (1 << 16); ++spin) {
          if (__hip_atomic_load(cptr, __ATOMIC_RELAXED, __HIP_MEMORY_SCOPE_AGENT) >= 32u) break;
          __builtin_amdgcn_s_sleep(2);
        }
      }
      __builtin_amdgcn_fence(__ATOMIC_ACQUIRE, "wavefront");
      asm volatile("global_load_dwordx4 %0, %1, off sc1" : "=v"(tval)
                   : "v"(tT + (size_t)(d0 + 8 * w + (lane >> 3)) * S_ + row0 + (lane & 7) * 8) : "memory");
    }
    SB;
#pragma unroll
    for (int n = 0; n < 12; ++n) {
      if (kt < K2_NT) read_piece(fs[kt & 1], kt, n);
      if (kt > 0 && n < 8) mma_one(fs[(kt - 1) & 1], n);
      if (kt + 3 < K2_NT && (n % 3) == 2) dma_piece(kt + 3, n / 3);
      if (kt == 7 && n == 0) asm volatile("global_load_dword %0, %1, off sc1" : "=v"(cval) : "v"(cptr) : "memory");
      if (kt == 8 && n == 5) glds16(x + (size_t)(row0 + 8 * w + (lane >> 3)) * D_ + d0 + (lane & 7) * 8, smem + K2_X + w * 1024);
      if (kt == 8 && n == 8) glds16(stats + row0 + 2 * (lane & 31), smem + K2_ST);
      SB;
    }
  });
  STAMP(12);
  asm volatile("s_waitcnt vmcnt(0)" : "+v"(tval)::"memory");
  *(u32x4*)(smem + K2_TT + tid * 16) = tval;
  raw_barrier();
  const int lrow = 32 * wm + 16 * kg + r;
  const u32x4 xv = *(const u32x4*)(smem + K2_X + lrow * 128 + (32 * wn + 16 * (g & 1) + 8 * (g >> 1)) * 2);
  const f32x2 stv = *(const f32x2*)(smem + K2_ST + lrow * 8);
  {
    f32x4* xb = (f32x4*)smem;
    const int wq = w & 3;
#pragma unroll
    for (int T = 0; T < 2; ++T) xb[((kg * 4 + wq) * 2 + T) * 64 + lane] = kg ? acc[T][0] : acc[T][1];
    raw_barrier();
    f32x4 fin[2];
#pragma unroll
    for (int T = 0; T < 2; ++T) {
      const f32x4 o = xb[(((kg ^ 1) * 4 + wq) * 2 + T) * 64 + lane];
      fin[T] = (kg ? acc[T][1] : acc[T][0]) + o;
    }
    unsigned pb[2][2], pt[2][2];
#pragma unroll
    for (int T = 0; T < 2; ++T) {
      pb[T][0] = pk_bf16(fin[T][0], fin[T][1]);
      pb[T][1] = pk_bf16(fin[T][2], fin[T][3]);
      const s16x4 tv = tr_read(smem + K2_TT + (32 * wn + 16 * T + 4 * g + q) * 128 + (32 * wm + 16 * kg + 4 * p) * 2);
      const u32x2 tu = __builtin_bit_cast(u32x2, tv);
      pt[T][0] = tu[0];
      pt[T][1] = tu[1];
    }
    auto b0 = __builtin_amdgcn_permlane16_swap(pb[0][0], pb[1][0], false, false);
    auto b1 = __builtin_amdgcn_permlane16_swap(pb[0][1], pb[1][1], false, false);
    auto t0 = __builtin_amdgcn_permlane16_swap(pt[0][0], pt[1][0], false, false);
    auto t1 = __builtin_amdgcn_permlane16_swap(pt[0][1], pt[1][1], false, false);
    const u32x4 bv = {b0[0], b1[0], b0[1], b1[1]};
    const u32x4 tv = {t0[0], t1[0], t0[1], t1[1]};
    u32x4 o;
#pragma unroll
    for (int j = 0; j < 4; ++j) {
      float res[2];
#pragma unroll
      for (int hh = 0; hh < 2; ++hh) {
        const float xe = hh ? bf_hi(xv[j]) : bf_lo(xv[j]);
        const float te = hh ? bf_hi(tv[j]) : bf_lo(tv[j]);
        const float be = hh ? bf_hi(bv[j]) : bf_lo(bv[j]);
        const float xn = round_bf(round_bf(xe - stv[0]) * stv[1]);
        const float a = round_bf(xn * te);
        res[hh] = round_bf(xe + a) + be;
      }
      o[j] = pk_bf16(res[0], res[1]);
    }
    *(u32x4*)(out + (size_t)erow * D_ + edoff) = o;
  }
  STAMP(15);
  STAMP_FLUSH(1);
}

extern "C" void kernel_launch(void* const* d_in, const int* in_sizes, int n_in, void* d_out, int out_size,
                              void* d_ws, size_t ws_size, hipStream_t stream) {
  const u16* q = (const u16*)d_in[0];
  const u16* k = (const u16*)d_in[1];
  const u16* v = (const u16*)d_in[2];
  const u16* shk = (const u16*)d_in[3];
  const u16* ssmk = (const u16*)d_in[4];
  const u16* ssmd = (const u16*)d_in[5];
  const u16* wg = (const u16*)d_in[7];
  const u16* wu = (const u16*)d_in[8];
  const u16* wd = (const u16*)d_in[9];
  char* ws = (char*)d_ws;
  u16* hbuf = (u16*)(ws + WS_H);
  u16* tT = (u16*)(ws + WS_T);
  u16* ur = (u16*)(ws + WS_U);
  float2* stats = (float2*)(ws + WS_ST);
  unsigned* cnt = (unsigned*)(ws + WS_CNT);
  k1_kernel<<<256, 512, 0, stream>>>(q, k, v, shk, ssmk, wg, wu, wd, hbuf, ur, stats, cnt);
  k2_kernel<<<256, 512, 0, stream>>>(q, hbuf, wd, ur, ssmk, ssmd, tT, stats, cnt, (u16*)d_out);
}
```

```cpp
#include <hip/hip_runtime.h>
#include <hip/hip_bf16.h>
#include <stdint.h>

typedef __attribute__((ext_vector_type(8))) short bf16x8;
typedef __attribute__((ext_vector_type(4))) short s16x4;
typedef __attribute__((ext_vector_type(2))) __bf16 bf16x2;
typedef __attribute__((ext_vector_type(4))) float f32x4;
typedef __attribute__((ext_vector_type(2))) float f32x2;
typedef __attribute__((ext_vector_type(4))) unsigned u32x4;
typedef __attribute__((ext_vector_type(2))) unsigned u32x2;
typedef __attribute__((address_space(3))) s16x4* lds_s16x4_ptr;
typedef unsigned short u16;

#define DI __device__ __forceinline__
#define HELPER_PF 1
#define TOUCH_FIRST 1

constexpr int S_ = 2048, D_ = 512, F_ = 1376, FP_ = 1408;

constexpr size_t WS_H = 0;
constexpr size_t WS_T = 6u * 1024 * 1024;
constexpr size_t WS_U = 8u * 1024 * 1024;
constexpr size_t WS_ST = 10u * 1024 * 1024;
constexpr size_t WS_CNT = 10u * 1024 * 1024 + 65536;

template <int N> DI void wait_vm() { asm volatile("s_waitcnt vmcnt(%0)" ::"i"(N) : "memory"); }
DI void wait_lgkm0() { asm volatile("s_waitcnt lgkmcnt(0)" ::: "memory"); }
DI void raw_barrier() {
  asm volatile("s_waitcnt lgkmcnt(0)" ::: "memory");
  __builtin_amdgcn_s_barrier();
  asm volatile("" ::: "memory");
}

DI unsigned pk_bf16(float lo, float hi) {
  f32x2 f = {lo, hi};
  return __builtin_bit_cast(unsigned, __builtin_convertvector(f, bf16x2));
}
DI float bf_lo(unsigned v) { return __uint_as_float(v << 16); }
DI float bf_hi(unsigned v) { return __uint_as_float(v & 0xffff0000u); }
DI float round_bf(float x) { return bf_lo(pk_bf16(x, x)); }
DI float dot2bf(unsigned a, unsigned b, float c) {
  return __builtin_amdgcn_fdot2_f32_bf16(__builtin_bit_cast(bf16x2, a), __builtin_bit_cast(bf16x2, b), c, false);
}
DI s16x4 tr_read(const char* p) {
  return __builtin_amdgcn_ds_read_tr16_b64_v4i16((lds_s16x4_ptr)p);
}
DI bf16x8 cat8(s16x4 lo, s16x4 hi) { return __builtin_shufflevector(lo, hi, 0, 1, 2, 3, 4, 5, 6, 7); }
DI f32x4 mfma16(bf16x8 a, bf16x8 b, f32x4 c) { return __builtin_amdgcn_mfma_f32_16x16x32_bf16(a, b, c, 0, 0, 0); }

DI void glds16(const void* g, char* lds) {
  unsigned keep;
  const unsigned dst = __builtin_amdgcn_readfirstlane((unsigned)(size_t)lds);
  asm volatile("s_mov_b32 %0, m0\n\ts_mov_b32 m0, %2\n\ts_nop 0\n\tglobal_load_lds_dwordx4 %1, off\n\ts_mov_b32 m0, %0"
               : "=&s"(keep) : "v"(g), "s"(dst) : "memory");
}
DI void glds16s(const void* sbase, unsigned voff, char* lds) {
  unsigned keep;
  const unsigned dst = __builtin_amdgcn_readfirstlane((unsigned)(size_t)lds);
  asm volatile("s_mov_b32 %0, m0\n\ts_mov_b32 m0, %3\n\ts_nop 0\n\tglobal_load_lds_dwordx4 %1, %2\n\ts_mov_b32 m0, %0"
               : "=&s"(keep) : "v"(voff), "s"(sbase), "s"(dst) : "memory");
}
DI float mul_s(float a, float s) { float d; asm("v_mul_f32 %0, %2, %1" : "=v"(d) : "v"(a), "s"(s)); return d; }
DI float fma_s(float a, float s, float c) { float d; asm("v_fma_f32 %0, %1, %2, %3" : "=v"(d) : "v"(a), "s"(s), "v"(c)); return d; }
DI float mul_v(float a, float b) { float d; asm("v_mul_f32 %0, %1, %2" : "=v"(d) : "v"(a), "v"(b)); return d; }
DI void touch(const void* g, char* dummy) {
  unsigned keep;
  const unsigned dst = __builtin_amdgcn_readfirstlane((unsigned)(size_t)dummy);
  asm volatile("s_mov_b32 %0, m0\n\ts_mov_b32 m0, %2\n\ts_nop 0\n\tglobal_load_lds_dword %1, off\n\ts_mov_b32 m0, %0"
               : "=&s"(keep) : "v"(g), "s"(dst) : "memory");
}

#define WT_MODE 1
DI void store16_wt(void* p, u32x4 v) {
  asm volatile("global_store_dwordx4 %0, %1, off sc1\n\ts_nop 1" ::"v"(p), "v"(v) : "memory");
}
DI void store8_wt(void* p, u32x2 v) {
  asm volatile("global_store_dwordx2 %0, %1, off sc1\n\ts_nop 1" ::"v"(p), "v"(v) : "memory");
}

#define STAMP_DECL do {} while (0)
#define STAMP(I) do {} while (0)
#define STAMP_FLUSH(K) do {} while (0)
#define STAMP_ARG
#define STAMP_PASS

template <int I, int N, class Fn> DI void static_for(Fn&& fn) {
  if constexpr (I < N) {
    fn(std::integral_constant<int, I>{});
    static_for<I + 1, N>(fn);
  }
}

constexpr int K1_DUMMY = 147456;
constexpr int K1_LDS = K1_DUMMY + 256;

constexpr int G1_A = 0;
constexpr int G1_W = 49152;
#define SB __builtin_amdgcn_sched_barrier(0)

DI void gemm1_block(char* smem, const u16* __restrict__ x, const u16* __restrict__ wg, const u16* __restrict__ wu,
                    u16* __restrict__ hbuf, float2* __restrict__ stats, int mt, int ft, int rank STAMP_ARG) {
  const int tid = threadIdx.x, lane = tid & 63;
  const int w = __builtin_amdgcn_readfirstlane(tid >> 6);
  const int wm = w >> 2, wn = w & 3;
  const int r = lane & 15, g = lane >> 4, q = (lane & 15) >> 2, p = lane & 3;
  const int row0 = mt * 128, f0 = ft * 128;

  unsigned aoffs[2], woffs[2];
#pragma unroll
  for (int e = 0; e < 2; ++e) {
    const int i = 2 * w + e;
    const int row = 8 * i + (lane >> 3);
    aoffs[e] = (unsigned)((row0 + row) * D_ + (((lane & 7) ^ (row & 7)) << 3)) * 2u;
    const int kr = 4 * i + (lane >> 4);
    const int phi = (kr & 3) | (((kr >> 3) & 1) << 2);
    const int sc = ((((lane & 15) >> 1) ^ phi) << 1) | (lane & 1);
    int col = f0 + sc * 8;
    col = col > F_ - 8 ? F_ - 8 : col;
    woffs[e] = (unsigned)(kr * F_ + col) * 2u;
  }
  auto dma_piece = [&](int kt, int n) {
    const int b = kt % 3, e = n & 1;
    if (n < 2) glds16s(x + kt * 64, aoffs[e], smem + G1_A + b * 16384 + (2 * w + e) * 1024);
    else if (n < 4) glds16s(wg + kt * 64 * F_, woffs[e], smem + G1_W + b * 32768 + (2 * w + e) * 1024);
    else glds16s(wu + kt * 64 * F_, woffs[e], smem + G1_W + b * 32768 + 16384 + (2 * w + e) * 1024);
  };
#pragma unroll
  for (int kt = 0; kt < 3; ++kt)
#pragma unroll
    for (int n = 0; n < 6; ++n) dma_piece(kt, n);

  int aoff[4][2];
#pragma unroll
  for (int i = 0; i < 4; ++i)
#pragma unroll
    for (int ks = 0; ks < 2; ++ks)
      aoff[i][ks] = G1_A + (64 * wm + 16 * (i ^ wn) + r) * 128 + (((4 * ks + g) ^ (r & 7)) << 4);
  const int phiL = q | ((g & 1) << 2);
  int boff[4];
#pragma unroll
  for (int T = 0; T < 4; ++T) {
    const int mat = (T >> 1) ^ wm;
    boff[T] = G1_W + mat * 16384 + (8 * g + q) * 256 + (((2 * wn + (T & 1)) ^ phiL) << 5) + p * 8;
  }

  f32x4 acc[4][4];
#pragma unroll
  for (int T = 0; T < 4; ++T)
#pragma unroll
    for (int i = 0; i < 4; ++i) acc[T][i] = f32x4{0.f, 0.f, 0.f, 0.f};
  f32x4 accS = {0.f, 0.f, 0.f, 0.f}, accR = accS, accC[2] = {accS, accS};
  const bf16x8 ones = {0x3f80, 0x3f80, 0x3f80, 0x3f80, 0x3f80, 0x3f80, 0x3f80, 0x3f80};

  struct Frags { s16x4 wl[4], wh[4]; bf16x8 xf[4]; };
  Frags fa, fb2;
  auto read_piece = [&](Frags& f, int kt, int ks, int n) {
    const int b = kt % 3;
    if (n < 4) f.xf[n] = *(const bf16x8*)(smem + b * 16384 + aoff[n][ks]);
    else {
      const int T = (n - 4) >> 1;
      if ((n & 1) == 0) f.wl[T] = tr_read(smem + b * 32768 + ks * 8192 + boff[T]);
      else f.wh[T] = tr_read(smem + b * 32768 + ks * 8192 + 1024 + boff[T]);
    }
  };
  auto mma_one = [&](const Frags& f, int m) {
    const int T = m / 5, i = m % 5;
    if (i < 4) acc[T][i] = mfma16(cat8(f.wl[T], f.wh[T]), f.xf[i], acc[T][i]);
    else if (T == 0) accS = mfma16(f.xf[0], f.xf[0], accS);
    else if (T == 1) accR = mfma16(ones, f.xf[0], accR);
    else if (T == 2) accC[0] = mfma16(cat8(f.wl[0], f.wh[0]), ones, accC[0]);
    else accC[1] = mfma16(cat8(f.wl[1], f.wh[1]), ones, accC[1]);
  };

  wait_vm<12>();
  raw_barrier();
  STAMP(1);
#pragma unroll
  for (int n = 0; n < 12; ++n) read_piece(fa, 0, 0, n);
  static_for<0, 8>([&](auto KT) {
    constexpr int kt = KT.value;
    SB;
#pragma unroll
    for (int m = 0; m < 16; ++m) {
      mma_one(fa, m);
      if (m < 12) read_piece(fb2, kt, 1, m);
      SB;
    }
    if constexpr (kt < 7) {
      if constexpr (kt < 6) wait_vm<6>(); else wait_vm<0>();
      raw_barrier();
      STAMP(2 + kt);
      SB;
    }
#pragma unroll
    for (int m = 16; m < 20; ++m) {
      mma_one(fa, m);
      if (m == 17 && kt < 5) dma_piece(kt + 3, 0);
      SB;
    }
#pragma unroll
    for (int m = 0; m < 20; ++m) {
      mma_one(fb2, m);
      if (m < 12 && kt < 7) read_piece(fa, kt + 1, 0, m);
      if ((m == 1 || m == 5 || m == 9 || m == 13 || m == 17) && kt < 5) dma_piece(kt + 3, 1 + (m >> 2));
      SB;
    }
  });
  __syncthreads();
  STAMP(9);
  float2* st_s = (float2*)smem;
  float* cs_s = (float*)(smem + 1024);
  if (g == (r >> 2)) {
    const int rr = r & 3;
    const float s2 = rr == 0 ? accS[0] : rr == 1 ? accS[1] : rr == 2 ? accS[2] : accS[3];
    const float mean = accR[0] * (1.f / D_);
    float var = s2 * (1.f / D_) - mean * mean;
    var = var < 0.f ? 0.f : var;
    const float mb = round_bf(mean);
    const float vb = round_bf(var);
    const float sd = round_bf(sqrtf(round_bf(vb + 1e-5f)));
    const float2 stv = make_float2(mb, 1.f / sd);
    const int row = 64 * wm + 16 * wn + r;
    st_s[row] = stv;
    if (ft == 0) stats[row0 + row] = stv;
  }
  if (r == 0) {
    *(f32x4*)(cs_s + wm * 128 + 32 * wn + 4 * g) = accC[0];
    *(f32x4*)(cs_s + wm * 128 + 32 * wn + 16 + 4 * g) = accC[1];
  }
  __syncthreads();
  const bool valid = (f0 + 32 * wn) < F_;
  f32x4 ca[2], cb[2];
#pragma unroll
  for (int T = 0; T < 2; ++T) {
    ca[T] = accC[T];
    cb[T] = *(const f32x4*)(cs_s + (wm ^ 1) * 128 + 32 * wn + 16 * T + 4 * g);
  }
#pragma unroll
  for (int i = 0; i < 4; ++i) {
    const int row = 64 * wm + 16 * (i ^ wn) + r;
    const float2 stv = st_s[row];
    const float nm = -stv.x, r2 = stv.y * stv.y;
    unsigned pk[2][2];
#pragma unroll
    for (int T = 0; T < 2; ++T) {
      const f32x4 ga = acc[T][i] + nm * ca[T];
      const f32x4 gb = acc[2 + T][i] + nm * cb[T];
      const f32x4 hv = (ga * r2) * gb;
      pk[T][0] = pk_bf16(hv[0], hv[1]);
      pk[T][1] = pk_bf16(hv[2], hv[3]);
    }
    auto r0 = __builtin_amdgcn_permlane16_swap(pk[0][0], pk[1][0], false, false);
    auto r1 = __builtin_amdgcn_permlane16_swap(pk[0][1], pk[1][1], false, false);
    u32x4 o = {r0[0], r1[0], r0[1], r1[1]};
    if (!valid) o = u32x4{0u, 0u, 0u, 0u};
    store16_wt(hbuf + (size_t)(row0 + row) * FP_ + f0 + 32 * wn + 16 * (g & 1) + 8 * (g >> 1), o);
  }
}

constexpr int PU_PITCH = 520;
DI void prep_block(char* smem, const u16* __restrict__ kin, const u16* __restrict__ vin,
                   const u16* __restrict__ shk, u16* __restrict__ ur, int stripe, int rb) {
  const int tid = threadIdx.x;
  const int rg = tid >> 3, cc = tid & 7;
  const int c0 = 64 * stripe + 8 * cc, s0 = 256 * rb + 4 * rg;
  u32x4 kx[7], vx[4];
#pragma unroll
  for (int i = 0; i < 7; ++i) {
    const int sr = s0 - 3 + i;
    kx[i] = *(const u32x4*)(kin + (size_t)(sr < 0 ? 0 : sr) * D_ + c0);
    if (i < 3 && sr < 0) kx[i] = u32x4{0u, 0u, 0u, 0u};
  }
#pragma unroll
  for (int i = 0; i < 4; ++i) vx[i] = *(const u32x4*)(vin + (size_t)(s0 + i) * D_ + c0);
  u32x4 wv[4];
#pragma unroll
  for (int i = 0; i < 4; ++i) wv[i] = *(const u32x4*)(shk + c0 * 4 + 8 * i);
#pragma unroll
  for (int d = 0; d < 4; ++d) {
#pragma unroll
    for (int hh = 0; hh < 2; ++hh) {
      const float w0 = bf_lo(wv[d][2 * hh]), w1 = bf_hi(wv[d][2 * hh]);
      const float w2 = bf_lo(wv[d][2 * hh + 1]), w3 = bf_hi(wv[d][2 * hh + 1]);
      float kf[7], u[4];
#pragma unroll
      for (int i = 0; i < 7; ++i) kf[i] = hh ? bf_hi(kx[i][d]) : bf_lo(kx[i][d]);
#pragma unroll
      for (int i = 0; i < 4; ++i) {
        float a = kf[i] * w3;
        a = fmaf(kf[i + 1], w2, a);
        a = fmaf(kf[i + 2], w1, a);
        a = fmaf(kf[i + 3], w0, a);
        u[i] = a * (hh ? bf_hi(vx[i][d]) : bf_lo(vx[i][d]));
      }
      const u32x2 o = {pk_bf16(u[3], u[2]), pk_bf16(u[1], u[0])};
      *(u32x2*)(smem + (8 * cc + 2 * d + hh) * PU_PITCH + (252 - 4 * rg) * 2) = o;
    }
  }
  __syncthreads();
#pragma unroll
  for (int i = 0; i < 8; ++i) {
    const int e = i * 512 + tid, ch = e >> 6, pos = e & 63;
    const u32x2 v = *(const u32x2*)(smem + ch * PU_PITCH + pos * 8);
    store8_wt(ur + (size_t)(64 * stripe + ch) * S_ + (S_ - 256 * (rb + 1)) + pos * 4, v);
  }
}

__global__ __launch_bounds__(512, 2) void k1_kernel(const u16* __restrict__ x, const u16* __restrict__ kin,
                                                     const u16* __restrict__ vin, const u16* __restrict__ shk,
                                                     const u16* __restrict__ ssmk,
                                                     const u16* __restrict__ wg, const u16* __restrict__ wu,
                                                     const u16* __restrict__ wd,
                                                     u16* __restrict__ hbuf, u16* __restrict__ ur,
                                                     float2* __restrict__ stats, unsigned* __restrict__ cnt) {
  __shared__ __attribute__((aligned(16))) char smem[K1_LDS];
  const int b = blockIdx.x, xcd = b & 7, idx = b >> 3;
  STAMP_DECL;
  STAMP(0);
  if (idx < 8 || idx >= 30) {
    const int hi = idx < 8 ? idx : idx - 22, tid = threadIdx.x;
    const int q4 = xcd & 3, clo = 128 * ((22 * q4) >> 3);
    int chi = 128 * (((22 * q4 + 21) >> 3) + 1) - 2;
    chi = chi > F_ - 2 ? F_ - 2 : chi;
    const int e = hi * 256 + (tid & 255), e2 = e - 1024;
    int col = clo + 64 * (e2 % 9);
    col = col > chi ? chi : col;
    const int krl = e2 / 18 > 63 ? 63 : e2 / 18;
    const u16* pb = e < 1024 ? x + (size_t)(1024 * (xcd >> 2) + e) * D_ : (((e2 % 18) / 9) ? wu : wg) + krl * F_ + col;
    const int step = e < 1024 ? 64 : 64 * F_;
    for (int st = 3 + (tid >> 8); st < 8; st += 2) touch(pb + st * step, smem + K1_DUMMY);
  }
  if (idx < 8) {
#define PREP_DELAY 40
    if (PREP_DELAY) __builtin_amdgcn_s_sleep(PREP_DELAY);
    prep_block(smem, kin, vin, shk, ur, xcd, idx);
#define PREP_SLEEP 1
    __builtin_amdgcn_s_sleep(PREP_SLEEP);
    for (int i = threadIdx.x; i < 1376; i += 512) touch(wd + (size_t)(idx * 1376 + i) * 64, smem + K1_DUMMY);
    wait_vm<0>();
  } else if (idx < 30) {
    const int t = 22 * (xcd & 3) + (idx - 8);
    gemm1_block(smem, x, wg, wu, hbuf, stats, 8 * (xcd >> 2) + (t & 7), t >> 3, (idx - 8) >> 3 STAMP_PASS);
  } else {
    const int part = (idx - 30) * 8 + xcd;
    if (part == 0 && threadIdx.x < 8) cnt[threadIdx.x * 64] = 0u;
    __builtin_amdgcn_s_sleep(100);
    for (int i = threadIdx.x; i < 1024; i += 512) {
      const int l = (idx - 30) * 1024 + i;
      touch(ssmk + (size_t)(2 * (8 * (l >> 6) + xcd)) * S_ + (l & 63) * 64, smem + K1_DUMMY);
    }
    wait_vm<0>();
  }
  STAMP(15);
  STAMP_FLUSH(0);
}

constexpr int K2_STAGE = 32768;
constexpr int K2_NT = FP_ / 128;
constexpr int CQ_PITCH = 4576;
constexpr int CT_PITCH = 16512;
constexpr int K2_Q = 3 * K2_STAGE;
constexpr int K2_TAB = K2_Q + 2 * CQ_PITCH;
constexpr int K2_B3 = K2_TAB;
constexpr int K2_TT = K2_B3 + K2_STAGE;
constexpr int K2_X = K2_TT + 8192;
constexpr int K2_ST = K2_Q;
constexpr int K2_DUMMY = K2_X + 8192;
constexpr int K2_LDS = K2_DUMMY + 256;
DI constexpr int k2_buf(int kt) { return (kt & 3) < 3 ? (kt & 3) * K2_STAGE : K2_B3; }

typedef __attribute__((address_space(1))) unsigned gu32;

constexpr int conv_pair(int m) {
  int c = 0;
  for (int t = 0; t < 8; ++t)
    for (int o = 0; o <= t; ++o)
      if (t + o < 8) {
        if (c++ == m) return t * 8 + o;
        if (o < t && c++ == m) return o * 8 + t;
      }
  return 0;
}

__global__ __launch_bounds__(512, 2) void k2_kernel(const u16* __restrict__ x, const u16* __restrict__ hbuf,
                                                     const u16* __restrict__ wd, const u16* __restrict__ ur,
                                                     const u16* __restrict__ ssmk, const u16* __restrict__ ssmd,
                                                     u16* tT, const float2* __restrict__ stats,
                                                     unsigned* cnt, u16* __restrict__ out) {
  __shared__ __attribute__((aligned(16))) char smem[K2_LDS];
  const int tid = threadIdx.x, lane = tid & 63;
  const int w = __builtin_amdgcn_readfirstlane(tid >> 6);
  const int kg = w >> 2, wm = (w >> 1) & 1, wn = w & 1;
  const int r = lane & 15, g = lane >> 4, q = (lane & 15) >> 2, p = lane & 3;
  const int b = blockIdx.x, xcd = b & 7, idx = b >> 3;
  const int row0 = (4 * xcd + (idx >> 3)) * 64, d0 = (idx & 7) * 64;
  STAMP_DECL;
  STAMP(0);

  const int cI = w & 1, qd = w >> 1;
  const int ch = 2 * b + cI;
  char* cq = smem + K2_Q + cI * CQ_PITCH;
  char* ct = smem + K2_TAB + cI * CT_PITCH;
  glds16(ur + (size_t)ch * S_ + (qd * 64 + lane) * 8, cq + qd * 1024);
  typedef __attribute__((ext_vector_type(3))) unsigned u32x3;
  unsigned he[2][3], dbits;
  u32x3 hm[3];
  const int mb = qd * 64 + lane - 8;
  {
    const unsigned* hrow = (const unsigned*)(ssmk + (size_t)ch * S_);
#pragma unroll
    for (int k = 0; k < 3; ++k) {
      const int m0 = mb + k;
      asm volatile("global_load_dword %0, %1, off" : "=v"(he[0][k]) : "v"(hrow + (m0 < 0 ? 0 : m0)) : "memory");
    }
#pragma unroll
    for (int it = 1; it < 4; ++it)
      asm volatile("global_load_dwordx3 %0, %1, off" : "=v"(hm[it - 1]) : "v"(hrow + it * 256 + mb) : "memory");
#pragma unroll
    for (int k = 0; k < 3; ++k) {
      const int m4 = 1024 + mb + k;
      asm volatile("global_load_dword %0, %1, off" : "=v"(he[1][k]) : "v"(hrow + (m4 > 1023 ? 1023 : m4)) : "memory");
    }
    asm volatile("global_load_ushort %0, %1, off" : "=v"(dbits) : "v"(ssmd + ch) : "memory");
  }

  unsigned aoffs[2], woffs[2], woffl[2];
#pragma unroll
  for (int e = 0; e < 2; ++e) {
    const int ar = 8 * w + 4 * e + (lane >> 4);
    aoffs[e] = (unsigned)((row0 + ar) * FP_ + (((lane & 15) ^ (ar & 15)) << 3)) * 2u;
    const int kr = 16 * w + 8 * e + (lane >> 3);
    const int phi_s = ((kr >> 1) & 1) | (((kr >> 3) & 1) << 1);
    const int wcol = d0 + ((((((lane & 7) >> 1) ^ phi_s) << 1) | (lane & 1)) << 3);
    woffs[e] = (unsigned)(kr * D_ + wcol) * 2u;
    const int krl = kr > F_ - 1 - 1280 ? F_ - 1 - 1280 : kr;
    woffl[e] = (unsigned)(krl * D_ + wcol) * 2u;
  }
  auto dma_piece = [&](int kt, int n) {
    char* sb = smem + k2_buf(kt);
    const int e = n & 1;
    if (n < 2) glds16s(hbuf + kt * 128, aoffs[e], sb + (2 * w + e) * 1024);
    else glds16s(wd + kt * 128 * D_, kt == K2_NT - 1 ? woffl[e] : woffs[e], sb + 16384 + (2 * w + e) * 1024);
  };
  const u16* ptouch;
  {
    const int hr = tid / 22, hl = tid % 22;
    int kr = 344 * (idx >> 3) + (tid - 176);
    kr = kr > F_ - 1 ? F_ - 1 : kr;
    const u16* ph = hbuf + (size_t)(row0 + 8 * (idx & 7) + (hr & 7)) * FP_ + hl * 64;
    const u16* pw = wd + (size_t)kr * D_ + d0;
    ptouch = tid < 176 ? ph : pw;
  }
#define K2_EARLY 0
  __builtin_amdgcn_s_barrier();
  touch(ptouch, smem + K2_DUMMY);
#pragma unroll
  for (int kt = 0; kt < K2_EARLY; ++kt)
#pragma unroll
    for (int n = 0; n < 4; ++n) dma_piece(kt, n);

  if (lane < 30 && qd == 0) *(u32x4*)(cq + 4096 + lane * 16) = u32x4{0u, 0u, 0u, 0u};
  asm volatile("s_waitcnt vmcnt(%10)"
               : "+v"(he[0][0]), "+v"(he[0][1]), "+v"(he[0][2]), "+v"(he[1][0]), "+v"(he[1][1]), "+v"(he[1][2]),
                 "+v"(hm[0]), "+v"(hm[1]), "+v"(hm[2]), "+v"(dbits) :
               "i"(1 + 4 * K2_EARLY)
               : "memory");
  const float dch = __uint_as_float(dbits << 16);
#pragma unroll
  for (int it = 0; it < 5; ++it) {
    const int i = it * 256 + qd * 64 + lane;
    unsigned d[3];
#pragma unroll
    for (int k = 0; k < 3; ++k) {
      if (it == 0) d[k] = mb + k < 0 ? 0u : he[0][k];
      else if (it == 4) d[k] = 1024 + mb + k > 1023 ? 0u : he[1][k];
      else d[k] = hm[it - 1][k];
    }
    u32x4 e;
    e[0] = d[0];
    e[1] = d[1];
    e[2] = __builtin_amdgcn_alignbit(d[1], d[0], 16);
    e[3] = __builtin_amdgcn_alignbit(d[2], d[1], 16);
    if (i < 1032) *(u32x4*)(ct + i * 16) = e;
  }
  raw_barrier();
  STAMP(13);
  {
    f32x4 cacc[8];
#pragma unroll
    for (int n = 0; n < 8; ++n) cacc[n] = f32x4{0.f, 0.f, 0.f, 0.f};
    const char* a_lane = ct + (64 * qd + 1 + r + 8 * g) * 8;
    const char* b_lane = cq + 4064 + 128 * qd - 32 * r + 16 * g;
    s16x4 al[2][8], ah[2][8];
    bf16x8 bq[2][8];
    auto rd = [&](int kk, int n) {
      const int t = n / 3, k = n % 3;
      if (k == 0) al[kk][t] = *(const s16x4*)(a_lane + kk * 256 + t * 2048);
      else if (k == 1) ah[kk][t] = *(const s16x4*)(a_lane + kk * 256 + t * 2048 + 32);
      else bq[kk][t] = *(const bf16x8*)(b_lane + kk * 64 - t * 512);
    };
#pragma unroll
    for (int n = 0; n < 24; ++n) rd(0, n);
    SB;
    auto side = [&](int sl) {
      if (sl < 24) rd(1, sl);
      SB;
    };
    static_for<0, 72>([&](auto SL) {
      constexpr int sl = SL.value, kk = sl / 36;
      constexpr int ai = conv_pair(sl % 36) >> 3, bi = conv_pair(sl % 36) & 7;
      cacc[ai + bi] = mfma16(cat8(al[kk][ai], ah[kk][ai]), bq[kk][bi], cacc[ai + bi]);
      side(sl);
    });
    if (kg == 0) {
#pragma unroll
      for (int kt = 0; kt < 3; ++kt)
#pragma unroll
        for (int n = 0; n < 4; ++n) {
          dma_piece(kt, n);
          char* sb = smem + k2_buf(kt);
          const int e = n & 1;
          if (n < 2) glds16s(hbuf + kt * 128 + 32 * FP_, aoffs[e], sb + (2 * w + e) * 1024 + 8192);
          else glds16s(wd + kt * 128 * D_ + 64 * D_, woffs[e], sb + 16384 + (2 * w + e) * 1024 + 8192);
        }
    }
    raw_barrier();
    char* red = smem + K2_TAB + cI * 24576;
    f32x4 own[4];
#pragma unroll
    for (int n = 0; n < 8; ++n) {
      const int o = 2 + (n >> 2);
      if (o == qd) own[n & 3] = cacc[n];
      else *(f32x4*)(red + (n * 3 + (qd < o ? qd : qd - 1)) * 1024 + lane * 16) = cacc[n];
    }
    raw_barrier();
    if (kg == 1) {
#pragma unroll
      for (int i = 0; i < 4; ++i) {
        const int n = 4 * (qd - 2) + i;
        f32x4 y = own[i];
#pragma unroll
        for (int k = 0; k < 3; ++k) y += *(const f32x4*)(red + (n * 3 + k) * 1024 + lane * 16);
        const int s = 256 * n + 16 * r + 4 * g;
        const u32x2 uu = *(const u32x2*)(cq + 2 * (2044 - s));
        const float t0 = fmaf(bf_hi(uu[1]), dch, y[0]);
        const float t1 = fmaf(bf_lo(uu[1]), dch, y[1]);
        const float t2 = fmaf(bf_hi(uu[0]), dch, y[2]);
        const float t3 = fmaf(bf_lo(uu[0]), dch, y[3]);
        const u32x2 o = {pk_bf16(t0, t1), pk_bf16(t2, t3)};
        asm volatile("global_store_dwordx2 %0, %1, off sc1\n\ts_nop 1" ::"v"(tT + (size_t)ch * S_ + s), "v"(o) : "memory");
      }
    }
    raw_barrier();
  }
  STAMP(14);

  const int erow = row0 + 32 * wm + 16 * kg + r;
  const int edoff = d0 + 32 * wn + 16 * (g & 1) + 8 * (g >> 1);

  int aoff[2][2], boff[2];
#pragma unroll
  for (int i = 0; i < 2; ++i)
#pragma unroll
    for (int hs = 0; hs < 2; ++hs)
      aoff[i][hs] = (32 * wm + 16 * i + r) * 256 + (((4 * (kg + 2 * hs) + g) ^ r) << 4);
  const int phiL = ((q >> 1) & 1) | ((g & 1) << 1);
#pragma unroll
  for (int T = 0; T < 2; ++T)
    boff[T] = 16384 + (32 * kg + 8 * g + q) * 128 + (((2 * wn + T) ^ phiL) << 5) + p * 8;

  f32x4 acc[2][2];
#pragma unroll
  for (int T = 0; T < 2; ++T)
#pragma unroll
    for (int i = 0; i < 2; ++i) acc[T][i] = f32x4{0.f, 0.f, 0.f, 0.f};

  struct Frags2 { bf16x8 hf[2][2]; s16x4 wl[2][2], wh[2][2]; };
  Frags2 fs[2];
  auto read_piece = [&](Frags2& f, int kt, int n) {
    const char* sb = smem + k2_buf(kt);
    const int hs = n / 6, k = n % 6;
    if (k < 2) f.hf[hs][k] = *(const bf16x8*)(sb + aoff[k][hs]);
    else if ((k & 1) == 0) f.wl[hs][(k - 2) >> 1] = tr_read(sb + boff[(k - 2) >> 1] + hs * 8192);
    else f.wh[hs][(k - 2) >> 1] = tr_read(sb + boff[(k - 2) >> 1] + hs * 8192 + 512);
  };
  auto mma_one = [&](const Frags2& f, int m) {
    const int hs = m >> 2, T = (m >> 1) & 1, i = m & 1;
    acc[T][i] = mfma16(cat8(f.wl[hs][T], f.wh[hs][T]), f.hf[hs][i], acc[T][i]);
  };
  u32x4 tval;
  unsigned cval;
  gu32* const cptr = (gu32*)cnt + (idx & 7) * 64;
  static_for<0, K2_NT + 1>([&](auto KT) {
    constexpr int kt = KT.value;
    if constexpr (kt < K2_NT) {
      if constexpr (kt == 0) wait_vm<16>();
      else if constexpr (kt == 1) wait_vm<12>();
      else if constexpr (kt <= 7) wait_vm<8>();
      else if constexpr (kt == 8) wait_vm<9>();
      else if constexpr (kt == 9) wait_vm<6>();
      else wait_vm<1>();
      raw_barrier();
      STAMP(1 + kt);
      if constexpr (kt == 2)
        if (tid == 0) __hip_atomic_fetch_add((gu32*)cnt + (b >> 5) * 64, 1u, __ATOMIC_RELAXED, __HIP_MEMORY_SCOPE_AGENT);
    }
    if constexpr (kt == 9) {
      asm volatile("" : "+v"(cval));
      if (__builtin_amdgcn_readfirstlane(cval) < 32u) {
        for (int spin = 0; spin < (1 << 16); ++spin) {
          if (__hip_atomic_load(cptr, __ATOMIC_RELAXED, __HIP_MEMORY_SCOPE_AGENT) >= 32u) break;
          __builtin_amdgcn_s_sleep(2);
        }
      }
      __builtin_amdgcn_fence(__ATOMIC_ACQUIRE, "wavefront");
      asm volatile("global_load_dwordx4 %0, %1, off sc1" : "=v"(tval)
                   : "v"(tT + (size_t)(d0 + 8 * w + (lane >> 3)) * S_ + row0 + (lane & 7) * 8) : "memory");
    }
    SB;
#pragma unroll
    for (int n = 0; n < 12; ++n) {
      if (kt < K2_NT) read_piece(fs[kt & 1], kt, n);
      if (kt > 0 && n < 8) mma_one(fs[(kt - 1) & 1], n);
      if (kt + 3 < K2_NT && (n % 3) == 2) dma_piece(kt + 3, n / 3);
      if (kt == 7 && n == 0) asm volatile("global_load_dword %0, %1, off sc1" : "=v"(cval) : "v"(cptr) : "memory");
      if (kt == 8 && n == 5) glds16(x + (size_t)(row0 + 8 * w + (lane >> 3)) * D_ + d0 + (lane & 7) * 8, smem + K2_X + w * 1024);
      if (kt == 8 && n == 8) glds16(stats + row0 + 2 * (lane & 31), smem + K2_ST);
      SB;
    }
  });
  STAMP(12);
  asm volatile("s_waitcnt vmcnt(0)" : "+v"(tval)::"memory");
  *(u32x4*)(smem + K2_TT + tid * 16) = tval;
  raw_barrier();
  const int lrow = 32 * wm + 16 * kg + r;
  const u32x4 xv = *(const u32x4*)(smem + K2_X + lrow * 128 + (32 * wn + 16 * (g & 1) + 8 * (g >> 1)) * 2);
  const f32x2 stv = *(const f32x2*)(smem + K2_ST + lrow * 8);
  {
    f32x4* xb = (f32x4*)smem;
    const int wq = w & 3;
#pragma unroll
    for (int T = 0; T < 2; ++T) xb[((kg * 4 + wq) * 2 + T) * 64 + lane] = kg ? acc[T][0] : acc[T][1];
    raw_barrier();
    f32x4 fin[2];
#pragma unroll
    for (int T = 0; T < 2; ++T) {
      const f32x4 o = xb[(((kg ^ 1) * 4 + wq) * 2 + T) * 64 + lane];
      fin[T] = (kg ? acc[T][1] : acc[T][0]) + o;
    }
    unsigned pb[2][2], pt[2][2];
#pragma unroll
    for (int T = 0; T < 2; ++T) {
      pb[T][0] = pk_bf16(fin[T][0], fin[T][1]);
      pb[T][1] = pk_bf16(fin[T][2], fin[T][3]);
      const s16x4 tv = tr_read(smem + K2_TT + (32 * wn + 16 * T + 4 * g + q) * 128 + (32 * wm + 16 * kg + 4 * p) * 2);
      const u32x2 tu = __builtin_bit_cast(u32x2, tv);
      pt[T][0] = tu[0];
      pt[T][1] = tu[1];
    }
    auto b0 = __builtin_amdgcn_permlane16_swap(pb[0][0], pb[1][0], false, false);
    auto b1 = __builtin_amdgcn_permlane16_swap(pb[0][1], pb[1][1], false, false);
    auto t0 = __builtin_amdgcn_permlane16_swap(pt[0][0], pt[1][0], false, false);
    auto t1 = __builtin_amdgcn_permlane16_swap(pt[0][1], pt[1][1], false, false);
    const u32x4 bv = {b0[0], b1[0], b0[1], b1[1]};
    const u32x4 tv = {t0[0], t1[0], t0[1], t1[1]};
    u32x4 o;
#pragma unroll
    for (int j = 0; j < 4; ++j) {
      float res[2];
#pragma unroll
      for (int hh = 0; hh < 2; ++hh) {
        const float xe = hh ? bf_hi(xv[j]) : bf_lo(xv[j]);
        const float te = hh ? bf_hi(tv[j]) : bf_lo(tv[j]);
        const float be = hh ? bf_hi(bv[j]) : bf_lo(bv[j]);
        const float xn = round_bf(round_bf(xe - stv[0]) * stv[1]);
        const float a = round_bf(xn * te);
        res[hh] = round_bf(xe + a) + be;
      }
      o[j] = pk_bf16(res[0], res[1]);
    }
    *(u32x4*)(out + (size_t)erow * D_ + edoff) = o;
  }
  STAMP(15);
  STAMP_FLUSH(1);
}

extern "C" void kernel_launch(void* const* d_in, const int* in_sizes, int n_in, void* d_out, int out_size,
                              void* d_ws, size_t ws_size, hipStream_t stream) {
  const u16* q = (const u16*)d_in[0];
  const u16* k = (const u16*)d_in[1];
  const u16* v = (const u16*)d_in[2];
  const u16* shk = (const u16*)d_in[3];
  const u16* ssmk = (const u16*)d_in[4];
  const u16* ssmd = (const u16*)d_in[5];
  const u16* wg = (const u16*)d_in[7];
  const u16* wu = (const u16*)d_in[8];
  const u16* wd = (const u16*)d_in[9];
  char* ws = (char*)d_ws;
  u16* hbuf = (u16*)(ws + WS_H);
  u16* tT = (u16*)(ws + WS_T);
  u16* ur = (u16*)(ws + WS_U);
  float2* stats = (float2*)(ws + WS_ST);
  unsigned* cnt = (unsigned*)(ws + WS_CNT);
  k1_kernel<<<256, 512, 0, stream>>>(q, k, v, shk, ssmk, wg, wu, wd, hbuf, ur, stats, cnt);
  k2_kernel<<<256, 512, 0, stream>>>(q, hbuf, wd, ur, ssmk, ssmd, tT, stats, cnt, (u16*)d_out);
}
```

```cpp
#include <hip/hip_runtime.h>
#include <hip/hip_bf16.h>
#include <stdint.h>

typedef __attribute__((ext_vector_type(8))) short bf16x8;
typedef __attribute__((ext_vector_type(4))) short s16x4;
typedef __attribute__((ext_vector_type(2))) __bf16 bf16x2;
typedef __attribute__((ext_vector_type(4))) float f32x4;
typedef __attribute__((ext_vector_type(2))) float f32x2;
typedef __attribute__((ext_vector_type(4))) unsigned u32x4;
typedef __attribute__((ext_vector_type(2))) unsigned u32x2;
typedef __attribute__((address_space(3))) s16x4* lds_s16x4_ptr;
typedef unsigned short u16;

#define DI __device__ __forceinline__
#define HELPER_PF 1
#define TOUCH_FIRST 1

constexpr int S_ = 2048, D_ = 512, F_ = 1376, FP_ = 1408;

constexpr size_t WS_H = 0;
constexpr size_t WS_T = 6u * 1024 * 1024;
constexpr size_t WS_U = 8u * 1024 * 1024;
constexpr size_t WS_ST = 10u * 1024 * 1024;
constexpr size_t WS_CNT = 10u * 1024 * 1024 + 65536;

template <int N> DI void wait_vm() { asm volatile("s_waitcnt vmcnt(%0)" ::"i"(N) : "memory"); }
DI void wait_lgkm0() { asm volatile("s_waitcnt lgkmcnt(0)" ::: "memory"); }
DI void raw_barrier() {
  asm volatile("s_waitcnt lgkmcnt(0)" ::: "memory");
  __builtin_amdgcn_s_barrier();
  asm volatile("" ::: "memory");
}

DI unsigned pk_bf16(float lo, float hi) {
  f32x2 f = {lo, hi};
  return __builtin_bit_cast(unsigned, __builtin_convertvector(f, bf16x2));
}
DI float bf_lo(unsigned v) { return __uint_as_float(v << 16); }
DI float bf_hi(unsigned v) { return __uint_as_float(v & 0xffff0000u); }
DI float round_bf(float x) { return bf_lo(pk_bf16(x, x)); }
DI float dot2bf(unsigned a, unsigned b, float c) {
  return __builtin_amdgcn_fdot2_f32_bf16(__builtin_bit_cast(bf16x2, a), __builtin_bit_cast(bf16x2, b), c, false);
}
DI s16x4 tr_read(const char* p) {
  return __builtin_amdgcn_ds_read_tr16_b64_v4i16((lds_s16x4_ptr)p);
}
DI bf16x8 cat8(s16x4 lo, s16x4 hi) { return __builtin_shufflevector(lo, hi, 0, 1, 2, 3, 4, 5, 6, 7); }
DI f32x4 mfma16(bf16x8 a, bf16x8 b, f32x4 c) { return __builtin_amdgcn_mfma_f32_16x16x32_bf16(a, b, c, 0, 0, 0); }

DI void glds16(const void* g, char* lds) {
  unsigned keep;
  const unsigned dst = __builtin_amdgcn_readfirstlane((unsigned)(size_t)lds);
  asm volatile("s_mov_b32 %0, m0\n\ts_mov_b32 m0, %2\n\ts_nop 0\n\tglobal_load_lds_dwordx4 %1, off\n\ts_mov_b32 m0, %0"
               : "=&s"(keep) : "v"(g), "s"(dst) : "memory");
}
DI void glds16s(const void* sbase, unsigned voff, char* lds) {
  unsigned keep;
  const unsigned dst = __builtin_amdgcn_readfirstlane((unsigned)(size_t)lds);
  asm volatile("s_mov_b32 %0, m0\n\ts_mov_b32 m0, %3\n\ts_nop 0\n\tglobal_load_lds_dwordx4 %1, %2\n\ts_mov_b32 m0, %0"
               : "=&s"(keep) : "v"(voff), "s"(sbase), "s"(dst) : "memory");
}
DI float mul_s(float a, float s) { float d; asm("v_mul_f32 %0, %2, %1" : "=v"(d) : "v"(a), "s"(s)); return d; }
DI float fma_s(float a, float s, float c) { float d; asm("v_fma_f32 %0, %1, %2, %3" : "=v"(d) : "v"(a), "s"(s), "v"(c)); return d; }
DI float mul_v(float a, float b) { float d; asm("v_mul_f32 %0, %1, %2" : "=v"(d) : "v"(a), "v"(b)); return d; }
DI void touch(const void* g, char* dummy) {
  unsigned keep;
  const unsigned dst = __builtin_amdgcn_readfirstlane((unsigned)(size_t)dummy);
  asm volatile("s_mov_b32 %0, m0\n\ts_mov_b32 m0, %2\n\ts_nop 0\n\tglobal_load_lds_dword %1, off\n\ts_mov_b32 m0, %0"
               : "=&s"(keep) : "v"(g), "s"(dst) : "memory");
}

#define WT_MODE 1
DI void store16_wt(void* p, u32x4 v) {
  asm volatile("global_store_dwordx4 %0, %1, off sc1\n\ts_nop 1" ::"v"(p), "v"(v) : "memory");
}
DI void store8_wt(void* p, u32x2 v) {
  asm volatile("global_store_dwordx2 %0, %1, off sc1\n\ts_nop 1" ::"v"(p), "v"(v) : "memory");
}

#define STAMP_DECL do {} while (0)
#define STAMP(I) do {} while (0)
#define STAMP_FLUSH(K) do {} while (0)
#define STAMP_ARG
#define STAMP_PASS

template <int I, int N, class Fn> DI void static_for(Fn&& fn) {
  if constexpr (I < N) {
    fn(std::integral_constant<int, I>{});
    static_for<I + 1, N>(fn);
  }
}

constexpr int K1_DUMMY = 147456;
constexpr int K1_LDS = K1_DUMMY + 256;

constexpr int G1_A = 0;
constexpr int G1_W = 49152;
#define SB __builtin_amdgcn_sched_barrier(0)

DI void gemm1_block(char* smem, const u16* __restrict__ x, const u16* __restrict__ wg, const u16* __restrict__ wu,
                    u16* __restrict__ hbuf, float2* __restrict__ stats, int mt, int ft, int rank STAMP_ARG) {
  const int tid = threadIdx.x, lane = tid & 63;
  const int w = __builtin_amdgcn_readfirstlane(tid >> 6);
  const int wm = w >> 2, wn = w & 3;
  const int r = lane & 15, g = lane >> 4, q = (lane & 15) >> 2, p = lane & 3;
  const int row0 = mt * 128, f0 = ft * 128;

  unsigned aoffs[2], woffs[2];
#pragma unroll
  for (int e = 0; e < 2; ++e) {
    const int i = 2 * w + e;
    const int row = 8 * i + (lane >> 3);
    aoffs[e] = (unsigned)((row0 + row) * D_ + (((lane & 7) ^ (row & 7)) << 3)) * 2u;
    const int kr = 4 * i + (lane >> 4);
    const int phi = (kr & 3) | (((kr >> 3) & 1) << 2);
    const int sc = ((((lane & 15) >> 1) ^ phi) << 1) | (lane & 1);
    int col = f0 + sc * 8;
    col = col > F_ - 8 ? F_ - 8 : col;
    woffs[e] = (unsigned)(kr * F_ + col) * 2u;
  }
  auto dma_piece = [&](int kt, int n) {
    const int b = kt % 3, e = n & 1;
    if (n < 2) glds16s(x + kt * 64, aoffs[e], smem + G1_A + b * 16384 + (2 * w + e) * 1024);
    else if (n < 4) glds16s(wg + kt * 64 * F_, woffs[e], smem + G1_W + b * 32768 + (2 * w + e) * 1024);
    else glds16s(wu + kt * 64 * F_, woffs[e], smem + G1_W + b * 32768 + 16384 + (2 * w + e) * 1024);
  };
#pragma unroll
  for (int kt = 0; kt < 3; ++kt)
#pragma unroll
    for (int n = 0; n < 6; ++n) dma_piece(kt, n);

  int aoff[4][2];
#pragma unroll
  for (int i = 0; i < 4; ++i)
#pragma unroll
    for (int ks = 0; ks < 2; ++ks)
      aoff[i][ks] = G1_A + (64 * wm + 16 * (i ^ wn) + r) * 128 + (((4 * ks + g) ^ (r & 7)) << 4);
  const int phiL = q | ((g & 1) << 2);
  int boff[4];
#pragma unroll
  for (int T = 0; T < 4; ++T) {
    const int mat = (T >> 1) ^ wm;
    boff[T] = G1_W + mat * 16384 + (8 * g + q) * 256 + (((2 * wn + (T & 1)) ^ phiL) << 5) + p * 8;
  }

  f32x4 acc[4][4];
#pragma unroll
  for (int T = 0; T < 4; ++T)
#pragma unroll
    for (int i = 0; i < 4; ++i) acc[T][i] = f32x4{0.f, 0.f, 0.f, 0.f};
  f32x4 accS = {0.f, 0.f, 0.f, 0.f}, accR = accS, accC[2] = {accS, accS};
  const bf16x8 ones = {0x3f80, 0x3f80, 0x3f80, 0x3f80, 0x3f80, 0x3f80, 0x3f80, 0x3f80};

  struct Frags { s16x4 wl[4], wh[4]; bf16x8 xf[4]; };
  Frags fa, fb2;
  auto read_piece = [&](Frags& f, int kt, int ks, int n) {
    const int b = kt % 3;
    if (n < 4) f.xf[n] = *(const bf16x8*)(smem + b * 16384 + aoff[n][ks]);
    else {
      const int T = (n - 4) >> 1;
      if ((n & 1) == 0) f.wl[T] = tr_read(smem + b * 32768 + ks * 8192 + boff[T]);
      else f.wh[T] = tr_read(smem + b * 32768 + ks * 8192 + 1024 + boff[T]);
    }
  };
  auto mma_one = [&](const Frags& f, int m) {
    const int T = m / 5, i = m % 5;
    if (i < 4) acc[T][i] = mfma16(cat8(f.wl[T], f.wh[T]), f.xf[i], acc[T][i]);
    else if (T == 0) accS = mfma16(f.xf[0], f.xf[0], accS);
    else if (T == 1) accR = mfma16(ones, f.xf[0], accR);
    else if (T == 2) accC[0] = mfma16(cat8(f.wl[0], f.wh[0]), ones, accC[0]);
    else accC[1] = mfma16(cat8(f.wl[1], f.wh[1]), ones, accC[1]);
  };

  wait_vm<12>();
  raw_barrier();
  STAMP(1);
#pragma unroll
  for (int n = 0; n < 12; ++n) read_piece(fa, 0, 0, n);
  static_for<0, 8>([&](auto KT) {
    constexpr int kt = KT.value;
    SB;
#pragma unroll
    for (int m = 0; m < 16; ++m) {
      mma_one(fa, m);
      if (m < 12) read_piece(fb2, kt, 1, m);
      SB;
    }
    if constexpr (kt < 7) {
      if constexpr (kt < 6) wait_vm<6>(); else wait_vm<0>();
      raw_barrier();
      STAMP(2 + kt);
      SB;
    }
#pragma unroll
    for (int m = 16; m < 20; ++m) {
      mma_one(fa, m);
      if (m == 17 && kt < 5) dma_piece(kt + 3, 0);
      SB;
    }
#pragma unroll
    for (int m = 0; m < 20; ++m) {
      mma_one(fb2, m);
      if (m < 12 && kt < 7) read_piece(fa, kt + 1, 0, m);
      if ((m == 1 || m == 5 || m == 9 || m == 13 || m == 17) && kt < 5) dma_piece(kt + 3, 1 + (m >> 2));
      SB;
    }
  });
  STAMP(9);
  float2* st_s = (float2*)smem;
  float* cs_s = (float*)(smem + 1024);
  if (g == (r >> 2)) {
    const int rr = r & 3;
    const float s2 = rr == 0 ? accS[0] : rr == 1 ? accS[1] : rr == 2 ? accS[2] : accS[3];
    const float mean = accR[0] * (1.f / D_);
    float var = s2 * (1.f / D_) - mean * mean;
    var = var < 0.f ? 0.f : var;
    const float mb = round_bf(mean);
    const float vb = round_bf(var);
    const float sd = round_bf(sqrtf(round_bf(vb + 1e-5f)));
    const float2 stv = make_float2(mb, 1.f / sd);
    const int row = 64 * wm + 16 * wn + r;
    st_s[row] = stv;
    if (ft == 0) stats[row0 + row] = stv;
  }
  if (r == 0) {
    *(f32x4*)(cs_s + wm * 128 + 32 * wn + 4 * g) = accC[0];
    *(f32x4*)(cs_s + wm * 128 + 32 * wn + 16 + 4 * g) = accC[1];
  }
  raw_barrier();
  const bool valid = (f0 + 32 * wn) < F_;
  f32x4 ca[2], cb[2];
#pragma unroll
  for (int T = 0; T < 2; ++T) {
    ca[T] = accC[T];
    cb[T] = *(const f32x4*)(cs_s + (wm ^ 1) * 128 + 32 * wn + 16 * T + 4 * g);
  }
#pragma unroll
  for (int i = 0; i < 4; ++i) {
    const int row = 64 * wm + 16 * (i ^ wn) + r;
    const float2 stv = st_s[row];
    const float nm = -stv.x, r2 = stv.y * stv.y;
    unsigned pk[2][2];
#pragma unroll
    for (int T = 0; T < 2; ++T) {
      const f32x4 ga = acc[T][i] + nm * ca[T];
      const f32x4 gb = acc[2 + T][i] + nm * cb[T];
      const f32x4 hv = (ga * r2) * gb;
      pk[T][0] = pk_bf16(hv[0], hv[1]);
      pk[T][1] = pk_bf16(hv[2], hv[3]);
    }
    auto r0 = __builtin_amdgcn_permlane16_swap(pk[0][0], pk[1][0], false, false);
    auto r1 = __builtin_amdgcn_permlane16_swap(pk[0][1], pk[1][1], false, false);
    u32x4 o = {r0[0], r1[0], r0[1], r1[1]};
    if (!valid) o = u32x4{0u, 0u, 0u, 0u};
    store16_wt(hbuf + (size_t)(row0 + row) * FP_ + f0 + 32 * wn + 16 * (g & 1) + 8 * (g >> 1), o);
  }
}

constexpr int PU_PITCH = 520;
DI void prep_block(char* smem, const u16* __restrict__ kin, const u16* __restrict__ vin,
                   const u16* __restrict__ shk, u16* __restrict__ ur, int stripe, int rb) {
  const int tid = threadIdx.x;
  const int rg = tid >> 3, cc = tid & 7;
  const int c0 = 64 * stripe + 8 * cc, s0 = 256 * rb + 4 * rg;
  u32x4 kx[7], vx[4];
#pragma unroll
  for (int i = 0; i < 7; ++i) {
    const int sr = s0 - 3 + i;
    kx[i] = *(const u32x4*)(kin + (size_t)(sr < 0 ? 0 : sr) * D_ + c0);
    if (i < 3 && sr < 0) kx[i] = u32x4{0u, 0u, 0u, 0u};
  }
#pragma unroll
  for (int i = 0; i < 4; ++i) vx[i] = *(const u32x4*)(vin + (size_t)(s0 + i) * D_ + c0);
  u32x4 wv[4];
#pragma unroll
  for (int i = 0; i < 4; ++i) wv[i] = *(const u32x4*)(shk + c0 * 4 + 8 * i);
#pragma unroll
  for (int d = 0; d < 4; ++d) {
#pragma unroll
    for (int hh = 0; hh < 2; ++hh) {
      const float w0 = bf_lo(wv[d][2 * hh]), w1 = bf_hi(wv[d][2 * hh]);
      const float w2 = bf_lo(wv[d][2 * hh + 1]), w3 = bf_hi(wv[d][2 * hh + 1]);
      float kf[7], u[4];
#pragma unroll
      for (int i = 0; i < 7; ++i) kf[i] = hh ? bf_hi(kx[i][d]) : bf_lo(kx[i][d]);
#pragma unroll
      for (int i = 0; i < 4; ++i) {
        float a = kf[i] * w3;
        a = fmaf(kf[i + 1], w2, a);
        a = fmaf(kf[i + 2], w1, a);
        a = fmaf(kf[i + 3], w0, a);
        u[i] = a * (hh ? bf_hi(vx[i][d]) : bf_lo(vx[i][d]));
      }
      const u32x2 o = {pk_bf16(u[3], u[2]), pk_bf16(u[1], u[0])};
      *(u32x2*)(smem + (8 * cc + 2 * d + hh) * PU_PITCH + (252 - 4 * rg) * 2) = o;
    }
  }
  __syncthreads();
#pragma unroll
  for (int i = 0; i < 8; ++i) {
    const int e = i * 512 + tid, ch = e >> 6, pos = e & 63;
    const u32x2 v = *(const u32x2*)(smem + ch * PU_PITCH + pos * 8);
    store8_wt(ur + (size_t)(64 * stripe + ch) * S_ + (S_ - 256 * (rb + 1)) + pos * 4, v);
  }
}

__global__ __launch_bounds__(512, 2) void k1_kernel(const u16* __restrict__ x, const u16* __restrict__ kin,
                                                     const u16* __restrict__ vin, const u16* __restrict__ shk,
                                                     const u16* __restrict__ ssmk,
                                                     const u16* __restrict__ wg, const u16* __restrict__ wu,
                                                     const u16* __restrict__ wd,
                                                     u16* __restrict__ hbuf, u16* __restrict__ ur,
                                                     float2* __restrict__ stats, unsigned* __restrict__ cnt) {
  __shared__ __attribute__((aligned(16))) char smem[K1_LDS];
  const int b = blockIdx.x, xcd = b & 7, idx = b >> 3;
  STAMP_DECL;
  STAMP(0);
  asm volatile("" ::"s"(x), "s"(kin), "s"(vin), "s"(shk), "s"(ssmk), "s"(wg), "s"(wu), "s"(wd), "s"(hbuf), "s"(ur),
               "s"(stats), "s"(cnt));
  if (idx < 8 || idx >= 30) {
    const int hi = idx < 8 ? idx : idx - 22, tid = threadIdx.x;
    const int q4 = xcd & 3, clo = 128 * ((22 * q4) >> 3);
    int chi = 128 * (((22 * q4 + 21) >> 3) + 1) - 2;
    chi = chi > F_ - 2 ? F_ - 2 : chi;
    const int e = hi * 256 + (tid & 255), e2 = e - 1024;
    int col = clo + 64 * (e2 % 9);
    col = col > chi ? chi : col;
    const int krl = e2 / 18 > 63 ? 63 : e2 / 18;
    const u16* pb = e < 1024 ? x + (size_t)(1024 * (xcd >> 2) + e) * D_ : (((e2 % 18) / 9) ? wu : wg) + krl * F_ + col;
    const int step = e < 1024 ? 64 : 64 * F_;
    for (int st = 3 + (tid >> 8); st < 8; st += 2) touch(pb + st * step, smem + K1_DUMMY);
  }
  if (idx < 8) {
#define PREP_DELAY 40
    if (PREP_DELAY) __builtin_amdgcn_s_sleep(PREP_DELAY);
    prep_block(smem, kin, vin, shk, ur, xcd, idx);
#define PREP_SLEEP 1
    __builtin_amdgcn_s_sleep(PREP_SLEEP);
    for (int i = threadIdx.x; i < 1376; i += 512) touch(wd + (size_t)(idx * 1376 + i) * 64, smem + K1_DUMMY);
    wait_vm<0>();
  } else if (idx < 30) {
    const int t = 22 * (xcd & 3) + (idx - 8);
    gemm1_block(smem, x, wg, wu, hbuf, stats, 8 * (xcd >> 2) + (t & 7), t >> 3, (idx - 8) >> 3 STAMP_PASS);
  } else {
    const int part = (idx - 30) * 8 + xcd;
    if (part == 0 && threadIdx.x < 8) cnt[threadIdx.x * 64] = 0u;
    __builtin_amdgcn_s_sleep(100);
    for (int i = threadIdx.x; i < 1024; i += 512) {
      const int l = (idx - 30) * 1024 + i;
      touch(ssmk + (size_t)(2 * (8 * (l >> 6) + xcd)) * S_ + (l & 63) * 64, smem + K1_DUMMY);
    }
    wait_vm<0>();
  }
  STAMP(15);
  STAMP_FLUSH(0);
}

constexpr int K2_STAGE = 32768;
constexpr int K2_NT = FP_ / 128;
constexpr int CQ_PITCH = 4576;
constexpr int CT_PITCH = 16512;
constexpr int K2_Q = 3 * K2_STAGE;
constexpr int K2_TAB = K2_Q + 2 * CQ_PITCH;
constexpr int K2_B3 = K2_TAB;
constexpr int K2_TT = K2_B3 + K2_STAGE;
constexpr int K2_X = K2_TT + 8192;
constexpr int K2_ST = K2_Q;
constexpr int K2_DUMMY = K2_X + 8192;
constexpr int K2_LDS = K2_DUMMY + 256;
DI constexpr int k2_buf(int kt) { return (kt & 3) < 3 ? (kt & 3) * K2_STAGE : K2_B3; }

typedef __attribute__((address_space(1))) unsigned gu32;

constexpr int conv_pair(int m) {
  int c = 0;
  for (int t = 0; t < 8; ++t)
    for (int o = 0; o <= t; ++o)
      if (t + o < 8) {
        if (c++ == m) return t * 8 + o;
        if (o < t && c++ == m) return o * 8 + t;
      }
  return 0;
}

__global__ __launch_bounds__(512, 2) void k2_kernel(const u16* __restrict__ x, const u16* __restrict__ hbuf,
                                                     const u16* __restrict__ wd, const u16* __restrict__ ur,
                                                     const u16* __restrict__ ssmk, const u16* __restrict__ ssmd,
                                                     u16* tT, const float2* __restrict__ stats,
                                                     unsigned* cnt, u16* __restrict__ out) {
  __shared__ __attribute__((aligned(16))) char smem[K2_LDS];
  const int tid = threadIdx.x, lane = tid & 63;
  const int w = __builtin_amdgcn_readfirstlane(tid >> 6);
  const int kg = w >> 2, wm = (w >> 1) & 1, wn = w & 1;
  const int r = lane & 15, g = lane >> 4, q = (lane & 15) >> 2, p = lane & 3;
  const int b = blockIdx.x, xcd = b & 7, idx = b >> 3;
  const int row0 = (4 * xcd + (idx >> 3)) * 64, d0 = (idx & 7) * 64;
  STAMP_DECL;
  STAMP(0);
  asm volatile("" ::"s"(x), "s"(hbuf), "s"(wd), "s"(ur), "s"(ssmk), "s"(ssmd), "s"(tT), "s"(stats), "s"(cnt), "s"(out));

  const int cI = w >> 2, qd = w & 3;
  const int ch = 2 * b + cI;
  char* cq = smem + K2_Q + cI * CQ_PITCH;
  char* ct = smem + K2_TAB + cI * CT_PITCH;
  glds16(ur + (size_t)ch * S_ + (qd * 64 + lane) * 8, cq + qd * 1024);
  typedef __attribute__((ext_vector_type(3))) unsigned u32x3;
  unsigned he[2][3], dbits;
  u32x3 hm[3];
  const int mb = qd * 64 + lane - 8;
  {
    const unsigned* hrow = (const unsigned*)(ssmk + (size_t)ch * S_);
#pragma unroll
    for (int k = 0; k < 3; ++k) {
      const int m0 = mb + k;
      asm volatile("global_load_dword %0, %1, off" : "=v"(he[0][k]) : "v"(hrow + (m0 < 0 ? 0 : m0)) : "memory");
    }
#pragma unroll
    for (int it = 1; it < 4; ++it)
      asm volatile("global_load_dwordx3 %0, %1, off" : "=v"(hm[it - 1]) : "v"(hrow + it * 256 + mb) : "memory");
#pragma unroll
    for (int k = 0; k < 3; ++k) {
      const int m4 = 1024 + mb + k;
      asm volatile("global_load_dword %0, %1, off" : "=v"(he[1][k]) : "v"(hrow + (m4 > 1023 ? 1023 : m4)) : "memory");
    }
    asm volatile("global_load_ushort %0, %1, off" : "=v"(dbits) : "v"(ssmd + ch) : "memory");
  }

  unsigned aoffs[2], woffs[2], woffl[2];
#pragma unroll
  for (int e = 0; e < 2; ++e) {
    const int ar = 8 * w + 4 * e + (lane >> 4);
    aoffs[e] = (unsigned)((row0 + ar) * FP_ + (((lane & 15) ^ (ar & 15)) << 3)) * 2u;
    const int kr = 16 * w + 8 * e + (lane >> 3);
    const int phi_s = ((kr >> 1) & 1) | (((kr >> 3) & 1) << 1);
    const int wcol = d0 + ((((((lane & 7) >> 1) ^ phi_s) << 1) | (lane & 1)) << 3);
    woffs[e] = (unsigned)(kr * D_ + wcol) * 2u;
    const int krl = kr > F_ - 1 - 1280 ? F_ - 1 - 1280 : kr;
    woffl[e] = (unsigned)(krl * D_ + wcol) * 2u;
  }
  auto dma_piece = [&](int kt, int n) {
    char* sb = smem + k2_buf(kt);
    const int e = n & 1;
    if (n < 2) glds16s(hbuf + kt * 128, aoffs[e], sb + (2 * w + e) * 1024);
    else glds16s(wd + kt * 128 * D_, kt == K2_NT - 1 ? woffl[e] : woffs[e], sb + 16384 + (2 * w + e) * 1024);
  };
  const u16* ptouch;
  {
    const int hr = tid / 22, hl = tid % 22;
    int kr = 344 * (idx >> 3) + (tid - 176);
    kr = kr > F_ - 1 ? F_ - 1 : kr;
    const u16* ph = hbuf + (size_t)(row0 + 8 * (idx & 7) + (hr & 7)) * FP_ + hl * 64;
    const u16* pw = wd + (size_t)kr * D_ + d0;
    ptouch = tid < 176 ? ph : pw;
  }
#define K2_EARLY 0
  __builtin_amdgcn_s_barrier();
  touch(ptouch, smem + K2_DUMMY);
#pragma unroll
  for (int kt = 0; kt < K2_EARLY; ++kt)
#pragma unroll
    for (int n = 0; n < 4; ++n) dma_piece(kt, n);

  if (lane < 30 && qd == 0) *(u32x4*)(cq + 4096 + lane * 16) = u32x4{0u, 0u, 0u, 0u};
  asm volatile("s_waitcnt vmcnt(%10)"
               : "+v"(he[0][0]), "+v"(he[0][1]), "+v"(he[0][2]), "+v"(he[1][0]), "+v"(he[1][1]), "+v"(he[1][2]),
                 "+v"(hm[0]), "+v"(hm[1]), "+v"(hm[2]), "+v"(dbits) :
               "i"(1 + 4 * K2_EARLY)
               : "memory");
  const float dch = __uint_as_float(dbits << 16);
#pragma unroll
  for (int it = 0; it < 5; ++it) {
    const int i = it * 256 + qd * 64 + lane;
    unsigned d[3];
#pragma unroll
    for (int k = 0; k < 3; ++k) {
      if (it == 0) d[k] = mb + k < 0 ? 0u : he[0][k];
      else if (it == 4) d[k] = 1024 + mb + k > 1023 ? 0u : he[1][k];
      else d[k] = hm[it - 1][k];
    }
    u32x4 e;
    e[0] = d[0];
    e[1] = d[1];
    e[2] = __builtin_amdgcn_alignbit(d[1], d[0], 16);
    e[3] = __builtin_amdgcn_alignbit(d[2], d[1], 16);
    if (i < 1032) *(u32x4*)(ct + i * 16) = e;
  }
  raw_barrier();
  STAMP(13);
  {
    f32x4 cacc[8];
#pragma unroll
    for (int n = 0; n < 8; ++n) cacc[n] = f32x4{0.f, 0.f, 0.f, 0.f};
    const char* a_lane = ct + (64 * qd + 1 + r + 8 * g) * 8;
    const char* b_lane = cq + 4064 + 128 * qd - 32 * r + 16 * g;
    s16x4 al[2][8], ah[2][8];
    bf16x8 bq[2][8];
    auto rd = [&](int kk, int n) {
      const int t = n / 3, k = n % 3;
      if (k == 0) al[kk][t] = *(const s16x4*)(a_lane + kk * 256 + t * 2048);
      else if (k == 1) ah[kk][t] = *(const s16x4*)(a_lane + kk * 256 + t * 2048 + 32);
      else bq[kk][t] = *(const bf16x8*)(b_lane + kk * 64 - t * 512);
    };
#pragma unroll
    for (int n = 0; n < 24; ++n) rd(0, n);
    SB;
    auto side = [&](int sl) {
      if (sl < 24) rd(1, sl);
      SB;
    };
    static_for<0, 72>([&](auto SL) {
      constexpr int sl = SL.value, kk = sl / 36;
      constexpr int ai = conv_pair(sl % 36) >> 3, bi = conv_pair(sl % 36) & 7;
      cacc[ai + bi] = mfma16(cat8(al[kk][ai], ah[kk][ai]), bq[kk][bi], cacc[ai + bi]);
      side(sl);
    });
    if (kg == 0) {
#pragma unroll
      for (int kt = 0; kt < 3; ++kt)
#pragma unroll
        for (int n = 0; n < 4; ++n) {
          dma_piece(kt, n);
          char* sb = smem + k2_buf(kt);
          const int e = n & 1;
          if (n < 2) glds16s(hbuf + kt * 128 + 32 * FP_, aoffs[e], sb + (2 * w + e) * 1024 + 8192);
          else glds16s(wd + kt * 128 * D_ + 64 * D_, woffs[e], sb + 16384 + (2 * w + e) * 1024 + 8192);
        }
    }
    raw_barrier();
    char* red = smem + K2_TAB + cI * 24576;
    f32x4 own[2];
    static_for<0, 4>([&](auto Q) {
      constexpr int qq = Q.value;
      if (qd == qq) {
#pragma unroll
        for (int n = 0; n < 8; ++n) {
          const int o = n >> 1;
          if (o != qq) *(f32x4*)(red + (n * 3 + (qq < o ? qq : qq - 1)) * 1024 + lane * 16) = cacc[n];
        }
        own[0] = cacc[2 * qq];
        own[1] = cacc[2 * qq + 1];
      }
    });
    raw_barrier();
#pragma unroll
    for (int i = 0; i < 2; ++i) {
      const int n = 2 * qd + i;
      f32x4 y = own[i];
#pragma unroll
      for (int k = 0; k < 3; ++k) y += *(const f32x4*)(red + (n * 3 + k) * 1024 + lane * 16);
      const int s = 256 * n + 16 * r + 4 * g;
      const u32x2 uu = *(const u32x2*)(cq + 2 * (2044 - s));
      const float t0 = fmaf(bf_hi(uu[1]), dch, y[0]);
      const float t1 = fmaf(bf_lo(uu[1]), dch, y[1]);
      const float t2 = fmaf(bf_hi(uu[0]), dch, y[2]);
      const float t3 = fmaf(bf_lo(uu[0]), dch, y[3]);
      const u32x2 o = {pk_bf16(t0, t1), pk_bf16(t2, t3)};
      asm volatile("global_store_dwordx2 %0, %1, off sc1\n\ts_nop 1" ::"v"(tT + (size_t)ch * S_ + s), "v"(o) : "memory");
    }
    raw_barrier();
  }
  STAMP(14);

  const int erow = row0 + 32 * wm + 16 * kg + r;
  const int edoff = d0 + 32 * wn + 16 * (g & 1) + 8 * (g >> 1);

  int aoff[2][2], boff[2];
#pragma unroll
  for (int i = 0; i < 2; ++i)
#pragma unroll
    for (int hs = 0; hs < 2; ++hs)
      aoff[i][hs] = (32 * wm + 16 * i + r) * 256 + (((4 * (kg + 2 * hs) + g) ^ r) << 4);
  const int phiL = ((q >> 1) & 1) | ((g & 1) << 1);
#pragma unroll
  for (int T = 0; T < 2; ++T)
    boff[T] = 16384 + (32 * kg + 8 * g + q) * 128 + (((2 * wn + T) ^ phiL) << 5) + p * 8;

  f32x4 acc[2][2];
#pragma unroll
  for (int T = 0; T < 2; ++T)
#pragma unroll
    for (int i = 0; i < 2; ++i) acc[T][i] = f32x4{0.f, 0.f, 0.f, 0.f};

  struct Frags2 { bf16x8 hf[2][2]; s16x4 wl[2][2], wh[2][2]; };
  Frags2 fs[2];
  auto read_piece = [&](Frags2& f, int kt, int n) {
    const char* sb = smem + k2_buf(kt);
    const int hs = n / 6, k = n % 6;
    if (k < 2) f.hf[hs][k] = *(const bf16x8*)(sb + aoff[k][hs]);
    else if ((k & 1) == 0) f.wl[hs][(k - 2) >> 1] = tr_read(sb + boff[(k - 2) >> 1] + hs * 8192);
    else f.wh[hs][(k - 2) >> 1] = tr_read(sb + boff[(k - 2) >> 1] + hs * 8192 + 512);
  };
  auto mma_one = [&](const Frags2& f, int m) {
    const int hs = m >> 2, T = (m >> 1) & 1, i = m & 1;
    acc[T][i] = mfma16(cat8(f.wl[hs][T], f.wh[hs][T]), f.hf[hs][i], acc[T][i]);
  };
  u32x4 tval;
  unsigned cval;
  gu32* const cptr = (gu32*)cnt + (idx & 7) * 64;
  static_for<0, K2_NT + 1>([&](auto KT) {
    constexpr int kt = KT.value;
    if constexpr (kt < K2_NT) {
      if constexpr (kt == 0) wait_vm<18>();
      else if constexpr (kt == 1) wait_vm<14>();
      else if constexpr (kt <= 7) wait_vm<8>();
      else if constexpr (kt == 8) wait_vm<9>();
      else if constexpr (kt == 9) wait_vm<6>();
      else wait_vm<1>();
      raw_barrier();
      STAMP(1 + kt);
      if constexpr (kt == 2)
        if (tid == 0) __hip_atomic_fetch_add((gu32*)cnt + (b >> 5) * 64, 1u, __ATOMIC_RELAXED, __HIP_MEMORY_SCOPE_AGENT);
    }
    if constexpr (kt == 9) {
      asm volatile("" : "+v"(cval));
      if (__builtin_amdgcn_readfirstlane(cval) < 32u) {
        for (int spin = 0; spin < (1 << 16); ++spin) {
          if (__hip_atomic_load(cptr, __ATOMIC_RELAXED, __HIP_MEMORY_SCOPE_AGENT) >= 32u) break;
          __builtin_amdgcn_s_sleep(2);
        }
      }
      __builtin_amdgcn_fence(__ATOMIC_ACQUIRE, "wavefront");
      asm volatile("global_load_dwordx4 %0, %1, off sc1" : "=v"(tval)
                   : "v"(tT + (size_t)(d0 + 8 * w + (lane >> 3)) * S_ + row0 + (lane & 7) * 8) : "memory");
    }
    SB;
#pragma unroll
    for (int n = 0; n < 12; ++n) {
      if (kt < K2_NT) read_piece(fs[kt & 1], kt, n);
      if (kt > 0 && n < 8) mma_one(fs[(kt - 1) & 1], n);
      if (kt + 3 < K2_NT && (n % 3) == 2) dma_piece(kt + 3, n / 3);
      if (kt == 7 && n == 0) asm volatile("global_load_dword %0, %1, off sc1" : "=v"(cval) : "v"(cptr) : "memory");
      if (kt == 8 && n == 5) glds16(x + (size_t)(row0 + 8 * w + (lane >> 3)) * D_ + d0 + (lane & 7) * 8, smem + K2_X + w * 1024);
      if (kt == 8 && n == 8) glds16(stats + row0 + 2 * (lane & 31), smem + K2_ST);
      SB;
    }
  });
  STAMP(12);
  asm volatile("s_waitcnt vmcnt(0)" : "+v"(tval)::"memory");
  *(u32x4*)(smem + K2_TT + tid * 16) = tval;
  {
    f32x4* xb = (f32x4*)smem;
    const int wq = w & 3;
#pragma unroll
    for (int T = 0; T < 2; ++T) xb[((kg * 4 + wq) * 2 + T) * 64 + lane] = kg ? acc[T][0] : acc[T][1];
    raw_barrier();
    const int lrow = 32 * wm + 16 * kg + r;
    const u32x4 xv = *(const u32x4*)(smem + K2_X + lrow * 128 + (32 * wn + 16 * (g & 1) + 8 * (g >> 1)) * 2);
    const f32x2 stv = *(const f32x2*)(smem + K2_ST + lrow * 8);
    f32x4 fin[2];
#pragma unroll
    for (int T = 0; T < 2; ++T) {
      const f32x4 o = xb[(((kg ^ 1) * 4 + wq) * 2 + T) * 64 + lane];
      fin[T] = (kg ? acc[T][1] : acc[T][0]) + o;
    }
    unsigned pb[2][2], pt[2][2];
#pragma unroll
    for (int T = 0; T < 2; ++T) {
      pb[T][0] = pk_bf16(fin[T][0], fin[T][1]);
      pb[T][1] = pk_bf16(fin[T][2], fin[T][3]);
      const s16x4 tv = tr_read(smem + K2_TT + (32 * wn + 16 * T + 4 * g + q) * 128 + (32 * wm + 16 * kg + 4 * p) * 2);
      const u32x2 tu = __builtin_bit_cast(u32x2, tv);
      pt[T][0] = tu[0];
      pt[T][1] = tu[1];
    }
    auto b0 = __builtin_amdgcn_permlane16_swap(pb[0][0], pb[1][0], false, false);
    auto b1 = __builtin_amdgcn_permlane16_swap(pb[0][1], pb[1][1], false, false);
    auto t0 = __builtin_amdgcn_permlane16_swap(pt[0][0], pt[1][0], false, false);
    auto t1 = __builtin_amdgcn_permlane16_swap(pt[0][1], pt[1][1], false, false);
    const u32x4 bv = {b0[0], b1[0], b0[1], b1[1]};
    const u32x4 tv = {t0[0], t1[0], t0[1], t1[1]};
    u32x4 o;
#pragma unroll
    for (int j = 0; j < 4; ++j) {
      float res[2];
#pragma unroll
      for (int hh = 0; hh < 2; ++hh) {
        const float xe = hh ? bf_hi(xv[j]) : bf_lo(xv[j]);
        const float te = hh ? bf_hi(tv[j]) : bf_lo(tv[j]);
        const float be = hh ? bf_hi(bv[j]) : bf_lo(bv[j]);
        const float xn = round_bf(round_bf(xe - stv[0]) * stv[1]);
        const float a = round_bf(xn * te);
        res[hh] = round_bf(xe + a) + be;
      }
      o[j] = pk_bf16(res[0], res[1]);
    }
    *(u32x4*)(out + (size_t)erow * D_ + edoff) = o;
  }
  STAMP(15);
  STAMP_FLUSH(1);
}

extern "C" void kernel_launch(void* const* d_in, const int* in_sizes, int n_in, void* d_out, int out_size,
                              void* d_ws, size_t ws_size, hipStream_t stream) {
  const u16* q = (const u16*)d_in[0];
  const u16* k = (const u16*)d_in[1];
  const u16* v = (const u16*)d_in[2];
  const u16* shk = (const u16*)d_in[3];
  const u16* ssmk = (const u16*)d_in[4];
  const u16* ssmd = (const u16*)d_in[5];
  const u16* wg = (const u16*)d_in[7];
  const u16* wu = (const u16*)d_in[8];
  const u16* wd = (const u16*)d_in[9];
  char* ws = (char*)d_ws;
  u16* hbuf = (u16*)(ws + WS_H);
  u16* tT = (u16*)(ws + WS_T);
  u16* ur = (u16*)(ws + WS_U);
  float2* stats = (float2*)(ws + WS_ST);
  unsigned* cnt = (unsigned*)(ws + WS_CNT);
  k1_kernel<<<256, 512, 0, stream>>>(q, k, v, shk, ssmk, wg, wu, wd, hbuf, ur, stats, cnt);
  k2_kernel<<<256, 512, 0, stream>>>(q, hbuf, wd, ur, ssmk, ssmd, tT, stats, cnt, (u16*)d_out);
}
```

```cpp
#include <hip/hip_runtime.h>
#include <hip/hip_bf16.h>
#include <stdint.h>

typedef __attribute__((ext_vector_type(8))) short bf16x8;
typedef __attribute__((ext_vector_type(4))) short s16x4;
typedef __attribute__((ext_vector_type(2))) __bf16 bf16x2;
typedef __attribute__((ext_vector_type(4))) float f32x4;
typedef __attribute__((ext_vector_type(2))) float f32x2;
typedef __attribute__((ext_vector_type(4))) unsigned u32x4;
typedef __attribute__((ext_vector_type(2))) unsigned u32x2;
typedef __attribute__((address_space(3))) s16x4* lds_s16x4_ptr;
typedef unsigned short u16;

#define DI __device__ __forceinline__

constexpr int S_ = 2048, D_ = 512, F_ = 1376, FP_ = 1408;

constexpr size_t WS_H = 0;
constexpr size_t WS_T = 6u * 1024 * 1024;
constexpr size_t WS_U = 8u * 1024 * 1024;
constexpr size_t WS_ST = 10u * 1024 * 1024;
constexpr size_t WS_CNT = 10u * 1024 * 1024 + 65536;

template <int N> DI void wait_vm() { asm volatile("s_waitcnt vmcnt(%0)" ::"i"(N) : "memory"); }
DI void raw_barrier() {
  asm volatile("s_waitcnt lgkmcnt(0)" ::: "memory");
  __builtin_amdgcn_s_barrier();
  asm volatile("" ::: "memory");
}

DI unsigned pk_bf16(float lo, float hi) {
  f32x2 f = {lo, hi};
  return __builtin_bit_cast(unsigned, __builtin_convertvector(f, bf16x2));
}
DI float bf_lo(unsigned v) { return __uint_as_float(v << 16); }
DI float bf_hi(unsigned v) { return __uint_as_float(v & 0xffff0000u); }
DI float round_bf(float x) { return bf_lo(pk_bf16(x, x)); }
DI s16x4 tr_read(const char* p) {
  return __builtin_amdgcn_ds_read_tr16_b64_v4i16((lds_s16x4_ptr)p);
}
DI bf16x8 cat8(s16x4 lo, s16x4 hi) { return __builtin_shufflevector(lo, hi, 0, 1, 2, 3, 4, 5, 6, 7); }
DI f32x4 mfma16(bf16x8 a, bf16x8 b, f32x4 c) { return __builtin_amdgcn_mfma_f32_16x16x32_bf16(a, b, c, 0, 0, 0); }

DI void glds16(const void* g, char* lds) {
  unsigned keep;
  const unsigned dst = __builtin_amdgcn_readfirstlane((unsigned)(size_t)lds);
  asm volatile("s_mov_b32 %0, m0\n\ts_mov_b32 m0, %2\n\ts_nop 0\n\tglobal_load_lds_dwordx4 %1, off\n\ts_mov_b32 m0, %0"
               : "=&s"(keep) : "v"(g), "s"(dst) : "memory");
}
DI void glds16s(const void* sbase, unsigned voff, char* lds) {
  unsigned keep;
  const unsigned dst = __builtin_amdgcn_readfirstlane((unsigned)(size_t)lds);
  asm volatile("s_mov_b32 %0, m0\n\ts_mov_b32 m0, %3\n\ts_nop 0\n\tglobal_load_lds_dwordx4 %1, %2\n\ts_mov_b32 m0, %0"
               : "=&s"(keep) : "v"(voff), "s"(sbase), "s"(dst) : "memory");
}
DI void touch(const void* g, char* dummy) {
  unsigned keep;
  const unsigned dst = __builtin_amdgcn_readfirstlane((unsigned)(size_t)dummy);
  asm volatile("s_mov_b32 %0, m0\n\ts_mov_b32 m0, %2\n\ts_nop 0\n\tglobal_load_lds_dword %1, off\n\ts_mov_b32 m0, %0"
               : "=&s"(keep) : "v"(g), "s"(dst) : "memory");
}

DI void store16_wt(void* p, u32x4 v) {
  asm volatile("global_store_dwordx4 %0, %1, off sc1\n\ts_nop 1" ::"v"(p), "v"(v) : "memory");
}
DI void store8_wt(void* p, u32x2 v) {
  asm volatile("global_store_dwordx2 %0, %1, off sc1\n\ts_nop 1" ::"v"(p), "v"(v) : "memory");
}


template <int I, int N, class Fn> DI void static_for(Fn&& fn) {
  if constexpr (I < N) {
    fn(std::integral_constant<int, I>{});
    static_for<I + 1, N>(fn);
  }
}

constexpr int K1_DUMMY = 147456;
constexpr int K1_LDS = K1_DUMMY + 256;

constexpr int G1_A = 0;
constexpr int G1_W = 49152;
#define SB __builtin_amdgcn_sched_barrier(0)

DI void gemm1_block(char* smem, const u16* __restrict__ x, const u16* __restrict__ wg, const u16* __restrict__ wu,
                    u16* __restrict__ hbuf, int mt, int ft) {
  const int tid = threadIdx.x, lane = tid & 63;
  const int w = __builtin_amdgcn_readfirstlane(tid >> 6);
  const int wm = w >> 2, wn = w & 3;
  const int r = lane & 15, g = lane >> 4, q = (lane & 15) >> 2, p = lane & 3;
  const int row0 = mt * 128, f0 = ft * 128;

  unsigned aoffs[2], woffs[2];
#pragma unroll
  for (int e = 0; e < 2; ++e) {
    const int i = 2 * w + e;
    const int row = 8 * i + (lane >> 3);
    aoffs[e] = (unsigned)((row0 + row) * D_ + (((lane & 7) ^ (row & 7)) << 3)) * 2u;
    const int kr = 4 * i + (lane >> 4);
    const int phi = (kr & 3) | (((kr >> 3) & 1) << 2);
    const int sc = ((((lane & 15) >> 1) ^ phi) << 1) | (lane & 1);
    int col = f0 + sc * 8;
    col = col > F_ - 8 ? F_ - 8 : col;
    woffs[e] = (unsigned)(kr * F_ + col) * 2u;
  }
  auto dma_piece = [&](int kt, int n) {
    const int b = kt % 3, e = n & 1;
    if (n < 2) glds16s(x + kt * 64, aoffs[e], smem + G1_A + b * 16384 + (2 * w + e) * 1024);
    else if (n < 4) glds16s(wg + kt * 64 * F_, woffs[e], smem + G1_W + b * 32768 + (2 * w + e) * 1024);
    else glds16s(wu + kt * 64 * F_, woffs[e], smem + G1_W + b * 32768 + 16384 + (2 * w + e) * 1024);
  };
#pragma unroll
  for (int kt = 0; kt < 3; ++kt) {
#pragma unroll
    for (int n = 0; n < 6; ++n) dma_piece(kt, n);
  }

  int aoff[4][2];
#pragma unroll
  for (int i = 0; i < 4; ++i)
#pragma unroll
    for (int ks = 0; ks < 2; ++ks)
      aoff[i][ks] = G1_A + (64 * wm + 16 * (i ^ wn) + r) * 128 + (((4 * ks + g) ^ (r & 7)) << 4);
  const int phiL = q | ((g & 1) << 2);
  int boff[4];
#pragma unroll
  for (int T = 0; T < 4; ++T) {
    const int mat = (T >> 1) ^ wm;
    boff[T] = G1_W + mat * 16384 + (8 * g + q) * 256 + (((2 * wn + (T & 1)) ^ phiL) << 5) + p * 8;
  }

  f32x4 acc[4][4];
#pragma unroll
  for (int T = 0; T < 4; ++T)
#pragma unroll
    for (int i = 0; i < 4; ++i) acc[T][i] = f32x4{0.f, 0.f, 0.f, 0.f};
  f32x4 accR = {0.f, 0.f, 0.f, 0.f}, accC[2] = {accR, accR};
  const bf16x8 ones = {0x3f80, 0x3f80, 0x3f80, 0x3f80, 0x3f80, 0x3f80, 0x3f80, 0x3f80};

  struct Frags { s16x4 wl[4], wh[4]; bf16x8 xf[4]; };
  Frags fa, fb2;
  auto read_piece = [&](Frags& f, int kt, int ks, int n) {
    const int b = kt % 3;
    if (n < 4) f.xf[n] = *(const bf16x8*)(smem + b * 16384 + aoff[n][ks]);
    else {
      const int T = (n - 4) >> 1;
      if ((n & 1) == 0) f.wl[T] = tr_read(smem + b * 32768 + ks * 8192 + boff[T]);
      else f.wh[T] = tr_read(smem + b * 32768 + ks * 8192 + 1024 + boff[T]);
    }
  };
  auto mma_one = [&](const Frags& f, int m) {
    const int T = m / 5, i = m % 5;
    if (i < 4) acc[T][i] = mfma16(cat8(f.wl[T], f.wh[T]), f.xf[i], acc[T][i]);
    else if (T == 0) {}
    else if (T == 1) accR = mfma16(ones, f.xf[0], accR);
    else if (T == 2) accC[0] = mfma16(cat8(f.wl[0], f.wh[0]), ones, accC[0]);
    else accC[1] = mfma16(cat8(f.wl[1], f.wh[1]), ones, accC[1]);
  };

  wait_vm<12>();
  raw_barrier();
#pragma unroll
  for (int n = 0; n < 12; ++n) read_piece(fa, 0, 0, n);
  static_for<0, 8>([&](auto KT) {
    constexpr int kt = KT.value;
    SB;
#pragma unroll
    for (int m = 0; m < 16; ++m) {
      mma_one(fa, m);
      if (m < 12) read_piece(fb2, kt, 1, m);
      SB;
    }
    if constexpr (kt < 7) {
      if constexpr (kt < 6) wait_vm<6>(); else wait_vm<0>();
      raw_barrier();
      SB;
    }
#pragma unroll
    for (int m = 16; m < 20; ++m) {
      mma_one(fa, m);
      if (m == 17 && kt < 5) dma_piece(kt + 3, 0);
      SB;
    }
    if constexpr (kt < 7) {
#pragma unroll
      for (int m = 0; m < 20; ++m) {
        mma_one(fb2, m);
        if (m < 12) read_piece(fa, kt + 1, 0, m);
        if ((m == 1 || m == 5 || m == 9 || m == 13 || m == 17) && kt < 5) dma_piece(kt + 3, 1 + (m >> 2));
        SB;
      }
    }
  });
#pragma unroll
  for (int T = 0; T < 4; ++T) mma_one(fb2, 5 * T + 4);
#pragma unroll
  for (int m = 0; m < 4; ++m) mma_one(fb2, m);
  SB;
  float* st_s = (float*)smem;
  float* cs_s = (float*)(smem + 1024);
  if (g == 0) st_s[64 * wm + 16 * wn + r] = -round_bf(accR[0] * (1.f / D_));
  if (r == 0) {
    *(f32x4*)(cs_s + wm * 128 + 32 * wn + 4 * g) = accC[0];
    *(f32x4*)(cs_s + wm * 128 + 32 * wn + 16 + 4 * g) = accC[1];
  }
#pragma unroll
  for (int m = 5; m < 20; ++m)
    if (m % 5 != 4) mma_one(fb2, m);
  raw_barrier();
  const bool valid = (f0 + 32 * wn) < F_;
  f32x4 ca[2], cb[2];
#pragma unroll
  for (int T = 0; T < 2; ++T) {
    ca[T] = accC[T];
    cb[T] = *(const f32x4*)(cs_s + (wm ^ 1) * 128 + 32 * wn + 16 * T + 4 * g);
  }
  float sta[4];
#pragma unroll
  for (int i = 0; i < 4; ++i) sta[i] = st_s[64 * wm + 16 * (i ^ wn) + r];
#pragma unroll
  for (int i = 0; i < 4; ++i) {
    const int row = 64 * wm + 16 * (i ^ wn) + r;
    const float nm = sta[i];
    unsigned pk[2][2];
#pragma unroll
    for (int T = 0; T < 2; ++T) {
      const f32x4 ga = acc[T][i] + nm * ca[T];
      const f32x4 gb = acc[2 + T][i] + nm * cb[T];
      const f32x4 hv = ga * gb;
      pk[T][0] = pk_bf16(hv[0], hv[1]);
      pk[T][1] = pk_bf16(hv[2], hv[3]);
    }
    auto r0 = __builtin_amdgcn_permlane16_swap(pk[0][0], pk[1][0], false, false);
    auto r1 = __builtin_amdgcn_permlane16_swap(pk[0][1], pk[1][1], false, false);
    u32x4 o = {r0[0], r1[0], r0[1], r1[1]};
    if (!valid) o = u32x4{0u, 0u, 0u, 0u};
    store16_wt(hbuf + (size_t)(row0 + row) * FP_ + f0 + 32 * wn + 16 * (g & 1) + 8 * (g >> 1), o);
  }
}

constexpr int PU_PITCH = 520;
DI void prep_block(char* smem, const u16* __restrict__ kin, const u16* __restrict__ vin,
                   const u16* __restrict__ shk, u16* __restrict__ ur, int stripe, int rb) {
  const int tid = threadIdx.x;
  const int rg = tid >> 3, cc = tid & 7;
  const int c0 = 64 * stripe + 8 * cc, s0 = 256 * rb + 4 * rg;
  u32x4 kx[7], vx[4];
#pragma unroll
  for (int i = 0; i < 7; ++i) {
    const int sr = s0 - 3 + i;
    kx[i] = *(const u32x4*)(kin + (size_t)(sr < 0 ? 0 : sr) * D_ + c0);
    if (i < 3 && sr < 0) kx[i] = u32x4{0u, 0u, 0u, 0u};
  }
#pragma unroll
  for (int i = 0; i < 4; ++i) vx[i] = *(const u32x4*)(vin + (size_t)(s0 + i) * D_ + c0);
  u32x4 wv[4];
#pragma unroll
  for (int i = 0; i < 4; ++i) wv[i] = *(const u32x4*)(shk + c0 * 4 + 8 * i);
#pragma unroll
  for (int d = 0; d < 4; ++d) {
#pragma unroll
    for (int hh = 0; hh < 2; ++hh) {
      const float w0 = bf_lo(wv[d][2 * hh]), w1 = bf_hi(wv[d][2 * hh]);
      const float w2 = bf_lo(wv[d][2 * hh + 1]), w3 = bf_hi(wv[d][2 * hh + 1]);
      float kf[7], u[4];
#pragma unroll
      for (int i = 0; i < 7; ++i) kf[i] = hh ? bf_hi(kx[i][d]) : bf_lo(kx[i][d]);
#pragma unroll
      for (int i = 0; i < 4; ++i) {
        float a = kf[i] * w3;
        a = fmaf(kf[i + 1], w2, a);
        a = fmaf(kf[i + 2], w1, a);
        a = fmaf(kf[i + 3], w0, a);
        u[i] = a * (hh ? bf_hi(vx[i][d]) : bf_lo(vx[i][d]));
      }
      const u32x2 o = {pk_bf16(u[3], u[2]), pk_bf16(u[1], u[0])};
      *(u32x2*)(smem + (8 * cc + 2 * d + hh) * PU_PITCH + (252 - 4 * rg) * 2) = o;
    }
  }
  __syncthreads();
#pragma unroll
  for (int i = 0; i < 8; ++i) {
    const int e = i * 512 + tid, ch = e >> 6, pos = e & 63;
    const u32x2 v = *(const u32x2*)(smem + ch * PU_PITCH + pos * 8);
    store8_wt(ur + (size_t)(64 * stripe + ch) * S_ + (S_ - 256 * (rb + 1)) + pos * 4, v);
  }
}

__global__ __launch_bounds__(512, 2) void k1_kernel(const u16* __restrict__ x, const u16* __restrict__ kin,
                                                     const u16* __restrict__ vin, const u16* __restrict__ shk,
                                                     const u16* __restrict__ ssmk,
                                                     const u16* __restrict__ wg, const u16* __restrict__ wu,
                                                     const u16* __restrict__ wd,
                                                     u16* __restrict__ hbuf, u16* __restrict__ ur,
                                                     float2* __restrict__ stats, unsigned* __restrict__ cnt) {
  __shared__ __attribute__((aligned(16))) char smem[K1_LDS];
  const int b = blockIdx.x, xcd = b & 7, idx = b >> 3;
  asm volatile("" ::"s"(x), "s"(kin), "s"(vin), "s"(shk), "s"(ssmk), "s"(wg), "s"(wu), "s"(wd), "s"(hbuf), "s"(ur),
               "s"(stats), "s"(cnt));
  if (idx < 8 || idx >= 30) {
    const int hi = idx < 8 ? idx : idx - 22, tid = threadIdx.x;
    const int q4 = xcd & 3, clo = 128 * ((22 * q4) >> 3);
    int chi = 128 * (((22 * q4 + 21) >> 3) + 1) - 2;
    chi = chi > F_ - 2 ? F_ - 2 : chi;
    const int e = hi * 256 + (tid & 255), e2 = e - 1024;
    int col = clo + 64 * (e2 % 9);
    col = col > chi ? chi : col;
    const int krl = e2 / 18 > 63 ? 63 : e2 / 18;
    const u16* pb = e < 1024 ? x + (size_t)(1024 * (xcd >> 2) + e) * D_ : (((e2 % 18) / 9) ? wu : wg) + krl * F_ + col;
    const int step = e < 1024 ? 64 : 64 * F_;
    for (int st = 3 + (tid >> 8); st < 8; st += 2) touch(pb + st * step, smem + K1_DUMMY);
  }
  if (idx < 8) {
    __builtin_amdgcn_s_sleep(40);
    prep_block(smem, kin, vin, shk, ur, xcd, idx);
    for (int i = threadIdx.x; i < 1376; i += 512) touch(wd + (size_t)(idx * 1376 + i) * 64, smem + K1_DUMMY);
    wait_vm<0>();
  } else if (idx < 30) {
    const int t = 22 * (xcd & 3) + (idx - 8);
    gemm1_block(smem, x, wg, wu, hbuf, 8 * (xcd >> 2) + (t & 7), t >> 3);
  } else {
    const int part = (idx - 30) * 8 + xcd;
    if (part == 0 && threadIdx.x < 8) cnt[threadIdx.x * 64] = 0u;
    {
      const int lane = threadIdx.x & 63, row = part * 128 + (threadIdx.x >> 6) * 16 + (lane & 15);
      u32x4 xr[16];
#pragma unroll
      for (int i = 0; i < 16; ++i) xr[i] = *(const u32x4*)(x + (size_t)row * D_ + i * 32 + (lane >> 4) * 8);
      float s1 = 0.f;
#pragma unroll
      for (int i = 0; i < 16; ++i)
#pragma unroll
        for (int j = 0; j < 4; ++j) s1 += bf_lo(xr[i][j]) + bf_hi(xr[i][j]);
      s1 += __shfl_xor(s1, 16);
      s1 += __shfl_xor(s1, 32);
      const float mean = s1 * (1.f / D_);
      float s2 = 0.f;
#pragma unroll
      for (int i = 0; i < 16; ++i)
#pragma unroll
        for (int j = 0; j < 4; ++j) {
          const float a = bf_lo(xr[i][j]) - mean, c = bf_hi(xr[i][j]) - mean;
          s2 = fmaf(a, a, fmaf(c, c, s2));
        }
      s2 += __shfl_xor(s2, 16);
      s2 += __shfl_xor(s2, 32);
      const float vb = round_bf(s2 * (1.f / D_));
      const float sd = round_bf(sqrtf(round_bf(vb + 1e-5f)));
      if (lane < 16) stats[row] = make_float2(round_bf(mean), 1.f / sd);
    }
    for (int i = threadIdx.x; i < 1024; i += 512) {
      const int l = (idx - 30) * 1024 + i;
      touch(ssmk + (size_t)(2 * (8 * (l >> 6) + xcd)) * S_ + (l & 63) * 64, smem + K1_DUMMY);
    }
    wait_vm<0>();
  }
}

constexpr int K2_STAGE = 32768;
constexpr int K2_NT = FP_ / 128;
constexpr int CQ_PITCH = 4576;
constexpr int CT_PITCH = 16512;
constexpr int K2_Q = 3 * K2_STAGE;
constexpr int K2_TAB = K2_Q + 2 * CQ_PITCH;
constexpr int K2_B3 = K2_TAB;
constexpr int K2_TT = K2_B3 + K2_STAGE;
constexpr int K2_X = K2_TT + 8192;
constexpr int K2_ST = K2_Q;
constexpr int K2_DUMMY = K2_X + 8192;
constexpr int K2_LDS = K2_DUMMY + 256;
DI constexpr int k2_buf(int kt) { return (kt & 3) < 3 ? (kt & 3) * K2_STAGE : K2_B3; }

typedef __attribute__((address_space(1))) unsigned gu32;

constexpr int conv_pair(int m) {
  int c = 0;
  for (int t = 0; t < 8; ++t)
    for (int o = 0; o <= t; ++o)
      if (t + o < 8) {
        if (c++ == m) return t * 8 + o;
        if (o < t && c++ == m) return o * 8 + t;
      }
  return 0;
}

__global__ __launch_bounds__(512, 2) void k2_kernel(const u16* __restrict__ x, const u16* __restrict__ hbuf,
                                                     const u16* __restrict__ wd, const u16* __restrict__ ur,
                                                     const u16* __restrict__ ssmk, const u16* __restrict__ ssmd,
                                                     u16* tT, const float2* __restrict__ stats,
                                                     unsigned* cnt, u16* __restrict__ out) {
  __shared__ __attribute__((aligned(16))) char smem[K2_LDS];
  const int tid = threadIdx.x, lane = tid & 63;
  const int w = __builtin_amdgcn_readfirstlane(tid >> 6);
  const int kg = w >> 2, wm = (w >> 1) & 1, wn = w & 1;
  const int r = lane & 15, g = lane >> 4, q = (lane & 15) >> 2, p = lane & 3;
  const int b = blockIdx.x, xcd = b & 7, idx = b >> 3;
  const int row0 = (4 * xcd + (idx >> 3)) * 64, d0 = (idx & 7) * 64;
  asm volatile("" ::"s"(x), "s"(hbuf), "s"(wd), "s"(ur), "s"(ssmk), "s"(ssmd), "s"(tT), "s"(stats), "s"(cnt), "s"(out));

  const int cI = w >> 2, qd = w & 3;
  const int ch = 2 * b + cI;
  char* cq = smem + K2_Q + cI * CQ_PITCH;
  char* ct = smem + K2_TAB + cI * CT_PITCH;
  glds16(ur + (size_t)ch * S_ + (qd * 64 + lane) * 8, cq + qd * 1024);
  typedef __attribute__((ext_vector_type(3))) unsigned u32x3;
  unsigned he[2][3], dbits;
  u32x3 hm[3];
  const int mb = qd * 64 + lane - 8;
  {
    const unsigned* hrow = (const unsigned*)(ssmk + (size_t)ch * S_);
#pragma unroll
    for (int k = 0; k < 3; ++k) {
      const int m0 = mb + k;
      asm volatile("global_load_dword %0, %1, off" : "=v"(he[0][k]) : "v"(hrow + (m0 < 0 ? 0 : m0)) : "memory");
    }
#pragma unroll
    for (int it = 1; it < 4; ++it)
      asm volatile("global_load_dwordx3 %0, %1, off" : "=v"(hm[it - 1]) : "v"(hrow + it * 256 + mb) : "memory");
#pragma unroll
    for (int k = 0; k < 3; ++k) {
      const int m4 = 1024 + mb + k;
      asm volatile("global_load_dword %0, %1, off" : "=v"(he[1][k]) : "v"(hrow + (m4 > 1023 ? 1023 : m4)) : "memory");
    }
    asm volatile("global_load_ushort %0, %1, off" : "=v"(dbits) : "v"(ssmd + ch) : "memory");
  }

  unsigned aoffs[2], woffs[2], woffl[2];
#pragma unroll
  for (int e = 0; e < 2; ++e) {
    const int ar = 8 * w + 4 * e + (lane >> 4);
    aoffs[e] = (unsigned)((row0 + ar) * FP_ + (((lane & 15) ^ (ar & 15)) << 3)) * 2u;
    const int kr = 16 * w + 8 * e + (lane >> 3);
    const int phi_s = ((kr >> 1) & 1) | (((kr >> 3) & 1) << 1);
    const int wcol = d0 + ((((((lane & 7) >> 1) ^ phi_s) << 1) | (lane & 1)) << 3);
    woffs[e] = (unsigned)(kr * D_ + wcol) * 2u;
    const int krl = kr > F_ - 1 - 1280 ? F_ - 1 - 1280 : kr;
    woffl[e] = (unsigned)(krl * D_ + wcol) * 2u;
  }
  auto dma_piece = [&](int kt, int n) {
    char* sb = smem + k2_buf(kt);
    const int e = n & 1;
    if (n < 2) glds16s(hbuf + kt * 128, aoffs[e], sb + (2 * w + e) * 1024);
    else glds16s(wd + kt * 128 * D_, kt == K2_NT - 1 ? woffl[e] : woffs[e], sb + 16384 + (2 * w + e) * 1024);
  };
  const u16* ptouch;
  {
    const int hr = tid / 22, hl = tid % 22;
    int kr = 344 * (idx >> 3) + (tid - 176);
    kr = kr > F_ - 1 ? F_ - 1 : kr;
    const u16* ph = hbuf + (size_t)(row0 + 8 * (idx & 7) + (hr & 7)) * FP_ + hl * 64;
    const u16* pw = wd + (size_t)kr * D_ + d0;
    ptouch = tid < 176 ? ph : pw;
  }
  __builtin_amdgcn_s_barrier();
  touch(ptouch, smem + K2_DUMMY);

  if (lane < 30 && qd == 0) *(u32x4*)(cq + 4096 + lane * 16) = u32x4{0u, 0u, 0u, 0u};
  asm volatile("s_waitcnt vmcnt(1)"
               : "+v"(he[0][0]), "+v"(he[0][1]), "+v"(he[0][2]), "+v"(he[1][0]), "+v"(he[1][1]), "+v"(he[1][2]),
                 "+v"(hm[0]), "+v"(hm[1]), "+v"(hm[2]), "+v"(dbits)::"memory");
  const float dch = __uint_as_float(dbits << 16);
#pragma unroll
  for (int it = 0; it < 5; ++it) {
    const int i = it * 256 + qd * 64 + lane;
    unsigned d[3];
#pragma unroll
    for (int k = 0; k < 3; ++k) {
      if (it == 0) d[k] = mb + k < 0 ? 0u : he[0][k];
      else if (it == 4) d[k] = 1024 + mb + k > 1023 ? 0u : he[1][k];
      else d[k] = hm[it - 1][k];
    }
    u32x4 e;
    e[0] = d[0];
    e[1] = d[1];
    e[2] = __builtin_amdgcn_alignbit(d[1], d[0], 16);
    e[3] = __builtin_amdgcn_alignbit(d[2], d[1], 16);
    if (i < 1032) *(u32x4*)(ct + i * 16) = e;
  }
  raw_barrier();
  char* const red = smem + K2_TAB + cI * 24576;
  f32x4 own[2];
  {
    f32x4 cacc[8];
#pragma unroll
    for (int n = 0; n < 8; ++n) cacc[n] = f32x4{0.f, 0.f, 0.f, 0.f};
    const char* a_lane = ct + (64 * qd + 1 + r + 8 * g) * 8;
    const char* b_lane = cq + 4064 + 128 * qd - 32 * r + 16 * g;
    s16x4 al[2][8], ah[2][8];
    bf16x8 bq[2][8];
    auto rd = [&](int kk, int n) {
      const int t = n / 3, k = n % 3;
      if (k == 0) al[kk][t] = *(const s16x4*)(a_lane + kk * 256 + t * 2048);
      else if (k == 1) ah[kk][t] = *(const s16x4*)(a_lane + kk * 256 + t * 2048 + 32);
      else bq[kk][t] = *(const bf16x8*)(b_lane + kk * 64 - t * 512);
    };
#pragma unroll
    for (int n = 0; n < 24; ++n) rd(0, n);
    SB;
    auto side = [&](int sl) {
      if (sl < 24) rd(1, sl);
      SB;
    };
    static_for<0, 72>([&](auto SL) {
      constexpr int sl = SL.value, kk = sl / 36;
      constexpr int ai = conv_pair(sl % 36) >> 3, bi = conv_pair(sl % 36) & 7;
      cacc[ai + bi] = mfma16(cat8(al[kk][ai], ah[kk][ai]), bq[kk][bi], cacc[ai + bi]);
      side(sl);
    });
    if (kg == 0) {
#pragma unroll
      for (int kt = 0; kt < 3; ++kt)
#pragma unroll
        for (int n = 0; n < 4; ++n) {
          dma_piece(kt, n);
          char* sb = smem + k2_buf(kt);
          const int e = n & 1;
          if (n < 2) glds16s(hbuf + kt * 128 + 32 * FP_, aoffs[e], sb + (2 * w + e) * 1024 + 8192);
          else glds16s(wd + kt * 128 * D_ + 64 * D_, woffs[e], sb + 16384 + (2 * w + e) * 1024 + 8192);
        }
    }
    raw_barrier();
    static_for<0, 4>([&](auto Q) {
      constexpr int qq = Q.value;
      if (qd == qq) {
#pragma unroll
        for (int n = 0; n < 8; ++n) {
          const int o = n >> 1;
          if (o != qq) *(f32x4*)(red + (n * 3 + (qq < o ? qq : qq - 1)) * 1024 + lane * 16) = cacc[n];
        }
        own[0] = cacc[2 * qq];
        own[1] = cacc[2 * qq + 1];
      }
    });
  }
  auto finish_t = [&]() {
#pragma unroll
    for (int i = 0; i < 2; ++i) {
      const int n = 2 * qd + i;
      f32x4 y = own[i];
#pragma unroll
      for (int k = 0; k < 3; ++k) y += *(const f32x4*)(red + (n * 3 + k) * 1024 + lane * 16);
      const int s = 256 * n + 16 * r + 4 * g;
      const u32x2 uu = *(const u32x2*)(cq + 2 * (2044 - s));
      const float t0 = fmaf(bf_hi(uu[1]), dch, y[0]);
      const float t1 = fmaf(bf_lo(uu[1]), dch, y[1]);
      const float t2 = fmaf(bf_hi(uu[0]), dch, y[2]);
      const float t3 = fmaf(bf_lo(uu[0]), dch, y[3]);
      const u32x2 o = {pk_bf16(t0, t1), pk_bf16(t2, t3)};
      store8_wt(tT + (size_t)ch * S_ + s, o);
    }
  };

  const int erow = row0 + 32 * wm + 16 * kg + r;
  const int edoff = d0 + 32 * wn + 16 * (g & 1) + 8 * (g >> 1);

  int aoff[2][2], boff[2];
#pragma unroll
  for (int i = 0; i < 2; ++i)
#pragma unroll
    for (int hs = 0; hs < 2; ++hs)
      aoff[i][hs] = (32 * wm + 16 * i + r) * 256 + (((4 * (kg + 2 * hs) + g) ^ r) << 4);
  const int phiL = ((q >> 1) & 1) | ((g & 1) << 1);
#pragma unroll
  for (int T = 0; T < 2; ++T)
    boff[T] = 16384 + (32 * kg + 8 * g + q) * 128 + (((2 * wn + T) ^ phiL) << 5) + p * 8;

  f32x4 acc[2][2];
#pragma unroll
  for (int T = 0; T < 2; ++T)
#pragma unroll
    for (int i = 0; i < 2; ++i) acc[T][i] = f32x4{0.f, 0.f, 0.f, 0.f};

  struct Frags2 { bf16x8 hf[2][2]; s16x4 wl[2][2], wh[2][2]; };
  Frags2 fs[2];
  auto read_piece = [&](Frags2& f, int kt, int n) {
    const char* sb = smem + k2_buf(kt);
    const int hs = n / 6, k = n % 6;
    if (k < 2) f.hf[hs][k] = *(const bf16x8*)(sb + aoff[k][hs]);
    else if ((k & 1) == 0) f.wl[hs][(k - 2) >> 1] = tr_read(sb + boff[(k - 2) >> 1] + hs * 8192);
    else f.wh[hs][(k - 2) >> 1] = tr_read(sb + boff[(k - 2) >> 1] + hs * 8192 + 512);
  };
  auto mma_one = [&](const Frags2& f, int m) {
    const int hs = m >> 2, T = (m >> 1) & 1, i = m & 1;
    acc[T][i] = mfma16(cat8(f.wl[hs][T], f.wh[hs][T]), f.hf[hs][i], acc[T][i]);
  };
  u32x4 tval;
  unsigned cval;
  gu32* const cptr = (gu32*)cnt + (idx & 7) * 64;
  static_for<0, K2_NT + 1>([&](auto KT) {
    constexpr int kt = KT.value;
    if constexpr (kt < K2_NT) {
      if constexpr (kt == 0) wait_vm<16>();
      else if constexpr (kt == 1) wait_vm<10>();
      else if constexpr (kt <= 7) wait_vm<8>();
      else if constexpr (kt == 8) wait_vm<9>();
      else if constexpr (kt == 9) wait_vm<6>();
      else wait_vm<1>();
      raw_barrier();
      if constexpr (kt == 2)
        if (tid == 0) __hip_atomic_fetch_add((gu32*)cnt + (b >> 5) * 64, 1u, __ATOMIC_RELAXED, __HIP_MEMORY_SCOPE_AGENT);
    }
    if constexpr (kt == 9) {
      asm volatile("" : "+v"(cval));
      if (__builtin_amdgcn_readfirstlane(cval) < 32u) {
        for (int spin = 0; spin < (1 << 16); ++spin) {
          if (__hip_atomic_load(cptr, __ATOMIC_RELAXED, __HIP_MEMORY_SCOPE_AGENT) >= 32u) break;
          __builtin_amdgcn_s_sleep(2);
        }
      }
      __builtin_amdgcn_fence(__ATOMIC_ACQUIRE, "wavefront");
      asm volatile("global_load_dwordx4 %0, %1, off sc1" : "=v"(tval)
                   : "v"(tT + (size_t)(d0 + 8 * w + (lane >> 3)) * S_ + row0 + (lane & 7) * 8) : "memory");
    }
    SB;
#pragma unroll
    for (int n = 0; n < 12; ++n) {
      if (kt < K2_NT) read_piece(fs[kt & 1], kt, n);
      if (kt > 0 && n < 8) mma_one(fs[(kt - 1) & 1], n);
      if (kt == 1 && (n % 3) == 0) dma_piece(3, n / 3);
      if (kt == 1 && n == 11) {
#pragma unroll
        for (int e = 0; e < 4; ++e) dma_piece(4, e);
      }
      if (kt > 1 && kt + 3 < K2_NT && (n % 3) == 2) dma_piece(kt + 3, n / 3);
      if (kt == 0 && n == 11) finish_t();
      if (kt == 7 && n == 0) asm volatile("global_load_dword %0, %1, off sc1" : "=v"(cval) : "v"(cptr) : "memory");
      if (kt == 8 && n == 5) glds16(x + (size_t)(row0 + 8 * w + (lane >> 3)) * D_ + d0 + (lane & 7) * 8, smem + K2_X + w * 1024);
      if (kt == 8 && n == 8) glds16(stats + row0 + 2 * (lane & 31), smem + K2_ST);
      SB;
    }
  });
  asm volatile("s_waitcnt vmcnt(0)" : "+v"(tval)::"memory");
  *(u32x4*)(smem + K2_TT + tid * 16) = tval;
  {
    f32x4* xb = (f32x4*)smem;
    const int wq = w & 3;
#pragma unroll
    for (int T = 0; T < 2; ++T) xb[((kg * 4 + wq) * 2 + T) * 64 + lane] = kg ? acc[T][0] : acc[T][1];
    raw_barrier();
    const int lrow = 32 * wm + 16 * kg + r;
    const u32x4 xv = *(const u32x4*)(smem + K2_X + lrow * 128 + (32 * wn + 16 * (g & 1) + 8 * (g >> 1)) * 2);
    const f32x2 stv = *(const f32x2*)(smem + K2_ST + lrow * 8);
    f32x4 fin[2];
#pragma unroll
    for (int T = 0; T < 2; ++T) {
      const f32x4 o = xb[(((kg ^ 1) * 4 + wq) * 2 + T) * 64 + lane];
      fin[T] = ((kg ? acc[T][1] : acc[T][0]) + o) * (stv[1] * stv[1]);
    }
    unsigned pb[2][2], pt[2][2];
#pragma unroll
    for (int T = 0; T < 2; ++T) {
      pb[T][0] = pk_bf16(fin[T][0], fin[T][1]);
      pb[T][1] = pk_bf16(fin[T][2], fin[T][3]);
      const s16x4 tv = tr_read(smem + K2_TT + (32 * wn + 16 * T + 4 * g + q) * 128 + (32 * wm + 16 * kg + 4 * p) * 2);
      const u32x2 tu = __builtin_bit_cast(u32x2, tv);
      pt[T][0] = tu[0];
      pt[T][1] = tu[1];
    }
    auto b0 = __builtin_amdgcn_permlane16_swap(pb[0][0], pb[1][0], false, false);
    auto b1 = __builtin_amdgcn_permlane16_swap(pb[0][1], pb[1][1], false, false);
    auto t0 = __builtin_amdgcn_permlane16_swap(pt[0][0], pt[1][0], false, false);
    auto t1 = __builtin_amdgcn_permlane16_swap(pt[0][1], pt[1][1], false, false);
    const u32x4 bv = {b0[0], b1[0], b0[1], b1[1]};
    const u32x4 tv = {t0[0], t1[0], t0[1], t1[1]};
    u32x4 o;
#pragma unroll
    for (int j = 0; j < 4; ++j) {
      float res[2];
#pragma unroll
      for (int hh = 0; hh < 2; ++hh) {
        const float xe = hh ? bf_hi(xv[j]) : bf_lo(xv[j]);
        const float te = hh ? bf_hi(tv[j]) : bf_lo(tv[j]);
        const float be = hh ? bf_hi(bv[j]) : bf_lo(bv[j]);
        const float xn = round_bf(round_bf(xe - stv[0]) * stv[1]);
        const float a = round_bf(xn * te);
        res[hh] = round_bf(xe + a) + be;
      }
      o[j] = pk_bf16(res[0], res[1]);
    }
    *(u32x4*)(out + (size_t)erow * D_ + edoff) = o;
  }
}

extern "C" void kernel_launch(void* const* d_in, const int* in_sizes, int n_in, void* d_out, int out_size,
                              void* d_ws, size_t ws_size, hipStream_t stream) {
  const u16* q = (const u16*)d_in[0];
  const u16* k = (const u16*)d_in[1];
  const u16* v = (const u16*)d_in[2];
  const u16* shk = (const u16*)d_in[3];
  const u16* ssmk = (const u16*)d_in[4];
  const u16* ssmd = (const u16*)d_in[5];
  const u16* wg = (const u16*)d_in[7];
  const u16* wu = (const u16*)d_in[8];
  const u16* wd = (const u16*)d_in[9];
  char* ws = (char*)d_ws;
  u16* hbuf = (u16*)(ws + WS_H);
  u16* tT = (u16*)(ws + WS_T);
  u16* ur = (u16*)(ws + WS_U);
  float2* stats = (float2*)(ws + WS_ST);
  unsigned* cnt = (unsigned*)(ws + WS_CNT);
  k1_kernel<<<256, 512, 0, stream>>>(q, k, v, shk, ssmk, wg, wu, wd, hbuf, ur, stats, cnt);
  k2_kernel<<<256, 512, 0, stream>>>(q, hbuf, wd, ur, ssmk, ssmd, tT, stats, cnt, (u16*)d_out);
}
```
